```python
import torch

def _softmax(x, axis=-1):
    return torch.softmax(x, dim=axis)
def _gelu(x):
    return torch.nn.functional.gelu(x, approximate="tanh")
def _silu(x):
    return torch.nn.functional.silu(x)
def _sigmoid(x):
    return torch.sigmoid(x)
def _layernorm(x, axis=-1, eps=1e-5):
    m = x.mean(dim=axis, keepdim=True)
    v = x.var(dim=axis, keepdim=True, unbiased=False)
    return (x - m) / torch.sqrt(v + eps)
def _scatter_add(tgt, idx, src, axis=0):
    return tgt.clone().index_add_(axis, idx, src)
def _segment_sum(src, seg, n):
    out = torch.zeros((n,) + src.shape[1:], dtype=src.dtype)
    return out.index_add_(0, seg, src)
def _pad(x, pw):
    flat = tuple(v for pair in reversed(pw) for v in pair)
    return torch.nn.functional.pad(x, flat)

kernel_name = 'sequential_preLN__softmax_full_mha__dense_activation_relu__prompt'

def setup_inputs():

    _w = (lambda a: (lambda t: t.to(torch.bfloat16) if t.is_floating_point() else t)(torch.as_tensor(a)))
    def _A_setup():
        B, S, D = 1, 2048, 640
        return {
            "x": _w((lambda *s: torch.randn(*s, dtype=torch.bfloat16) * 0.05)(B, S, D)),
            "wqkv": _w((lambda *s: torch.randn(*s, dtype=torch.bfloat16) * 0.05)(D, 3 * D)),
            "wo": _w((lambda *s: torch.randn(*s, dtype=torch.bfloat16) * 0.05)(D, D)),
        }
    _A_in = _A_setup()

    def _B_setup():
        B, S, D, Dff = 1, 2048, 640, 1728
        return {
            "x": _w((lambda *s: torch.randn(*s, dtype=torch.bfloat16) * 0.05)(B, S, D)),
            "w1": _w((lambda *s: torch.randn(*s, dtype=torch.bfloat16) * 0.05)(D, Dff)),
            "w2": _w((lambda *s: torch.randn(*s, dtype=torch.bfloat16) * 0.05)(Dff, D)),
        }
    _B_in = _B_setup()

    return {
        "A_x": _A_in["x"],
        "A_wqkv": _A_in["wqkv"],
        "A_wo": _A_in["wo"],
        "B_x": _B_in["x"],
        "B_w1": _B_in["w1"],
        "B_w2": _B_in["w2"],
    }

def reference(A_x, A_wqkv, A_wo, B_x, B_w1, B_w2):
    def _A_ref(x, wqkv, wo):
        B, S, D = x.shape
        H = 32; Dh = D // H
        qkv = (lambda x, *d: x.permute(*d))((x @ wqkv).reshape(B, S, 3, H, Dh), 2, 0, 3, 1, 4)
        q, k, v = qkv[0], qkv[1], qkv[2]
        scale = Dh ** -0.5
        scores = torch.einsum("bhqd,bhkd->bhqk", q, k) * scale
        p = _softmax(scores, axis=-1)
        o = torch.einsum("bhqk,bhkd->bhqd", p, v)
        return (lambda x, *d: x.permute(*d))(o, 0, 2, 1, 3).reshape(B, S, D) @ wo

    def _B_ref(x, w1, w2):
        h = torch.einsum("bsd,df->bsf", x, w1)
        zero = torch.zeros_like(h)
        h = torch.maximum(h, zero)
        return torch.einsum("bsf,fd->bsd", h, w2)

    x = A_x
    h = x + _A_ref(_layernorm(x, axis=-1), A_wqkv, A_wo)
    h2 = h + _B_ref(_layernorm(h, axis=-1), B_w1, B_w2)
    return h2
```

```cpp
#include <hip/hip_runtime.h>
#include <hip/hip_bf16.h>
typedef short bf16x8 __attribute__((ext_vector_type(8)));
typedef float f32x4 __attribute__((ext_vector_type(4)));
typedef float f32x16 __attribute__((ext_vector_type(16)));
typedef unsigned short u16;
constexpr int S = 2048, D = 640, H = 32, DH = 20, FF = 1728;
__device__ __forceinline__ float b2f(u16 v) { return __uint_as_float((unsigned)v << 16); }
__device__ __forceinline__ u16 f2b(float f) { unsigned u = __float_as_uint(f); u += 0x7fff + ((u >> 16) & 1); return u >> 16; }
__global__ void ln_k(const u16* x, u16* y) {
  int row = blockIdx.x * 4 + (threadIdx.x >> 6), l = threadIdx.x & 63; float v[10], s = 0;
  for (int i = 0; i < 10; ++i) { v[i] = b2f(x[row * D + l + 64 * i]); s += v[i]; }
  for (int m = 32; m; m >>= 1) s += __shfl_xor(s, m); float mean = s / D, q = 0;
  for (int i = 0; i < 10; ++i) { v[i] -= mean; q += v[i] * v[i]; }
  for (int m = 32; m; m >>= 1) q += __shfl_xor(q, m); float r = rsqrtf(q / D + 1e-5f);
  for (int i = 0; i < 10; ++i) y[row * D + l + 64 * i] = f2b(v[i] * r);
}
template<int EPI> __global__ void __launch_bounds__(256) gemm_k(const u16* A, const u16* B, const u16* R, u16* C, int N, int K) {
  __shared__ u16 As[64][40], Bs[64][40];
  int t = threadIdx.x, w = t >> 6, l = t & 63, m0 = blockIdx.y * 64, n0 = blockIdx.x * 64;
  f32x16 acc = {};
  for (int k0 = 0; k0 < K; k0 += 32) {
    *(uint4*)&As[t >> 2][(t & 3) * 8] = *(const uint4*)&A[(size_t)(m0 + (t >> 2)) * K + k0 + (t & 3) * 8];
    { uint4 b = *(const uint4*)&B[(size_t)(k0 + (t >> 3)) * N + n0 + (t & 7) * 8]; u16* p = (u16*)&b; for (int i = 0; i < 8; ++i) Bs[(t & 7) * 8 + i][t >> 3] = p[i]; }
    __syncthreads();
    for (int kk = 0; kk < 32; kk += 16) {
      bf16x8 a = *(bf16x8*)&As[(w >> 1) * 32 + (l & 31)][kk + (l >> 5) * 8], b = *(bf16x8*)&Bs[(w & 1) * 32 + (l & 31)][kk + (l >> 5) * 8];
      acc = __builtin_amdgcn_mfma_f32_32x32x16_bf16(a, b, acc, 0, 0, 0);
    }
    __syncthreads();
  }
  for (int r = 0; r < 16; ++r) { int m = m0 + (w >> 1) * 32 + (r & 3) + 8 * (r >> 2) + 4 * (l >> 5), n = n0 + (w & 1) * 32 + (l & 31);
    float v = acc[r]; if (EPI == 1) v = b2f(f2b(v)) + b2f(R[(size_t)m * N + n]); if (EPI == 2) v = fmaxf(v, 0.f); C[(size_t)m * N + n] = f2b(v); }
}
__global__ void __launch_bounds__(256) attn_k(const u16* qkv, u16* o) {
  __shared__ u16 Ks[64][40], Vt[32][72];
  int t = threadIdx.x, w = t >> 6, l = t & 63, h = l >> 5, hd = blockIdx.y, q0 = blockIdx.x * 128 + w * 32;
  bf16x8 q1, q2; const float c = 0.22360679775f * 1.44269504089f;
  for (int j = 0; j < 8; ++j) { q1[j] = qkv[(size_t)(q0 + (l & 31)) * 1920 + hd * 20 + h * 8 + j]; int d = 16 + h * 8 + j; q2[j] = d < 20 ? qkv[(size_t)(q0 + (l & 31)) * 1920 + hd * 20 + d] : 0; }
  for (int i = t; i < 64 * 40; i += 256) Ks[i / 40][i % 40] = 0;
  for (int i = t; i < 32 * 72; i += 256) Vt[i / 72][i % 72] = 0;
  f32x16 O = {}; float m = -1e30f, lsum = 0;
  for (int k0 = 0; k0 < S; k0 += 64) {
    __syncthreads();
    for (int i = t; i < 64 * 10; i += 256) { int r = i / 10, p = i % 10; *(unsigned*)&Ks[r][2 * p] = *(const unsigned*)&qkv[(size_t)(k0 + r) * 1920 + 640 + hd * 20 + 2 * p];
      unsigned v = *(const unsigned*)&qkv[(size_t)(k0 + r) * 1920 + 1280 + hd * 20 + 2 * p]; Vt[2 * p][r] = v & 0xffff; Vt[2 * p + 1][r] = v >> 16; }
    __syncthreads();
    for (int kt = 0; kt < 2; ++kt) {
      bf16x8 a1 = *(bf16x8*)&Ks[kt * 32 + (l & 31)][h * 8], a2 = *(bf16x8*)&Ks[kt * 32 + (l & 31)][16 + h * 8];
      f32x16 s = {}; s = __builtin_amdgcn_mfma_f32_32x32x16_bf16(a1, q1, s, 0, 0, 0); s = __builtin_amdgcn_mfma_f32_32x32x16_bf16(a2, q2, s, 0, 0, 0);
      float mx = -1e30f; for (int r = 0; r < 16; ++r) { s[r] *= c; mx = fmaxf(mx, s[r]); } mx = fmaxf(mx, __shfl_xor(mx, 32));
      float mn = fmaxf(m, mx), al = exp2f(m - mn); m = mn; lsum *= al; for (int r = 0; r < 16; ++r) O[r] *= al;
      bf16x8 p[2]; for (int r = 0; r < 16; ++r) { u16 e = f2b(exp2f(s[r] - mn)); lsum += b2f(e); p[r >> 3][r & 7] = e; }
      for (int ss = 0; ss < 2; ++ss) { bf16x8 v; for (int j = 0; j < 8; ++j) v[j] = Vt[l & 31][kt * 32 + 16 * ss + 8 * (j >> 2) + 4 * h + (j & 3)];
        O = __builtin_amdgcn_mfma_f32_32x32x16_bf16(v, p[ss], O, 0, 0, 0); }
    }
  }
  lsum += __shfl_xor(lsum, 32);
  for (int r = 0; r < 12; ++r) { int d = (r & 3) + 8 * (r >> 2) + 4 * h; if (d < 20) o[(size_t)(q0 + (l & 31)) * D + hd * 20 + d] = f2b(O[r] / lsum); }
}
__global__ void tiny(unsigned* p) { p[threadIdx.x] = 0; }
__global__ void __launch_bounds__(256) wide(unsigned* p) { if (threadIdx.x == 0) p[blockIdx.x * 32] = 0; }
extern "C" void kernel_launch(void* const* d_in, const int* in_sizes, int n_in, void* d_out, int out_size, void* d_ws, size_t ws_size, hipStream_t s) {
  const u16 *x = (const u16*)d_in[0], *wqkv = (const u16*)d_in[1], *wo = (const u16*)d_in[2], *w1 = (const u16*)d_in[4], *w2 = (const u16*)d_in[5];
  char* ws = (char*)d_ws; u16 *xn = (u16*)(ws + (1 << 20)), *qkv = (u16*)(ws + (4 << 20)), *o = (u16*)(ws + (12 << 20)), *hh = (u16*)(ws + (16 << 20)), *hn = (u16*)(ws + (20 << 20)), *f = (u16*)(ws + (24 << 20));

  ln_k<<<512, 256, 0, s>>>(x, xn);
  gemm_k<0><<<dim3(30, 32), 256, 0, s>>>(xn, wqkv, nullptr, qkv, 1920, 640);
  attn_k<<<dim3(16, 32), 256, 0, s>>>(qkv, o);

  gemm_k<1><<<dim3(10, 32), 256, 0, s>>>(o, wo, x, hh, 640, 640);
  ln_k<<<512, 256, 0, s>>>(hh, hn);
  gemm_k<2><<<dim3(27, 32), 256, 0, s>>>(hn, w1, nullptr, f, 1728, 640);
  gemm_k<1><<<dim3(10, 32), 256, 0, s>>>(f, w2, hh, (u16*)d_out, 640, 1728);
}
```

```cpp
#include <hip/hip_runtime.h>
typedef unsigned u32x4 __attribute__((ext_vector_type(4)));
#define LDS3 __attribute__((address_space(3)))
__device__ __forceinline__ float bf_lo(unsigned u) { return __uint_as_float(u << 16); }
__device__ __forceinline__ float bf_hi(unsigned u) { return __uint_as_float(u & 0xffff0000u); }
__device__ __forceinline__ unsigned bf_rn(float f) { unsigned u = __float_as_uint(f); return (u + 0x7fffu + ((u >> 16) & 1u)) >> 16; }
__device__ __forceinline__ void side_jobs(int ri, char* ws, const char* wo, char* lds) {
  const int t = threadIdx.x, l = t & 63;
  if (ri == 15) for (int o = t * 16; o < 16384 + 5120; o += 8192) *(u32x4*)(ws + o) = (u32x4){0u, 0u, 0u, 0u};
  LDS3 float* rsum = (LDS3 float*)lds;
  for (int u = ri; u < 20; u += 16) {
    const int r = t >> 4, e = r >> 4, kg = (r >> 2) & 3, c = 4 * u + kg, hp = c / 5, ee = c - 5 * hp;
    const int row = (ee < 4 ? (2 * hp + (ee >> 1)) * 20 + 4 * (ee & 1) + 8 * e : (2 * hp + e) * 20 + 16) + (r & 3);
    float s = 0.f;
#pragma unroll
    for (int i = 0; i < 5; ++i) { const u32x4 w = *(const u32x4*)(wo + row * 1280 + ((t & 15) + 16 * i) * 16);
      s += (bf_lo(w.x) + bf_hi(w.x)) + (bf_lo(w.y) + bf_hi(w.y)) + (bf_lo(w.z) + bf_hi(w.z)) + (bf_lo(w.w) + bf_hi(w.w)); }
#pragma unroll
    for (int m = 8; m; m >>= 1) s += __shfl_xor(s, m);
    __syncthreads();
    if ((t & 15) == 0) rsum[r] = s;
    __syncthreads();
    if (t < 64) { unsigned h[8]; const int g = l >> 4, cc = l & 3;
#pragma unroll
      for (int j = 0; j < 8; ++j) { const float x = rsum[16 * (j >> 2) + 4 * g + (j & 3)];
        const unsigned b0 = bf_rn(x); const float r1 = x - bf_lo(b0); const unsigned b1 = bf_rn(r1), b2 = bf_rn(r1 - bf_lo(b1));
        h[j] = cc == 0 ? b0 : cc == 1 ? b1 : cc == 2 ? b2 : 0u; }
      *(u32x4*)(ws + 32768 + u * 1024 + l * 16) = (u32x4){h[0] | h[1] << 16, h[2] | h[3] << 16, h[4] | h[5] << 16, h[6] | h[7] << 16}; }
  }
}
typedef unsigned u32x2 __attribute__((ext_vector_type(2)));
typedef short s16x4 __attribute__((ext_vector_type(4)));
__device__ __forceinline__ void retile_w2(int ri, char* ws, const char* wo, const char* w2, char* lds) {
  const int t = threadIdx.x, wv = __builtin_amdgcn_readfirstlane(t >> 6), l = t & 63;
  constexpr int RSTR = 1312;
  constexpr int NU = 48, NK = (NU + 15) / 16;

  u32x4 v[NK][5];
#pragma unroll
  for (int k = 0; k < NK; ++k) { const char* s = w2 + (32 * min(ri + 16 * k, NU - 1) + 4 * wv) * 1280;
#pragma unroll
    for (int i = 0; i < 5; ++i) v[k][i] = *(const u32x4*)(s + i * 1024 + l * 16); }
  const int lrow0 = 16 * (wv & 1) + 4 * (wv >> 1);

#pragma unroll
  for (int k = 0; k < NK; ++k) { const int u = ri + 16 * k; if (u >= NU) break;
    LDS3 char* L = (LDS3 char*)lds + 1024 + (k & 1) * 49152;
#pragma unroll
    for (int i = 0; i < 5; ++i) { const int idx = 64 * i + l, q = idx / 80, c = idx - 80 * q; *(LDS3 u32x4*)(L + (lrow0 + q) * RSTR + c * 16) = v[k][i]; }
    __syncthreads();
    char* dst = ws + 9273344 + (wv * 54 + u) * 5120 + l * 16;
#pragma unroll
    for (int sl = 0; sl < 5; ++sl) {
      LDS3 char* a = L + (l >> 2) * RSTR + (5 * wv + sl) * 32 + (l & 3) * 8;
      const s16x4 lo = __builtin_amdgcn_ds_read_tr16_b64_v4i16((LDS3 s16x4*)a), hi = __builtin_amdgcn_ds_read_tr16_b64_v4i16((LDS3 s16x4*)(a + 16 * RSTR));
      const u32x2 a0 = __builtin_bit_cast(u32x2, lo), a1 = __builtin_bit_cast(u32x2, hi);
      const u32x4 val = {a0.x, a0.y, a1.x, a1.y}; char* q_ = dst + sl * 1024;
      asm volatile("global_store_dwordx4 %0, %1, off sc1\n s_nop 1" :: "v"(q_), "v"(val) : "memory"); }
  }
  side_jobs(ri, ws, wo, lds);
}

extern "C" __global__ void __launch_bounds__(512, 2) k1_ln_qkv(char* ws, const void* x, const void* wqkv, const void* wo, const void* w2, const void* w1) {
  __shared__ __attribute__((aligned(16))) char lds[163840];
  if ((blockIdx.x & 1) && (blockIdx.x >> 5) == 7) {  retile_w2(((blockIdx.x & 7) >> 1) * 4 + ((blockIdx.x >> 3) & 3), ws, (const char*)wo, (const char*)w2, lds);  return; }
  asm volatile(
R"ASM(k1_code_begin:
s_load_dwordx8 s[20:27], %0, 0
s_load_dwordx4 s[56:59], %0, 32
s_mov_b32 s28, %1
v_mov_b32 v1, %2
v_lshrrev_b32 v2, 6, v1
v_and_b32 v0, 63, v1
v_readfirstlane_b32 s29, v2
s_getpc_b64 s[38:39]
k1_pc_ref2_m:
v_lshlrev_b32 v3, 7, v1
v_min_u32 v3, k1_code_end-k1_pc_ref2_m-4, v3
s_cmp_lt_u32 s29, 4
s_cbranch_scc0 Lpf_1_%=
global_load_dword v7, v3, s[38:39]
Lpf_1_%=:
s_and_b32 s35, s28, 7
s_lshr_b32 s47, s28, 3
s_lshr_b32 s36, s35, 1
s_lshl_b32 s36, s36, 2
s_and_b32 s37, s47, 3
s_add_u32 s31, s36, s37
s_and_b32 s35, s35, 1
s_lshl_b32 s35, s35, 3
s_lshr_b32 s36, s47, 2
s_add_u32 s30, s35, s36
s_cmp_lt_u32 s29, 4
s_cbranch_scc1 Lcomp_2_%=
; ==== waves 4-7: stream the x and W tiles into a five-slot LDS ring (LDS-DMA), then re-tile one unit of the later weights
s_and_b32 s46, s29, 3
v_lshrrev_b32 v2, 3, v0
v_and_b32 v3, 7, v0
v_lshrrev_b32 v4, 4, v0
v_xor_b32 v3, v3, v4
v_mul_u32_u24 v2, 0x500, v2
v_lshl_add_u32 v8, v3, 4, v2
v_xor_b32 v3, 4, v3
v_lshl_add_u32 v9, v3, 4, v2
v_add_u32 v9, 0x2800, v9
v_add_u32 v10, 0x5000, v8
v_add_u32 v11, 0x5000, v9
s_lshl_b32 s35, s31, 2
s_add_u32 s35, s35, s46
s_mul_i32 s37, s35, 0xa000
v_and_b32 v3, 15, v0
v_lshlrev_b32 v5, 2, v4
v_xor_b32 v3, v3, v5
v_mul_u32_u24 v4, 0xf00, v4
v_lshl_add_u32 v12, v3, 4, v4
v_add_u32 v13, 0x3c00, v12
v_add_u32 v14, 0x7800, v12
v_add_u32 v15, 0xb400, v12
s_mul_i32 s35, s46, 0xf000
s_lshl_b32 s36, s30, 8
s_add_u32 s35, s35, s36
s_lshl_b32 s46, s46, 12
s_mov_b32 s18, s37
s_mov_b32 s19, s35
s_and_b32 s35, s31, 3
s_mul_i32 s35, s35, 10
s_lshr_b32 s35, s35, 1
s_and_b32 s36, s30, 7
s_mul_i32 s36, s36, 5
s_add_u32 s12, s35, s36
s_lshr_b32 s35, s31, 2
s_mul_i32 s35, s35, 0
s_add_u32 s12, s12, s35
s_lshr_b32 s35, s30, 3
s_mul_i32 s35, s35, 0
s_add_u32 s12, s12, s35
s_mul_i32 s35, s12, 0x199a
s_lshr_b32 s35, s35, 16
s_mul_i32 s35, s35, 10
s_sub_u32 s12, s12, s35
s_lshl_b32 s35, s12, 7
s_add_u32 s18, s18, s35
s_mul_i32 s35, s12, 0x3c000
s_add_u32 s19, s19, s35
s_waitcnt lgkmcnt(0)
s_add_u32 s38, s22, s18
s_addc_u32 s39, s23, 0
s_add_u32 s40, s24, s19
s_addc_u32 s41, s25, 0
s_add_u32 m0, s46, 0
s_nop 0
global_load_lds_dwordx4 v8, s[38:39]
s_add_u32 m0, s46, 0x4000
s_nop 0
global_load_lds_dwordx4 v12, s[40:41]
s_add_u32 m0, s46, 0x400
s_nop 0
global_load_lds_dwordx4 v9, s[38:39]
s_add_u32 m0, s46, 0x4400
s_nop 0
global_load_lds_dwordx4 v13, s[40:41]
s_add_u32 m0, s46, 0x800
s_nop 0
global_load_lds_dwordx4 v10, s[38:39]
s_add_u32 m0, s46, 0x4800
s_nop 0
global_load_lds_dwordx4 v14, s[40:41]
s_add_u32 m0, s46, 0xc00
s_nop 0
global_load_lds_dwordx4 v11, s[38:39]
s_add_u32 m0, s46, 0x4c00
s_nop 0
global_load_lds_dwordx4 v15, s[40:41]
s_add_u32 s12, s12, 1
s_cmp_eq_u32 s12, 10
s_cselect_b32 s15, -1, 0
s_cselect_b32 s35, 10, 0
s_sub_u32 s12, s12, s35
s_mul_i32 s14, s35, 0xffffff80
s_add_u32 s14, s14, 0x80
s_mul_i32 s16, s35, 0xfffc4000
s_add_u32 s16, s16, 0x3c000
s_add_u32 s38, s38, s14
s_addc_u32 s39, s39, s15
s_add_u32 s40, s40, s16
s_addc_u32 s41, s41, s15
s_add_u32 m0, s46, 0x8000
s_nop 0
global_load_lds_dwordx4 v8, s[38:39]
s_add_u32 m0, s46, 0xc000
s_nop 0
global_load_lds_dwordx4 v12, s[40:41]
s_add_u32 m0, s46, 0x8400
s_nop 0
global_load_lds_dwordx4 v9, s[38:39]
s_add_u32 m0, s46, 0xc400
s_nop 0
global_load_lds_dwordx4 v13, s[40:41]
s_add_u32 m0, s46, 0x8800
s_nop 0
global_load_lds_dwordx4 v10, s[38:39]
s_add_u32 m0, s46, 0xc800
s_nop 0
global_load_lds_dwordx4 v14, s[40:41]
s_add_u32 m0, s46, 0x8c00
s_nop 0
global_load_lds_dwordx4 v11, s[38:39]
s_add_u32 m0, s46, 0xcc00
s_nop 0
global_load_lds_dwordx4 v15, s[40:41]
s_waitcnt vmcnt(8)
s_barrier
s_add_u32 s12, s12, 1
s_cmp_eq_u32 s12, 10
s_cselect_b32 s15, -1, 0
s_cselect_b32 s35, 10, 0
s_sub_u32 s12, s12, s35
s_mul_i32 s14, s35, 0xffffff80
s_add_u32 s14, s14, 0x80
s_mul_i32 s16, s35, 0xfffc4000
s_add_u32 s16, s16, 0x3c000
s_add_u32 s38, s38, s14
s_addc_u32 s39, s39, s15
s_add_u32 s40, s40, s16
s_addc_u32 s41, s41, s15
s_add_u32 m0, s46, 0x10000
s_nop 0
global_load_lds_dwordx4 v8, s[38:39]
s_add_u32 m0, s46, 0x14000
s_nop 0
global_load_lds_dwordx4 v12, s[40:41]
s_add_u32 m0, s46, 0x10400
s_nop 0
global_load_lds_dwordx4 v9, s[38:39]
s_add_u32 m0, s46, 0x14400
s_nop 0
global_load_lds_dwordx4 v13, s[40:41]
s_add_u32 m0, s46, 0x10800
s_nop 0
global_load_lds_dwordx4 v10, s[38:39]
s_add_u32 m0, s46, 0x14800
s_nop 0
global_load_lds_dwordx4 v14, s[40:41]
s_add_u32 m0, s46, 0x10c00
s_nop 0
global_load_lds_dwordx4 v11, s[38:39]
s_add_u32 m0, s46, 0x14c00
s_nop 0
global_load_lds_dwordx4 v15, s[40:41]
s_add_u32 s12, s12, 1
s_cmp_eq_u32 s12, 10
s_cselect_b32 s15, -1, 0
s_cselect_b32 s35, 10, 0
s_sub_u32 s12, s12, s35
s_mul_i32 s14, s35, 0xffffff80
s_add_u32 s14, s14, 0x80
s_mul_i32 s16, s35, 0xfffc4000
s_add_u32 s16, s16, 0x3c000
s_add_u32 s38, s38, s14
s_addc_u32 s39, s39, s15
s_add_u32 s40, s40, s16
s_addc_u32 s41, s41, s15
s_add_u32 m0, s46, 0x18000
s_nop 0
global_load_lds_dwordx4 v8, s[38:39]
s_add_u32 m0, s46, 0x1c000
)ASM"
R"ASM(s_nop 0
global_load_lds_dwordx4 v12, s[40:41]
s_add_u32 m0, s46, 0x18400
s_nop 0
global_load_lds_dwordx4 v9, s[38:39]
s_add_u32 m0, s46, 0x1c400
s_nop 0
global_load_lds_dwordx4 v13, s[40:41]
s_add_u32 m0, s46, 0x18800
s_nop 0
global_load_lds_dwordx4 v10, s[38:39]
s_add_u32 m0, s46, 0x1c800
s_nop 0
global_load_lds_dwordx4 v14, s[40:41]
s_add_u32 m0, s46, 0x18c00
s_nop 0
global_load_lds_dwordx4 v11, s[38:39]
s_add_u32 m0, s46, 0x1cc00
s_nop 0
global_load_lds_dwordx4 v15, s[40:41]
s_waitcnt vmcnt(16)
s_barrier
s_add_u32 s12, s12, 1
s_cmp_eq_u32 s12, 10
s_cselect_b32 s15, -1, 0
s_cselect_b32 s35, 10, 0
s_sub_u32 s12, s12, s35
s_mul_i32 s14, s35, 0xffffff80
s_add_u32 s14, s14, 0x80
s_mul_i32 s16, s35, 0xfffc4000
s_add_u32 s16, s16, 0x3c000
s_add_u32 s38, s38, s14
s_addc_u32 s39, s39, s15
s_add_u32 s40, s40, s16
s_addc_u32 s41, s41, s15
s_add_u32 m0, s46, 0x20000
s_nop 0
global_load_lds_dwordx4 v8, s[38:39]
s_add_u32 m0, s46, 0x24000
s_nop 0
global_load_lds_dwordx4 v12, s[40:41]
s_add_u32 m0, s46, 0x20400
s_nop 0
global_load_lds_dwordx4 v9, s[38:39]
s_add_u32 m0, s46, 0x24400
s_nop 0
global_load_lds_dwordx4 v13, s[40:41]
s_add_u32 m0, s46, 0x20800
s_nop 0
global_load_lds_dwordx4 v10, s[38:39]
s_add_u32 m0, s46, 0x24800
s_nop 0
global_load_lds_dwordx4 v14, s[40:41]
s_add_u32 m0, s46, 0x20c00
s_nop 0
global_load_lds_dwordx4 v11, s[38:39]
s_add_u32 m0, s46, 0x24c00
s_nop 0
global_load_lds_dwordx4 v15, s[40:41]
s_waitcnt vmcnt(16)
s_barrier
s_add_u32 s12, s12, 1
s_cmp_eq_u32 s12, 10
s_cselect_b32 s15, -1, 0
s_cselect_b32 s35, 10, 0
s_sub_u32 s12, s12, s35
s_mul_i32 s14, s35, 0xffffff80
s_add_u32 s14, s14, 0x80
s_mul_i32 s16, s35, 0xfffc4000
s_add_u32 s16, s16, 0x3c000
s_add_u32 s38, s38, s14
s_addc_u32 s39, s39, s15
s_add_u32 s40, s40, s16
s_addc_u32 s41, s41, s15
s_add_u32 m0, s46, 0
s_nop 0
global_load_lds_dwordx4 v8, s[38:39]
s_add_u32 m0, s46, 0x4000
s_nop 0
global_load_lds_dwordx4 v12, s[40:41]
s_add_u32 m0, s46, 0x400
s_nop 0
global_load_lds_dwordx4 v9, s[38:39]
s_add_u32 m0, s46, 0x4400
s_nop 0
global_load_lds_dwordx4 v13, s[40:41]
s_add_u32 m0, s46, 0x800
s_nop 0
global_load_lds_dwordx4 v10, s[38:39]
s_add_u32 m0, s46, 0x4800
s_nop 0
global_load_lds_dwordx4 v14, s[40:41]
s_add_u32 m0, s46, 0xc00
s_nop 0
global_load_lds_dwordx4 v11, s[38:39]
s_add_u32 m0, s46, 0x4c00
s_nop 0
global_load_lds_dwordx4 v15, s[40:41]
s_waitcnt vmcnt(16)
s_barrier
s_add_u32 s12, s12, 1
s_cmp_eq_u32 s12, 10
s_cselect_b32 s15, -1, 0
s_cselect_b32 s35, 10, 0
s_sub_u32 s12, s12, s35
s_mul_i32 s14, s35, 0xffffff80
s_add_u32 s14, s14, 0x80
s_mul_i32 s16, s35, 0xfffc4000
s_add_u32 s16, s16, 0x3c000
s_add_u32 s38, s38, s14
s_addc_u32 s39, s39, s15
s_add_u32 s40, s40, s16
s_addc_u32 s41, s41, s15
s_add_u32 m0, s46, 0x8000
s_nop 0
global_load_lds_dwordx4 v8, s[38:39]
s_add_u32 m0, s46, 0xc000
s_nop 0
global_load_lds_dwordx4 v12, s[40:41]
s_add_u32 m0, s46, 0x8400
s_nop 0
global_load_lds_dwordx4 v9, s[38:39]
s_add_u32 m0, s46, 0xc400
s_nop 0
global_load_lds_dwordx4 v13, s[40:41]
s_add_u32 m0, s46, 0x8800
s_nop 0
global_load_lds_dwordx4 v10, s[38:39]
s_add_u32 m0, s46, 0xc800
s_nop 0
global_load_lds_dwordx4 v14, s[40:41]
s_add_u32 m0, s46, 0x8c00
s_nop 0
global_load_lds_dwordx4 v11, s[38:39]
s_add_u32 m0, s46, 0xcc00
s_nop 0
global_load_lds_dwordx4 v15, s[40:41]
s_waitcnt vmcnt(16)
s_barrier
s_add_u32 s12, s12, 1
s_cmp_eq_u32 s12, 10
s_cselect_b32 s15, -1, 0
s_cselect_b32 s35, 10, 0
s_sub_u32 s12, s12, s35
s_mul_i32 s14, s35, 0xffffff80
s_add_u32 s14, s14, 0x80
s_mul_i32 s16, s35, 0xfffc4000
s_add_u32 s16, s16, 0x3c000
s_add_u32 s38, s38, s14
s_addc_u32 s39, s39, s15
s_add_u32 s40, s40, s16
s_addc_u32 s41, s41, s15
s_add_u32 m0, s46, 0x10000
s_nop 0
global_load_lds_dwordx4 v8, s[38:39]
s_add_u32 m0, s46, 0x14000
s_nop 0
global_load_lds_dwordx4 v12, s[40:41]
s_add_u32 m0, s46, 0x10400
s_nop 0
global_load_lds_dwordx4 v9, s[38:39]
s_add_u32 m0, s46, 0x14400
s_nop 0
global_load_lds_dwordx4 v13, s[40:41]
s_add_u32 m0, s46, 0x10800
s_nop 0
global_load_lds_dwordx4 v10, s[38:39]
s_add_u32 m0, s46, 0x14800
s_nop 0
global_load_lds_dwordx4 v14, s[40:41]
s_add_u32 m0, s46, 0x10c00
s_nop 0
global_load_lds_dwordx4 v11, s[38:39]
s_add_u32 m0, s46, 0x14c00
s_nop 0
global_load_lds_dwordx4 v15, s[40:41]
s_waitcnt vmcnt(16)
s_barrier
s_add_u32 s12, s12, 1
s_cmp_eq_u32 s12, 10
s_cselect_b32 s15, -1, 0
s_cselect_b32 s35, 10, 0
s_sub_u32 s12, s12, s35
s_mul_i32 s14, s35, 0xffffff80
s_add_u32 s14, s14, 0x80
s_mul_i32 s16, s35, 0xfffc4000
s_add_u32 s16, s16, 0x3c000
s_add_u32 s38, s38, s14
s_addc_u32 s39, s39, s15
s_add_u32 s40, s40, s16
s_addc_u32 s41, s41, s15
s_add_u32 m0, s46, 0x18000
s_nop 0
global_load_lds_dwordx4 v8, s[38:39]
s_add_u32 m0, s46, 0x1c000
s_nop 0
global_load_lds_dwordx4 v12, s[40:41]
s_add_u32 m0, s46, 0x18400
s_nop 0
global_load_lds_dwordx4 v9, s[38:39]
)ASM"
R"ASM(s_add_u32 m0, s46, 0x1c400
s_nop 0
global_load_lds_dwordx4 v13, s[40:41]
s_add_u32 m0, s46, 0x18800
s_nop 0
global_load_lds_dwordx4 v10, s[38:39]
s_add_u32 m0, s46, 0x1c800
s_nop 0
global_load_lds_dwordx4 v14, s[40:41]
s_add_u32 m0, s46, 0x18c00
s_nop 0
global_load_lds_dwordx4 v11, s[38:39]
s_add_u32 m0, s46, 0x1cc00
s_nop 0
global_load_lds_dwordx4 v15, s[40:41]
s_mul_i32 s4, s31, 15
s_add_u32 s4, s4, s30
s_and_b32 s36, s29, 3
s_mul_i32 s36, s36, 0xf0
s_add_u32 s4, s4, s36
s_mov_b32 s11, s4
s_min_u32 s4, s4, 0x20f
s_cmp_lt_u32 s4, 0x140
s_cbranch_scc0 Lra_4_%=
s_mul_i32 s6, s4, 0xccd
s_lshr_b32 s6, s6, 16
s_mul_i32 s35, s6, 20
s_sub_u32 s5, s4, s35
s_mul_i32 s35, s6, 0x1af
s_lshr_b32 s35, s35, 6
s_lshl_b32 s35, s35, 5
s_mul_i32 s36, s5, 0x1b000
s_add_u32 s35, s35, s36
s_add_u32 s60, s58, s35
s_addc_u32 s61, s59, 0
s_mov_b32 s35, s4
s_mul_i32 s35, s35, 0x1c00
s_add_u32 s35, s35, 0xaf4c00
s_add_u32 s62, s20, s35
s_addc_u32 s63, s21, 0
s_mov_b32 s55, 14
s_mov_b32 s7, 0x124a
s_mov_b32 s8, 0xd80
s_mov_b32 s5, -1
s_branch Lrc_6_%=
Lra_4_%=:
s_mov_b32 s55, 10
s_mov_b32 s7, 0x199a
s_mov_b32 s8, 0x500
s_cmp_lt_u32 s4, 0x1e0
s_cbranch_scc0 Lrb_5_%=
s_sub_u32 s4, s4, 0x140
s_mul_i32 s6, s4, 0xccd
s_lshr_b32 s6, s6, 16
s_mul_i32 s35, s6, 20
s_sub_u32 s5, s4, s35
s_mul_i32 s35, s6, 0xa0
s_add_u32 s60, s26, s35
s_addc_u32 s61, s27, 0
s_mul_i32 s35, s4, 0x1400
s_add_u32 s35, s35, 0x810c00
s_add_u32 s62, s20, s35
s_addc_u32 s63, s21, 0
s_branch Lrc_6_%=
Lrb_5_%=:
s_sub_u32 s4, s4, 0x1e0
s_mul_i32 s6, s4, 0x2aab
s_lshr_b32 s6, s6, 16
s_mul_i32 s35, s6, 6
s_sub_u32 s5, s4, s35
s_add_u32 s5, s5, 48
s_mul_i32 s35, s6, 0xa0
s_mul_i32 s36, s5, 0xa000
s_add_u32 s35, s35, s36
s_add_u32 s60, s56, s35
s_addc_u32 s61, s57, 0
s_mul_i32 s35, s6, 54
s_add_u32 s35, s35, s5
s_mul_i32 s35, s35, 0x1400
s_add_u32 s35, s35, 0x8d8c00
s_add_u32 s62, s20, s35
s_addc_u32 s63, s21, 0
s_mov_b32 s5, -1
Lrc_6_%=:
s_lshl_b32 s54, s55, 4
s_lshl_b32 s37, s5, 2
v_add_u32 v2, 0, v0
v_mul_u32_u24 v3, s7, v2
v_lshrrev_b32 v3, 16, v3
v_mul_u32_u24 v4, s55, v3
v_sub_u32 v2, v2, v4
v_min_u32 v3, 31, v3
s_cmp_lt_i32 s5, 0
s_cbranch_scc1 Lrw_7_%=
v_bfe_u32 v4, v3, 2, 2
v_add_u32 v4, s37, v4
v_mul_u32_u24 v5, 0xcd, v4
v_lshrrev_b32 v5, 10, v5
v_mul_u32_u24 v6, 5, v5
v_sub_u32 v4, v4, v6
v_mul_u32_u24 v5, 40, v5
v_and_b32 v6, 3, v3
v_add_u32 v5, v5, v6
v_lshrrev_b32 v3, 4, v3
v_cmp_gt_u32 vcc, 4, v4
v_lshrrev_b32 v6, 1, v4
v_mul_u32_u24 v6, 20, v6
v_and_b32 v4, 1, v4
v_lshl_add_u32 v6, v4, 2, v6
v_lshl_add_u32 v6, v3, 3, v6
v_mad_u32_u24 v3, v3, 20, 16
v_cndmask_b32 v3, v3, v6, vcc
v_add_u32 v3, v3, v5
s_nop 4
s_branch Lrx_8_%=
Lrw_7_%=:
v_bfe_u32 v4, v3, 2, 2
v_and_b32 v5, 3, v3
v_lshl_add_u32 v5, v4, 3, v5
v_lshrrev_b32 v3, 4, v3
v_lshl_add_u32 v3, v3, 2, v5
s_nop 4
Lrx_8_%=:
v_mul_u32_u24 v3, s8, v3
v_lshl_add_u32 v26, v2, 4, v3
v_add_u32 v2, 64, v0
v_mul_u32_u24 v3, s7, v2
v_lshrrev_b32 v3, 16, v3
v_mul_u32_u24 v4, s55, v3
v_sub_u32 v2, v2, v4
v_min_u32 v3, 31, v3
s_cmp_lt_i32 s5, 0
s_cbranch_scc1 Lrw_9_%=
v_bfe_u32 v4, v3, 2, 2
v_add_u32 v4, s37, v4
v_mul_u32_u24 v5, 0xcd, v4
v_lshrrev_b32 v5, 10, v5
v_mul_u32_u24 v6, 5, v5
v_sub_u32 v4, v4, v6
v_mul_u32_u24 v5, 40, v5
v_and_b32 v6, 3, v3
v_add_u32 v5, v5, v6
v_lshrrev_b32 v3, 4, v3
v_cmp_gt_u32 vcc, 4, v4
v_lshrrev_b32 v6, 1, v4
v_mul_u32_u24 v6, 20, v6
v_and_b32 v4, 1, v4
v_lshl_add_u32 v6, v4, 2, v6
v_lshl_add_u32 v6, v3, 3, v6
v_mad_u32_u24 v3, v3, 20, 16
v_cndmask_b32 v3, v3, v6, vcc
v_add_u32 v3, v3, v5
s_nop 4
s_branch Lrx_10_%=
Lrw_9_%=:
v_bfe_u32 v4, v3, 2, 2
v_and_b32 v5, 3, v3
v_lshl_add_u32 v5, v4, 3, v5
v_lshrrev_b32 v3, 4, v3
v_lshl_add_u32 v3, v3, 2, v5
s_nop 4
Lrx_10_%=:
v_mul_u32_u24 v3, s8, v3
v_lshl_add_u32 v27, v2, 4, v3
v_add_u32 v2, 0x80, v0
v_mul_u32_u24 v3, s7, v2
v_lshrrev_b32 v3, 16, v3
v_mul_u32_u24 v4, s55, v3
v_sub_u32 v2, v2, v4
v_min_u32 v3, 31, v3
s_cmp_lt_i32 s5, 0
s_cbranch_scc1 Lrw_11_%=
v_bfe_u32 v4, v3, 2, 2
v_add_u32 v4, s37, v4
v_mul_u32_u24 v5, 0xcd, v4
v_lshrrev_b32 v5, 10, v5
v_mul_u32_u24 v6, 5, v5
v_sub_u32 v4, v4, v6
v_mul_u32_u24 v5, 40, v5
v_and_b32 v6, 3, v3
v_add_u32 v5, v5, v6
v_lshrrev_b32 v3, 4, v3
v_cmp_gt_u32 vcc, 4, v4
v_lshrrev_b32 v6, 1, v4
v_mul_u32_u24 v6, 20, v6
v_and_b32 v4, 1, v4
v_lshl_add_u32 v6, v4, 2, v6
v_lshl_add_u32 v6, v3, 3, v6
v_mad_u32_u24 v3, v3, 20, 16
v_cndmask_b32 v3, v3, v6, vcc
v_add_u32 v3, v3, v5
s_nop 4
s_branch Lrx_12_%=
Lrw_11_%=:
v_bfe_u32 v4, v3, 2, 2
v_and_b32 v5, 3, v3
v_lshl_add_u32 v5, v4, 3, v5
v_lshrrev_b32 v3, 4, v3
v_lshl_add_u32 v3, v3, 2, v5
s_nop 4
)ASM"
R"ASM(Lrx_12_%=:
v_mul_u32_u24 v3, s8, v3
v_lshl_add_u32 v28, v2, 4, v3
v_add_u32 v2, 0xc0, v0
v_mul_u32_u24 v3, s7, v2
v_lshrrev_b32 v3, 16, v3
v_mul_u32_u24 v4, s55, v3
v_sub_u32 v2, v2, v4
v_min_u32 v3, 31, v3
s_cmp_lt_i32 s5, 0
s_cbranch_scc1 Lrw_13_%=
v_bfe_u32 v4, v3, 2, 2
v_add_u32 v4, s37, v4
v_mul_u32_u24 v5, 0xcd, v4
v_lshrrev_b32 v5, 10, v5
v_mul_u32_u24 v6, 5, v5
v_sub_u32 v4, v4, v6
v_mul_u32_u24 v5, 40, v5
v_and_b32 v6, 3, v3
v_add_u32 v5, v5, v6
v_lshrrev_b32 v3, 4, v3
v_cmp_gt_u32 vcc, 4, v4
v_lshrrev_b32 v6, 1, v4
v_mul_u32_u24 v6, 20, v6
v_and_b32 v4, 1, v4
v_lshl_add_u32 v6, v4, 2, v6
v_lshl_add_u32 v6, v3, 3, v6
v_mad_u32_u24 v3, v3, 20, 16
v_cndmask_b32 v3, v3, v6, vcc
v_add_u32 v3, v3, v5
s_nop 4
s_branch Lrx_14_%=
Lrw_13_%=:
v_bfe_u32 v4, v3, 2, 2
v_and_b32 v5, 3, v3
v_lshl_add_u32 v5, v4, 3, v5
v_lshrrev_b32 v3, 4, v3
v_lshl_add_u32 v3, v3, 2, v5
s_nop 4
Lrx_14_%=:
v_mul_u32_u24 v3, s8, v3
v_lshl_add_u32 v29, v2, 4, v3
v_add_u32 v2, 0x100, v0
v_mul_u32_u24 v3, s7, v2
v_lshrrev_b32 v3, 16, v3
v_mul_u32_u24 v4, s55, v3
v_sub_u32 v2, v2, v4
v_min_u32 v3, 31, v3
s_cmp_lt_i32 s5, 0
s_cbranch_scc1 Lrw_15_%=
v_bfe_u32 v4, v3, 2, 2
v_add_u32 v4, s37, v4
v_mul_u32_u24 v5, 0xcd, v4
v_lshrrev_b32 v5, 10, v5
v_mul_u32_u24 v6, 5, v5
v_sub_u32 v4, v4, v6
v_mul_u32_u24 v5, 40, v5
v_and_b32 v6, 3, v3
v_add_u32 v5, v5, v6
v_lshrrev_b32 v3, 4, v3
v_cmp_gt_u32 vcc, 4, v4
v_lshrrev_b32 v6, 1, v4
v_mul_u32_u24 v6, 20, v6
v_and_b32 v4, 1, v4
v_lshl_add_u32 v6, v4, 2, v6
v_lshl_add_u32 v6, v3, 3, v6
v_mad_u32_u24 v3, v3, 20, 16
v_cndmask_b32 v3, v3, v6, vcc
v_add_u32 v3, v3, v5
s_nop 4
s_branch Lrx_16_%=
Lrw_15_%=:
v_bfe_u32 v4, v3, 2, 2
v_and_b32 v5, 3, v3
v_lshl_add_u32 v5, v4, 3, v5
v_lshrrev_b32 v3, 4, v3
v_lshl_add_u32 v3, v3, 2, v5
s_nop 4
Lrx_16_%=:
v_mul_u32_u24 v3, s8, v3
v_lshl_add_u32 v30, v2, 4, v3
v_add_u32 v2, 0x140, v0
v_mul_u32_u24 v3, s7, v2
v_lshrrev_b32 v3, 16, v3
v_mul_u32_u24 v4, s55, v3
v_sub_u32 v2, v2, v4
v_min_u32 v3, 31, v3
s_cmp_lt_i32 s5, 0
s_cbranch_scc1 Lrw_17_%=
v_bfe_u32 v4, v3, 2, 2
v_add_u32 v4, s37, v4
v_mul_u32_u24 v5, 0xcd, v4
v_lshrrev_b32 v5, 10, v5
v_mul_u32_u24 v6, 5, v5
v_sub_u32 v4, v4, v6
v_mul_u32_u24 v5, 40, v5
v_and_b32 v6, 3, v3
v_add_u32 v5, v5, v6
v_lshrrev_b32 v3, 4, v3
v_cmp_gt_u32 vcc, 4, v4
v_lshrrev_b32 v6, 1, v4
v_mul_u32_u24 v6, 20, v6
v_and_b32 v4, 1, v4
v_lshl_add_u32 v6, v4, 2, v6
v_lshl_add_u32 v6, v3, 3, v6
v_mad_u32_u24 v3, v3, 20, 16
v_cndmask_b32 v3, v3, v6, vcc
v_add_u32 v3, v3, v5
s_nop 4
s_branch Lrx_18_%=
Lrw_17_%=:
v_bfe_u32 v4, v3, 2, 2
v_and_b32 v5, 3, v3
v_lshl_add_u32 v5, v4, 3, v5
v_lshrrev_b32 v3, 4, v3
v_lshl_add_u32 v3, v3, 2, v5
s_nop 4
Lrx_18_%=:
v_mul_u32_u24 v3, s8, v3
v_lshl_add_u32 v31, v2, 4, v3
v_add_u32 v2, 0x180, v0
v_mul_u32_u24 v3, s7, v2
v_lshrrev_b32 v3, 16, v3
v_mul_u32_u24 v4, s55, v3
v_sub_u32 v2, v2, v4
v_min_u32 v3, 31, v3
s_cmp_lt_i32 s5, 0
s_cbranch_scc1 Lrw_19_%=
v_bfe_u32 v4, v3, 2, 2
v_add_u32 v4, s37, v4
v_mul_u32_u24 v5, 0xcd, v4
v_lshrrev_b32 v5, 10, v5
v_mul_u32_u24 v6, 5, v5
v_sub_u32 v4, v4, v6
v_mul_u32_u24 v5, 40, v5
v_and_b32 v6, 3, v3
v_add_u32 v5, v5, v6
v_lshrrev_b32 v3, 4, v3
v_cmp_gt_u32 vcc, 4, v4
v_lshrrev_b32 v6, 1, v4
v_mul_u32_u24 v6, 20, v6
v_and_b32 v4, 1, v4
v_lshl_add_u32 v6, v4, 2, v6
v_lshl_add_u32 v6, v3, 3, v6
v_mad_u32_u24 v3, v3, 20, 16
v_cndmask_b32 v3, v3, v6, vcc
v_add_u32 v3, v3, v5
s_nop 4
s_branch Lrx_20_%=
Lrw_19_%=:
v_bfe_u32 v4, v3, 2, 2
v_and_b32 v5, 3, v3
v_lshl_add_u32 v5, v4, 3, v5
v_lshrrev_b32 v3, 4, v3
v_lshl_add_u32 v3, v3, 2, v5
s_nop 4
Lrx_20_%=:
v_mul_u32_u24 v3, s8, v3
v_lshl_add_u32 v32, v2, 4, v3
s_waitcnt vmcnt(16)
s_barrier
s_add_u32 s12, s12, 1
s_cmp_eq_u32 s12, 10
s_cselect_b32 s15, -1, 0
s_cselect_b32 s35, 10, 0
s_sub_u32 s12, s12, s35
s_mul_i32 s14, s35, 0xffffff80
s_add_u32 s14, s14, 0x80
s_mul_i32 s16, s35, 0xfffc4000
s_add_u32 s16, s16, 0x3c000
s_add_u32 s38, s38, s14
s_addc_u32 s39, s39, s15
s_add_u32 s40, s40, s16
s_addc_u32 s41, s41, s15
s_add_u32 m0, s46, 0x20000
s_nop 0
global_load_lds_dwordx4 v8, s[38:39]
s_add_u32 m0, s46, 0x24000
s_nop 0
global_load_lds_dwordx4 v12, s[40:41]
s_add_u32 m0, s46, 0x20400
s_nop 0
global_load_lds_dwordx4 v9, s[38:39]
s_add_u32 m0, s46, 0x24400
s_nop 0
global_load_lds_dwordx4 v13, s[40:41]
s_add_u32 m0, s46, 0x20800
s_nop 0
global_load_lds_dwordx4 v10, s[38:39]
s_add_u32 m0, s46, 0x24800
s_nop 0
global_load_lds_dwordx4 v14, s[40:41]
s_add_u32 m0, s46, 0x20c00
s_nop 0
global_load_lds_dwordx4 v11, s[38:39]
s_add_u32 m0, s46, 0x24c00
s_nop 0
global_load_lds_dwordx4 v15, s[40:41]
s_cmp_ge_u32 s11, 0x210
s_cbranch_scc0 Lra_21_%=
)ASM"
R"ASM(s_waitcnt vmcnt(16)
s_barrier
s_waitcnt vmcnt(8)
s_barrier
s_waitcnt vmcnt(0)
s_barrier
s_barrier
s_branch Lrj_22_%=
Lra_21_%=:
s_and_b32 s35, s29, 3
s_lshl_b32 s35, s35, 13
s_add_u32 m0, s35, 0
s_nop 0
global_load_lds_dwordx4 v26, s[60:61]
s_add_u32 m0, s35, 0x400
s_nop 0
global_load_lds_dwordx4 v27, s[60:61]
s_add_u32 m0, s35, 0x800
s_nop 0
global_load_lds_dwordx4 v28, s[60:61]
s_add_u32 m0, s35, 0xc00
s_nop 0
global_load_lds_dwordx4 v29, s[60:61]
s_add_u32 m0, s35, 0x1000
s_nop 0
global_load_lds_dwordx4 v30, s[60:61]
s_add_u32 m0, s35, 0x1400
s_nop 0
global_load_lds_dwordx4 v31, s[60:61]
s_add_u32 m0, s35, 0x1800
s_nop 0
global_load_lds_dwordx4 v32, s[60:61]
s_waitcnt vmcnt(23)
s_barrier
s_waitcnt vmcnt(15)
s_barrier
s_waitcnt vmcnt(7)
s_barrier
s_and_b32 s35, s29, 3
s_lshl_b32 s35, s35, 13
v_lshrrev_b32 v2, 2, v0
v_mul_u32_u24 v2, s54, v2
v_and_b32 v3, 3, v0
v_lshl_add_u32 v2, v3, 3, v2
v_add_u32 v2, s35, v2
s_lshl_b32 s36, s54, 4
v_add_u32 v3, s36, v2
v_lshlrev_b32 v4, 4, v0
s_waitcnt vmcnt(0)
ds_read_b64_tr_b16 v[36:37], v2 offset:0
ds_read_b64_tr_b16 v[38:39], v3 offset:0
ds_read_b64_tr_b16 v[40:41], v2 offset:32
ds_read_b64_tr_b16 v[42:43], v3 offset:32
ds_read_b64_tr_b16 v[44:45], v2 offset:64
ds_read_b64_tr_b16 v[46:47], v3 offset:64
ds_read_b64_tr_b16 v[48:49], v2 offset:96
ds_read_b64_tr_b16 v[50:51], v3 offset:96
ds_read_b64_tr_b16 v[52:53], v2 offset:128
ds_read_b64_tr_b16 v[54:55], v3 offset:128
ds_read_b64_tr_b16 v[56:57], v2 offset:160
ds_read_b64_tr_b16 v[58:59], v3 offset:160
ds_read_b64_tr_b16 v[60:61], v2 offset:192
ds_read_b64_tr_b16 v[62:63], v3 offset:192
s_waitcnt lgkmcnt(12)
global_store_dwordx4 v4, v[36:39], s[62:63] offset:-3072 sc1
s_waitcnt lgkmcnt(10)
global_store_dwordx4 v4, v[40:43], s[62:63] offset:-2048 sc1
s_waitcnt lgkmcnt(8)
global_store_dwordx4 v4, v[44:47], s[62:63] offset:-1024 sc1
s_waitcnt lgkmcnt(6)
global_store_dwordx4 v4, v[48:51], s[62:63] offset:0 sc1
s_waitcnt lgkmcnt(4)
global_store_dwordx4 v4, v[52:55], s[62:63] offset:1024 sc1
s_cmp_eq_u32 s55, 10
s_cbranch_scc1 Lrf_23_%=
s_waitcnt lgkmcnt(2)
global_store_dwordx4 v4, v[56:59], s[62:63] offset:2048 sc1
s_waitcnt lgkmcnt(0)
global_store_dwordx4 v4, v[60:63], s[62:63] offset:3072 sc1
Lrf_23_%=:
s_waitcnt lgkmcnt(0)
s_barrier
Lrj_22_%=:
s_and_b32 s46, s29, 3
s_add_u32 s38, s20, 0x290000
s_addc_u32 s39, s21, 0
s_lshl_b32 s36, s31, 2
s_sub_u32 s37, s30, 10
s_cmp_lt_u32 s37, 4
s_cbranch_scc0 Lo2_25_%=
s_lshl_b32 s35, s37, 2
s_add_u32 s35, s35, s46
s_lshl_b32 s35, s35, 6
s_add_u32 s35, s35, s36
s_mul_i32 s35, s35, 0x1600
s_add_u32 s38, s38, s35
s_addc_u32 s39, s39, 0
v_lshrrev_b32 v3, 5, v0
v_mul_u32_u24 v3, 0x1600, v3
v_bfe_u32 v4, v0, 2, 3
v_mul_u32_u24 v4, 0x180, v4
v_add_u32 v3, v3, v4
v_and_b32 v4, 3, v0
v_lshl_add_u32 v3, v4, 4, v3
v_cmp_eq_u32 vcc, 0, v4
v_mov_b32 v5, 0x3f803f80
v_add_u32 v6, 0x2c00, v3
v_cndmask_b32 v16, 0, v5, vcc
v_cndmask_b32 v17, 0, v5, vcc
v_cndmask_b32 v18, 0, v5, vcc
v_cndmask_b32 v19, 0, v5, vcc
global_store_dwordx4 v3, v[16:19], s[38:39] offset:2880 sc1
global_store_dwordx4 v6, v[16:19], s[38:39] offset:2880 sc1
s_nop 2
Lo2_25_%=:
s_cmp_eq_u32 s47, 1
s_cbranch_scc0 Lo3_26_%=
s_getpc_b64 s[38:39]
k1_pc_ref:
s_add_u32 s38, s38, k2_code_begin@rel32@lo+4
s_addc_u32 s39, s39, k2_code_begin@rel32@hi+12
s_lshl_b32 s35, s46, 13
v_lshlrev_b32 v2, 7, v0
v_add_u32 v2, s35, v2
v_min_u32 v2, k2_code_end-k2_code_begin-4, v2
global_load_dword v3, v2, s[38:39]
v_add_u32 v2, 0x8000, v2
v_min_u32 v2, k2_code_end-k2_code_begin-4, v2
global_load_dword v4, v2, s[38:39]
s_nop 2
Lo3_26_%=:
s_branch Lend_3_%=
Lcomp_2_%=:
; ==== waves 0-3: 64x64 output tile each; row sums, sums of squares and column sums beside the MFMAs
s_lshr_b32 s33, s29, 1
s_and_b32 s34, s29, 1
v_lshrrev_b32 v18, 5, v0
v_and_b32 v19, 31, v0
s_lshl_b32 s35, s33, 6
s_lshl_b32 s36, s34, 5
s_add_u32 s35, s35, s36
v_add_u32 v2, s35, v19
v_bfe_u32 v4, v19, 1, 3
v_xor_b32 v4, v4, v18
v_lshlrev_b32 v2, 7, v2
v_xor_b32 v5, 0, v4
v_lshl_add_u32 v8, v5, 4, v2
v_xor_b32 v12, 0x1000, v8
v_xor_b32 v5, 2, v4
v_lshl_add_u32 v9, v5, 4, v2
v_xor_b32 v13, 0x1000, v9
v_xor_b32 v5, 4, v4
v_lshl_add_u32 v10, v5, 4, v2
v_xor_b32 v14, 0x1000, v10
v_xor_b32 v5, 6, v4
v_lshl_add_u32 v11, v5, 4, v2
v_xor_b32 v15, 0x1000, v11
v_bfe_u32 v2, v0, 2, 2
v_lshl_add_u32 v4, v18, 3, v2
v_lshlrev_b32 v4, 8, v4
v_bfe_u32 v5, v0, 4, 1
v_bfe_u32 v6, v0, 1, 1
v_lshl_add_u32 v5, v5, 1, v6
s_lshl_b32 s35, s34, 3
s_lshl_b32 s36, s33, 2
s_add_u32 s35, s35, s36
v_add_u32 v5, s35, v5
v_lshlrev_b32 v2, 2, v2
v_xor_b32 v5, v5, v2
v_lshl_add_u32 v4, v5, 4, v4
v_and_b32 v6, 1, v0
v_lshl_add_u32 v4, v6, 3, v4
v_add_u32 v16, 0x4000, v4
v_xor_b32 v17, 64, v16
v_mov_b32 v28, 0
v_mov_b32 v29, 0
v_bfe_u32 v2, v0, 2, 1
v_bfe_u32 v4, v0, 4, 1
v_cmp_eq_u32 vcc, v2, v4
v_mov_b32 v2, 0x3f803f80
s_nop 0
v_cndmask_b32 v208, 0, v2, vcc
v_cndmask_b32 v209, 0, v2, vcc
v_cndmask_b32 v210, 0, v2, vcc
v_cndmask_b32 v211, 0, v2, vcc
s_cmp_ge_u32 s30, 10
s_nop 3
s_cbranch_scc1 Lvt_27_%=
; ---- Q / K column tiles (rows on the lanes)
s_cmp_ge_u32 s30, 5
s_cselect_b32 s48, 0x1600, 0
s_cselect_b32 s37, 5, 0
s_cselect_b32 s49, 1.0, 0
s_cselect_b32 s46, 0x290000, 0
s_cmp_lt_u32 s30, 5
s_cselect_b32 s48, 0xa00, s48
s_cselect_b32 s49, 0x3ea52b5c, s49
s_cselect_b32 s46, 0x10000, s46
s_lshl_b32 s35, s31, 2
s_lshl_b32 s36, s33, 1
)ASM"
R"ASM(s_add_u32 s35, s35, s36
s_add_u32 s36, s35, s34
s_mul_i32 s36, s36, s48
s_add_u32 s36, s36, s46
s_waitcnt lgkmcnt(0)
s_add_u32 s38, s20, s36
s_addc_u32 s39, s21, 0
s_xor_b32 s36, s34, 1
s_add_u32 s36, s35, s36
s_mul_i32 s36, s36, s48
s_add_u32 s36, s36, s46
s_add_u32 s40, s20, s36
s_addc_u32 s41, s21, 0
v_and_b32 v2, 19, v19
v_bfe_u32 v3, v19, 2, 1
v_lshl_add_u32 v2, v3, 3, v2
v_bfe_u32 v3, v19, 3, 1
v_lshl_add_u32 v2, v3, 2, v2
s_cmp_ge_u32 s30, 5
s_cselect_b64 vcc, -1, 0
v_cndmask_b32 v2, v19, v2, vcc
v_lshlrev_b32 v2, 4, v2
s_sub_u32 s37, s30, s37
s_lshl_b32 s37, s37, 4
s_lshl_b32 s35, s34, 3
s_add_u32 s37, s37, s35
s_lshl_b32 s48, s48, 6
v_mov_b32 v6, 0x32410
s_xor_b32 s35, s33, 0
s_lshl_b32 s35, s35, 2
s_add_u32 s35, s35, s37
s_add_u32 s36, s35, 0
v_add_u32 v3, s36, v18
v_mul_u32_u24 v4, 0xcd, v3
v_lshrrev_b32 v4, 10, v4
v_mul_u32_u24 v5, 5, v4
v_sub_u32 v3, v3, v5
v_lshlrev_b32 v3, 2, v3
v_bfe_u32 v3, v6, v3, 4
v_lshl_add_u32 v3, v3, 9, v2
v_mad_u32_u24 v128, v4, s48, v3
s_add_u32 s36, s35, 2
v_add_u32 v3, s36, v18
v_mul_u32_u24 v4, 0xcd, v3
v_lshrrev_b32 v4, 10, v4
v_mul_u32_u24 v5, 5, v4
v_sub_u32 v3, v3, v5
v_lshlrev_b32 v3, 2, v3
v_bfe_u32 v3, v6, v3, 4
v_lshl_add_u32 v3, v3, 9, v2
v_mad_u32_u24 v129, v4, s48, v3
s_xor_b32 s35, s33, 1
s_lshl_b32 s35, s35, 2
s_add_u32 s35, s35, s37
s_add_u32 s36, s35, 0
v_add_u32 v3, s36, v18
v_mul_u32_u24 v4, 0xcd, v3
v_lshrrev_b32 v4, 10, v4
v_mul_u32_u24 v5, 5, v4
v_sub_u32 v3, v3, v5
v_lshlrev_b32 v3, 2, v3
v_bfe_u32 v3, v6, v3, 4
v_lshl_add_u32 v3, v3, 9, v2
v_mad_u32_u24 v130, v4, s48, v3
s_add_u32 s36, s35, 2
v_add_u32 v3, s36, v18
v_mul_u32_u24 v4, 0xcd, v3
v_lshrrev_b32 v4, 10, v4
v_mul_u32_u24 v5, 5, v4
v_sub_u32 v3, v3, v5
v_lshlrev_b32 v3, 2, v3
v_bfe_u32 v3, v6, v3, 4
v_lshl_add_u32 v3, v3, 9, v2
v_mad_u32_u24 v131, v4, s48, v3
s_barrier
ds_read_b128 v[96:99], v8
ds_read_b64_tr_b16 v[104:105], v16 offset:0
ds_read_b64_tr_b16 v[106:107], v16 offset:1024
ds_read_b64_tr_b16 v[108:109], v17 offset:0
ds_read_b64_tr_b16 v[110:111], v17 offset:1024
ds_read_b128 v[100:103], v12
s_waitcnt lgkmcnt(0)
v_mfma_f32_32x32x16_bf16 v[32:47], v[104:107], v[96:99], 0
ds_read_b128 v[112:115], v9
ds_read_b64_tr_b16 v[120:121], v16 offset:4096
v_lshlrev_b32 v30, 16, v96
v_and_b32 v31, 0xffff0000, v96
v_fmac_f32 v28, v30, v30
v_fmac_f32 v29, v31, v31
v_mfma_f32_32x32x16_bf16 v[48:63], v[108:111], v[96:99], 0
ds_read_b64_tr_b16 v[122:123], v16 offset:5120
ds_read_b64_tr_b16 v[124:125], v17 offset:4096
v_lshlrev_b32 v30, 16, v97
v_and_b32 v31, 0xffff0000, v97
v_fmac_f32 v28, v30, v30
v_fmac_f32 v29, v31, v31
v_mfma_f32_16x16x32_bf16 v[20:23], v[208:211], v[96:99], 0
v_mfma_f32_32x32x16_bf16 v[64:79], v[104:107], v[100:103], 0
ds_read_b64_tr_b16 v[126:127], v17 offset:5120
ds_read_b128 v[116:119], v13
v_lshlrev_b32 v30, 16, v98
v_and_b32 v31, 0xffff0000, v98
v_fmac_f32 v28, v30, v30
v_fmac_f32 v29, v31, v31
v_mfma_f32_32x32x16_bf16 v[80:95], v[108:111], v[100:103], 0
v_lshlrev_b32 v30, 16, v99
v_and_b32 v31, 0xffff0000, v99
v_fmac_f32 v28, v30, v30
v_fmac_f32 v29, v31, v31
v_mfma_f32_16x16x32_bf16 v[24:27], v[208:211], v[104:107], 0
s_waitcnt lgkmcnt(0)
v_mfma_f32_32x32x16_bf16 v[32:47], v[120:123], v[112:115], v[32:47]
ds_read_b128 v[96:99], v10
ds_read_b64_tr_b16 v[104:105], v16 offset:8192
v_lshlrev_b32 v30, 16, v112
v_and_b32 v31, 0xffff0000, v112
v_fmac_f32 v28, v30, v30
v_fmac_f32 v29, v31, v31
v_mfma_f32_32x32x16_bf16 v[48:63], v[124:127], v[112:115], v[48:63]
ds_read_b64_tr_b16 v[106:107], v16 offset:9216
ds_read_b64_tr_b16 v[108:109], v17 offset:8192
v_lshlrev_b32 v30, 16, v113
v_and_b32 v31, 0xffff0000, v113
v_fmac_f32 v28, v30, v30
v_fmac_f32 v29, v31, v31
v_mfma_f32_16x16x32_bf16 v[20:23], v[208:211], v[112:115], v[20:23]
v_mfma_f32_32x32x16_bf16 v[64:79], v[120:123], v[116:119], v[64:79]
ds_read_b64_tr_b16 v[110:111], v17 offset:9216
ds_read_b128 v[100:103], v14
v_lshlrev_b32 v30, 16, v114
v_and_b32 v31, 0xffff0000, v114
v_fmac_f32 v28, v30, v30
v_fmac_f32 v29, v31, v31
v_mfma_f32_32x32x16_bf16 v[80:95], v[124:127], v[116:119], v[80:95]
v_lshlrev_b32 v30, 16, v115
v_and_b32 v31, 0xffff0000, v115
v_fmac_f32 v28, v30, v30
v_fmac_f32 v29, v31, v31
v_mfma_f32_16x16x32_bf16 v[24:27], v[208:211], v[120:123], v[24:27]
s_waitcnt lgkmcnt(0)
v_mfma_f32_32x32x16_bf16 v[32:47], v[104:107], v[96:99], v[32:47]
ds_read_b128 v[112:115], v11
ds_read_b64_tr_b16 v[120:121], v16 offset:12288
v_lshlrev_b32 v30, 16, v96
v_and_b32 v31, 0xffff0000, v96
v_fmac_f32 v28, v30, v30
v_fmac_f32 v29, v31, v31
v_mfma_f32_32x32x16_bf16 v[48:63], v[108:111], v[96:99], v[48:63]
ds_read_b64_tr_b16 v[122:123], v16 offset:13312
ds_read_b64_tr_b16 v[124:125], v17 offset:12288
v_lshlrev_b32 v30, 16, v97
v_and_b32 v31, 0xffff0000, v97
v_fmac_f32 v28, v30, v30
v_fmac_f32 v29, v31, v31
v_mfma_f32_16x16x32_bf16 v[20:23], v[208:211], v[96:99], v[20:23]
v_mfma_f32_32x32x16_bf16 v[64:79], v[104:107], v[100:103], v[64:79]
ds_read_b64_tr_b16 v[126:127], v17 offset:13312
ds_read_b128 v[116:119], v15
v_lshlrev_b32 v30, 16, v98
v_and_b32 v31, 0xffff0000, v98
v_fmac_f32 v28, v30, v30
v_fmac_f32 v29, v31, v31
v_mfma_f32_32x32x16_bf16 v[80:95], v[108:111], v[100:103], v[80:95]
v_lshlrev_b32 v30, 16, v99
v_and_b32 v31, 0xffff0000, v99
v_fmac_f32 v28, v30, v30
v_fmac_f32 v29, v31, v31
v_mfma_f32_16x16x32_bf16 v[24:27], v[208:211], v[104:107], v[24:27]
s_waitcnt lgkmcnt(0)
s_barrier
v_add_u32 v8, 0x8000, v8
v_add_u32 v16, 0x8000, v16
v_add_u32 v17, 0x8000, v17
v_add_u32 v12, 0x8000, v12
v_mfma_f32_32x32x16_bf16 v[32:47], v[120:123], v[112:115], v[32:47]
ds_read_b128 v[96:99], v8
ds_read_b64_tr_b16 v[104:105], v16 offset:0
v_lshlrev_b32 v30, 16, v112
v_and_b32 v31, 0xffff0000, v112
v_fmac_f32 v28, v30, v30
v_fmac_f32 v29, v31, v31
v_mfma_f32_32x32x16_bf16 v[48:63], v[124:127], v[112:115], v[48:63]
ds_read_b64_tr_b16 v[106:107], v16 offset:1024
ds_read_b64_tr_b16 v[108:109], v17 offset:0
v_lshlrev_b32 v30, 16, v113
v_and_b32 v31, 0xffff0000, v113
v_fmac_f32 v28, v30, v30
v_fmac_f32 v29, v31, v31
v_mfma_f32_16x16x32_bf16 v[20:23], v[208:211], v[112:115], v[20:23]
v_mfma_f32_32x32x16_bf16 v[64:79], v[120:123], v[116:119], v[64:79]
ds_read_b64_tr_b16 v[110:111], v17 offset:1024
ds_read_b128 v[100:103], v12
v_lshlrev_b32 v30, 16, v114
v_and_b32 v31, 0xffff0000, v114
v_fmac_f32 v28, v30, v30
v_fmac_f32 v29, v31, v31
v_mfma_f32_32x32x16_bf16 v[80:95], v[124:127], v[116:119], v[80:95]
v_lshlrev_b32 v30, 16, v115
v_and_b32 v31, 0xffff0000, v115
v_fmac_f32 v28, v30, v30
)ASM"
R"ASM(v_fmac_f32 v29, v31, v31
v_mfma_f32_16x16x32_bf16 v[24:27], v[208:211], v[120:123], v[24:27]
s_waitcnt lgkmcnt(0)
v_add_u32 v9, 0x8000, v9
v_add_u32 v13, 0x8000, v13
v_mfma_f32_32x32x16_bf16 v[32:47], v[104:107], v[96:99], v[32:47]
ds_read_b128 v[112:115], v9
ds_read_b64_tr_b16 v[120:121], v16 offset:4096
v_lshlrev_b32 v30, 16, v96
v_and_b32 v31, 0xffff0000, v96
v_fmac_f32 v28, v30, v30
v_fmac_f32 v29, v31, v31
v_mfma_f32_32x32x16_bf16 v[48:63], v[108:111], v[96:99], v[48:63]
ds_read_b64_tr_b16 v[122:123], v16 offset:5120
ds_read_b64_tr_b16 v[124:125], v17 offset:4096
v_lshlrev_b32 v30, 16, v97
v_and_b32 v31, 0xffff0000, v97
v_fmac_f32 v28, v30, v30
v_fmac_f32 v29, v31, v31
v_mfma_f32_16x16x32_bf16 v[20:23], v[208:211], v[96:99], v[20:23]
v_mfma_f32_32x32x16_bf16 v[64:79], v[104:107], v[100:103], v[64:79]
ds_read_b64_tr_b16 v[126:127], v17 offset:5120
ds_read_b128 v[116:119], v13
v_lshlrev_b32 v30, 16, v98
v_and_b32 v31, 0xffff0000, v98
v_fmac_f32 v28, v30, v30
v_fmac_f32 v29, v31, v31
v_mfma_f32_32x32x16_bf16 v[80:95], v[108:111], v[100:103], v[80:95]
v_lshlrev_b32 v30, 16, v99
v_and_b32 v31, 0xffff0000, v99
v_fmac_f32 v28, v30, v30
v_fmac_f32 v29, v31, v31
v_mfma_f32_16x16x32_bf16 v[24:27], v[208:211], v[104:107], v[24:27]
s_waitcnt lgkmcnt(0)
v_add_u32 v10, 0x8000, v10
v_add_u32 v14, 0x8000, v14
v_mfma_f32_32x32x16_bf16 v[32:47], v[120:123], v[112:115], v[32:47]
ds_read_b128 v[96:99], v10
ds_read_b64_tr_b16 v[104:105], v16 offset:8192
v_lshlrev_b32 v30, 16, v112
v_and_b32 v31, 0xffff0000, v112
v_fmac_f32 v28, v30, v30
v_fmac_f32 v29, v31, v31
v_mfma_f32_32x32x16_bf16 v[48:63], v[124:127], v[112:115], v[48:63]
ds_read_b64_tr_b16 v[106:107], v16 offset:9216
ds_read_b64_tr_b16 v[108:109], v17 offset:8192
v_lshlrev_b32 v30, 16, v113
v_and_b32 v31, 0xffff0000, v113
v_fmac_f32 v28, v30, v30
v_fmac_f32 v29, v31, v31
v_mfma_f32_16x16x32_bf16 v[20:23], v[208:211], v[112:115], v[20:23]
v_mfma_f32_32x32x16_bf16 v[64:79], v[120:123], v[116:119], v[64:79]
ds_read_b64_tr_b16 v[110:111], v17 offset:9216
ds_read_b128 v[100:103], v14
v_lshlrev_b32 v30, 16, v114
v_and_b32 v31, 0xffff0000, v114
v_fmac_f32 v28, v30, v30
v_fmac_f32 v29, v31, v31
v_mfma_f32_32x32x16_bf16 v[80:95], v[124:127], v[116:119], v[80:95]
v_lshlrev_b32 v30, 16, v115
v_and_b32 v31, 0xffff0000, v115
v_fmac_f32 v28, v30, v30
v_fmac_f32 v29, v31, v31
v_mfma_f32_16x16x32_bf16 v[24:27], v[208:211], v[120:123], v[24:27]
s_waitcnt lgkmcnt(0)
v_add_u32 v11, 0x8000, v11
v_add_u32 v15, 0x8000, v15
v_mfma_f32_32x32x16_bf16 v[32:47], v[104:107], v[96:99], v[32:47]
ds_read_b128 v[112:115], v11
ds_read_b64_tr_b16 v[120:121], v16 offset:12288
v_lshlrev_b32 v30, 16, v96
v_and_b32 v31, 0xffff0000, v96
v_fmac_f32 v28, v30, v30
v_fmac_f32 v29, v31, v31
v_mfma_f32_32x32x16_bf16 v[48:63], v[108:111], v[96:99], v[48:63]
ds_read_b64_tr_b16 v[122:123], v16 offset:13312
ds_read_b64_tr_b16 v[124:125], v17 offset:12288
v_lshlrev_b32 v30, 16, v97
v_and_b32 v31, 0xffff0000, v97
v_fmac_f32 v28, v30, v30
v_fmac_f32 v29, v31, v31
v_mfma_f32_16x16x32_bf16 v[20:23], v[208:211], v[96:99], v[20:23]
v_mfma_f32_32x32x16_bf16 v[64:79], v[104:107], v[100:103], v[64:79]
ds_read_b64_tr_b16 v[126:127], v17 offset:13312
ds_read_b128 v[116:119], v15
v_lshlrev_b32 v30, 16, v98
v_and_b32 v31, 0xffff0000, v98
v_fmac_f32 v28, v30, v30
v_fmac_f32 v29, v31, v31
v_mfma_f32_32x32x16_bf16 v[80:95], v[108:111], v[100:103], v[80:95]
v_lshlrev_b32 v30, 16, v99
v_and_b32 v31, 0xffff0000, v99
v_fmac_f32 v28, v30, v30
v_fmac_f32 v29, v31, v31
v_mfma_f32_16x16x32_bf16 v[24:27], v[208:211], v[104:107], v[24:27]
s_waitcnt lgkmcnt(0)
s_barrier
v_add_u32 v8, 0x8000, v8
v_add_u32 v16, 0x8000, v16
v_add_u32 v17, 0x8000, v17
v_add_u32 v12, 0x8000, v12
v_mfma_f32_32x32x16_bf16 v[32:47], v[120:123], v[112:115], v[32:47]
ds_read_b128 v[96:99], v8
ds_read_b64_tr_b16 v[104:105], v16 offset:0
v_lshlrev_b32 v30, 16, v112
v_and_b32 v31, 0xffff0000, v112
v_fmac_f32 v28, v30, v30
v_fmac_f32 v29, v31, v31
v_mfma_f32_32x32x16_bf16 v[48:63], v[124:127], v[112:115], v[48:63]
ds_read_b64_tr_b16 v[106:107], v16 offset:1024
ds_read_b64_tr_b16 v[108:109], v17 offset:0
v_lshlrev_b32 v30, 16, v113
v_and_b32 v31, 0xffff0000, v113
v_fmac_f32 v28, v30, v30
v_fmac_f32 v29, v31, v31
v_mfma_f32_16x16x32_bf16 v[20:23], v[208:211], v[112:115], v[20:23]
v_mfma_f32_32x32x16_bf16 v[64:79], v[120:123], v[116:119], v[64:79]
ds_read_b64_tr_b16 v[110:111], v17 offset:1024
ds_read_b128 v[100:103], v12
v_lshlrev_b32 v30, 16, v114
v_and_b32 v31, 0xffff0000, v114
v_fmac_f32 v28, v30, v30
v_fmac_f32 v29, v31, v31
v_mfma_f32_32x32x16_bf16 v[80:95], v[124:127], v[116:119], v[80:95]
v_lshlrev_b32 v30, 16, v115
v_and_b32 v31, 0xffff0000, v115
v_fmac_f32 v28, v30, v30
v_fmac_f32 v29, v31, v31
v_mfma_f32_16x16x32_bf16 v[24:27], v[208:211], v[120:123], v[24:27]
s_waitcnt lgkmcnt(0)
v_add_u32 v9, 0x8000, v9
v_add_u32 v13, 0x8000, v13
v_mfma_f32_32x32x16_bf16 v[32:47], v[104:107], v[96:99], v[32:47]
ds_read_b128 v[112:115], v9
ds_read_b64_tr_b16 v[120:121], v16 offset:4096
v_lshlrev_b32 v30, 16, v96
v_and_b32 v31, 0xffff0000, v96
v_fmac_f32 v28, v30, v30
v_fmac_f32 v29, v31, v31
v_mfma_f32_32x32x16_bf16 v[48:63], v[108:111], v[96:99], v[48:63]
ds_read_b64_tr_b16 v[122:123], v16 offset:5120
ds_read_b64_tr_b16 v[124:125], v17 offset:4096
v_lshlrev_b32 v30, 16, v97
v_and_b32 v31, 0xffff0000, v97
v_fmac_f32 v28, v30, v30
v_fmac_f32 v29, v31, v31
v_mfma_f32_16x16x32_bf16 v[20:23], v[208:211], v[96:99], v[20:23]
v_mfma_f32_32x32x16_bf16 v[64:79], v[104:107], v[100:103], v[64:79]
ds_read_b64_tr_b16 v[126:127], v17 offset:5120
ds_read_b128 v[116:119], v13
v_lshlrev_b32 v30, 16, v98
v_and_b32 v31, 0xffff0000, v98
v_fmac_f32 v28, v30, v30
v_fmac_f32 v29, v31, v31
v_mfma_f32_32x32x16_bf16 v[80:95], v[108:111], v[100:103], v[80:95]
v_lshlrev_b32 v30, 16, v99
v_and_b32 v31, 0xffff0000, v99
v_fmac_f32 v28, v30, v30
v_fmac_f32 v29, v31, v31
v_mfma_f32_16x16x32_bf16 v[24:27], v[208:211], v[104:107], v[24:27]
s_waitcnt lgkmcnt(0)
v_add_u32 v10, 0x8000, v10
v_add_u32 v14, 0x8000, v14
v_mfma_f32_32x32x16_bf16 v[32:47], v[120:123], v[112:115], v[32:47]
ds_read_b128 v[96:99], v10
ds_read_b64_tr_b16 v[104:105], v16 offset:8192
v_lshlrev_b32 v30, 16, v112
v_and_b32 v31, 0xffff0000, v112
v_fmac_f32 v28, v30, v30
v_fmac_f32 v29, v31, v31
v_mfma_f32_32x32x16_bf16 v[48:63], v[124:127], v[112:115], v[48:63]
ds_read_b64_tr_b16 v[106:107], v16 offset:9216
ds_read_b64_tr_b16 v[108:109], v17 offset:8192
v_lshlrev_b32 v30, 16, v113
v_and_b32 v31, 0xffff0000, v113
v_fmac_f32 v28, v30, v30
v_fmac_f32 v29, v31, v31
v_mfma_f32_16x16x32_bf16 v[20:23], v[208:211], v[112:115], v[20:23]
v_mfma_f32_32x32x16_bf16 v[64:79], v[120:123], v[116:119], v[64:79]
ds_read_b64_tr_b16 v[110:111], v17 offset:9216
ds_read_b128 v[100:103], v14
v_lshlrev_b32 v30, 16, v114
v_and_b32 v31, 0xffff0000, v114
v_fmac_f32 v28, v30, v30
v_fmac_f32 v29, v31, v31
v_mfma_f32_32x32x16_bf16 v[80:95], v[124:127], v[116:119], v[80:95]
v_lshlrev_b32 v30, 16, v115
v_and_b32 v31, 0xffff0000, v115
v_fmac_f32 v28, v30, v30
v_fmac_f32 v29, v31, v31
v_mfma_f32_16x16x32_bf16 v[24:27], v[208:211], v[120:123], v[24:27]
s_waitcnt lgkmcnt(0)
v_add_u32 v11, 0x8000, v11
v_add_u32 v15, 0x8000, v15
v_mfma_f32_32x32x16_bf16 v[32:47], v[104:107], v[96:99], v[32:47]
ds_read_b128 v[112:115], v11
ds_read_b64_tr_b16 v[120:121], v16 offset:12288
v_lshlrev_b32 v30, 16, v96
v_and_b32 v31, 0xffff0000, v96
v_fmac_f32 v28, v30, v30
)ASM"
R"ASM(v_fmac_f32 v29, v31, v31
v_mfma_f32_32x32x16_bf16 v[48:63], v[108:111], v[96:99], v[48:63]
ds_read_b64_tr_b16 v[122:123], v16 offset:13312
ds_read_b64_tr_b16 v[124:125], v17 offset:12288
v_lshlrev_b32 v30, 16, v97
v_and_b32 v31, 0xffff0000, v97
v_fmac_f32 v28, v30, v30
v_fmac_f32 v29, v31, v31
v_mfma_f32_16x16x32_bf16 v[20:23], v[208:211], v[96:99], v[20:23]
v_mfma_f32_32x32x16_bf16 v[64:79], v[104:107], v[100:103], v[64:79]
ds_read_b64_tr_b16 v[126:127], v17 offset:13312
ds_read_b128 v[116:119], v15
v_lshlrev_b32 v30, 16, v98
v_and_b32 v31, 0xffff0000, v98
v_fmac_f32 v28, v30, v30
v_fmac_f32 v29, v31, v31
v_mfma_f32_32x32x16_bf16 v[80:95], v[108:111], v[100:103], v[80:95]
v_lshlrev_b32 v30, 16, v99
v_and_b32 v31, 0xffff0000, v99
v_fmac_f32 v28, v30, v30
v_fmac_f32 v29, v31, v31
v_mfma_f32_16x16x32_bf16 v[24:27], v[208:211], v[104:107], v[24:27]
s_waitcnt lgkmcnt(0)
s_barrier
v_add_u32 v8, 0x8000, v8
v_add_u32 v16, 0x8000, v16
v_add_u32 v17, 0x8000, v17
v_add_u32 v12, 0x8000, v12
v_mfma_f32_32x32x16_bf16 v[32:47], v[120:123], v[112:115], v[32:47]
ds_read_b128 v[96:99], v8
ds_read_b64_tr_b16 v[104:105], v16 offset:0
v_lshlrev_b32 v30, 16, v112
v_and_b32 v31, 0xffff0000, v112
v_fmac_f32 v28, v30, v30
v_fmac_f32 v29, v31, v31
v_mfma_f32_32x32x16_bf16 v[48:63], v[124:127], v[112:115], v[48:63]
ds_read_b64_tr_b16 v[106:107], v16 offset:1024
ds_read_b64_tr_b16 v[108:109], v17 offset:0
v_lshlrev_b32 v30, 16, v113
v_and_b32 v31, 0xffff0000, v113
v_fmac_f32 v28, v30, v30
v_fmac_f32 v29, v31, v31
v_mfma_f32_16x16x32_bf16 v[20:23], v[208:211], v[112:115], v[20:23]
v_mfma_f32_32x32x16_bf16 v[64:79], v[120:123], v[116:119], v[64:79]
ds_read_b64_tr_b16 v[110:111], v17 offset:1024
ds_read_b128 v[100:103], v12
v_lshlrev_b32 v30, 16, v114
v_and_b32 v31, 0xffff0000, v114
v_fmac_f32 v28, v30, v30
v_fmac_f32 v29, v31, v31
v_mfma_f32_32x32x16_bf16 v[80:95], v[124:127], v[116:119], v[80:95]
v_lshlrev_b32 v30, 16, v115
v_and_b32 v31, 0xffff0000, v115
v_fmac_f32 v28, v30, v30
v_fmac_f32 v29, v31, v31
v_mfma_f32_16x16x32_bf16 v[24:27], v[208:211], v[120:123], v[24:27]
s_waitcnt lgkmcnt(0)
v_add_u32 v9, 0x8000, v9
v_add_u32 v13, 0x8000, v13
v_mfma_f32_32x32x16_bf16 v[32:47], v[104:107], v[96:99], v[32:47]
ds_read_b128 v[112:115], v9
ds_read_b64_tr_b16 v[120:121], v16 offset:4096
v_lshlrev_b32 v30, 16, v96
v_and_b32 v31, 0xffff0000, v96
v_fmac_f32 v28, v30, v30
v_fmac_f32 v29, v31, v31
v_mfma_f32_32x32x16_bf16 v[48:63], v[108:111], v[96:99], v[48:63]
ds_read_b64_tr_b16 v[122:123], v16 offset:5120
ds_read_b64_tr_b16 v[124:125], v17 offset:4096
v_lshlrev_b32 v30, 16, v97
v_and_b32 v31, 0xffff0000, v97
v_fmac_f32 v28, v30, v30
v_fmac_f32 v29, v31, v31
v_mfma_f32_16x16x32_bf16 v[20:23], v[208:211], v[96:99], v[20:23]
v_mfma_f32_32x32x16_bf16 v[64:79], v[104:107], v[100:103], v[64:79]
ds_read_b64_tr_b16 v[126:127], v17 offset:5120
ds_read_b128 v[116:119], v13
v_lshlrev_b32 v30, 16, v98
v_and_b32 v31, 0xffff0000, v98
v_fmac_f32 v28, v30, v30
v_fmac_f32 v29, v31, v31
v_mfma_f32_32x32x16_bf16 v[80:95], v[108:111], v[100:103], v[80:95]
v_lshlrev_b32 v30, 16, v99
v_and_b32 v31, 0xffff0000, v99
v_fmac_f32 v28, v30, v30
v_fmac_f32 v29, v31, v31
v_mfma_f32_16x16x32_bf16 v[24:27], v[208:211], v[104:107], v[24:27]
s_waitcnt lgkmcnt(0)
v_add_u32 v10, 0x8000, v10
v_add_u32 v14, 0x8000, v14
v_mfma_f32_32x32x16_bf16 v[32:47], v[120:123], v[112:115], v[32:47]
ds_read_b128 v[96:99], v10
ds_read_b64_tr_b16 v[104:105], v16 offset:8192
v_lshlrev_b32 v30, 16, v112
v_and_b32 v31, 0xffff0000, v112
v_fmac_f32 v28, v30, v30
v_fmac_f32 v29, v31, v31
v_mfma_f32_32x32x16_bf16 v[48:63], v[124:127], v[112:115], v[48:63]
ds_read_b64_tr_b16 v[106:107], v16 offset:9216
ds_read_b64_tr_b16 v[108:109], v17 offset:8192
v_lshlrev_b32 v30, 16, v113
v_and_b32 v31, 0xffff0000, v113
v_fmac_f32 v28, v30, v30
v_fmac_f32 v29, v31, v31
v_mfma_f32_16x16x32_bf16 v[20:23], v[208:211], v[112:115], v[20:23]
v_mfma_f32_32x32x16_bf16 v[64:79], v[120:123], v[116:119], v[64:79]
ds_read_b64_tr_b16 v[110:111], v17 offset:9216
ds_read_b128 v[100:103], v14
v_lshlrev_b32 v30, 16, v114
v_and_b32 v31, 0xffff0000, v114
v_fmac_f32 v28, v30, v30
v_fmac_f32 v29, v31, v31
v_mfma_f32_32x32x16_bf16 v[80:95], v[124:127], v[116:119], v[80:95]
v_lshlrev_b32 v30, 16, v115
v_and_b32 v31, 0xffff0000, v115
v_fmac_f32 v28, v30, v30
v_fmac_f32 v29, v31, v31
v_mfma_f32_16x16x32_bf16 v[24:27], v[208:211], v[120:123], v[24:27]
s_waitcnt lgkmcnt(0)
v_add_u32 v11, 0x8000, v11
v_add_u32 v15, 0x8000, v15
v_mfma_f32_32x32x16_bf16 v[32:47], v[104:107], v[96:99], v[32:47]
ds_read_b128 v[112:115], v11
ds_read_b64_tr_b16 v[120:121], v16 offset:12288
v_lshlrev_b32 v30, 16, v96
v_and_b32 v31, 0xffff0000, v96
v_fmac_f32 v28, v30, v30
v_fmac_f32 v29, v31, v31
v_mfma_f32_32x32x16_bf16 v[48:63], v[108:111], v[96:99], v[48:63]
ds_read_b64_tr_b16 v[122:123], v16 offset:13312
ds_read_b64_tr_b16 v[124:125], v17 offset:12288
v_lshlrev_b32 v30, 16, v97
v_and_b32 v31, 0xffff0000, v97
v_fmac_f32 v28, v30, v30
v_fmac_f32 v29, v31, v31
v_mfma_f32_16x16x32_bf16 v[20:23], v[208:211], v[96:99], v[20:23]
v_mfma_f32_32x32x16_bf16 v[64:79], v[104:107], v[100:103], v[64:79]
ds_read_b64_tr_b16 v[126:127], v17 offset:13312
ds_read_b128 v[116:119], v15
v_lshlrev_b32 v30, 16, v98
v_and_b32 v31, 0xffff0000, v98
v_fmac_f32 v28, v30, v30
v_fmac_f32 v29, v31, v31
v_mfma_f32_32x32x16_bf16 v[80:95], v[108:111], v[100:103], v[80:95]
v_lshlrev_b32 v30, 16, v99
v_and_b32 v31, 0xffff0000, v99
v_fmac_f32 v28, v30, v30
v_fmac_f32 v29, v31, v31
v_mfma_f32_16x16x32_bf16 v[24:27], v[208:211], v[104:107], v[24:27]
s_waitcnt lgkmcnt(0)
s_barrier
v_add_u32 v8, 0x8000, v8
v_add_u32 v16, 0x8000, v16
v_add_u32 v17, 0x8000, v17
v_add_u32 v12, 0x8000, v12
v_mfma_f32_32x32x16_bf16 v[32:47], v[120:123], v[112:115], v[32:47]
ds_read_b128 v[96:99], v8
ds_read_b64_tr_b16 v[104:105], v16 offset:0
v_lshlrev_b32 v30, 16, v112
v_and_b32 v31, 0xffff0000, v112
v_fmac_f32 v28, v30, v30
v_fmac_f32 v29, v31, v31
v_mfma_f32_32x32x16_bf16 v[48:63], v[124:127], v[112:115], v[48:63]
ds_read_b64_tr_b16 v[106:107], v16 offset:1024
ds_read_b64_tr_b16 v[108:109], v17 offset:0
v_lshlrev_b32 v30, 16, v113
v_and_b32 v31, 0xffff0000, v113
v_fmac_f32 v28, v30, v30
v_fmac_f32 v29, v31, v31
v_mfma_f32_16x16x32_bf16 v[20:23], v[208:211], v[112:115], v[20:23]
v_mfma_f32_32x32x16_bf16 v[64:79], v[120:123], v[116:119], v[64:79]
ds_read_b64_tr_b16 v[110:111], v17 offset:1024
ds_read_b128 v[100:103], v12
v_lshlrev_b32 v30, 16, v114
v_and_b32 v31, 0xffff0000, v114
v_fmac_f32 v28, v30, v30
v_fmac_f32 v29, v31, v31
v_mfma_f32_32x32x16_bf16 v[80:95], v[124:127], v[116:119], v[80:95]
v_lshlrev_b32 v30, 16, v115
v_and_b32 v31, 0xffff0000, v115
v_fmac_f32 v28, v30, v30
v_fmac_f32 v29, v31, v31
v_mfma_f32_16x16x32_bf16 v[24:27], v[208:211], v[120:123], v[24:27]
s_waitcnt lgkmcnt(0)
v_add_u32 v9, 0x8000, v9
v_add_u32 v13, 0x8000, v13
v_mfma_f32_32x32x16_bf16 v[32:47], v[104:107], v[96:99], v[32:47]
ds_read_b128 v[112:115], v9
ds_read_b64_tr_b16 v[120:121], v16 offset:4096
v_lshlrev_b32 v30, 16, v96
v_and_b32 v31, 0xffff0000, v96
v_fmac_f32 v28, v30, v30
v_fmac_f32 v29, v31, v31
v_mfma_f32_32x32x16_bf16 v[48:63], v[108:111], v[96:99], v[48:63]
ds_read_b64_tr_b16 v[122:123], v16 offset:5120
ds_read_b64_tr_b16 v[124:125], v17 offset:4096
v_lshlrev_b32 v30, 16, v97
v_and_b32 v31, 0xffff0000, v97
v_fmac_f32 v28, v30, v30
v_fmac_f32 v29, v31, v31
)ASM"
R"ASM(v_mfma_f32_16x16x32_bf16 v[20:23], v[208:211], v[96:99], v[20:23]
v_mfma_f32_32x32x16_bf16 v[64:79], v[104:107], v[100:103], v[64:79]
ds_read_b64_tr_b16 v[126:127], v17 offset:5120
ds_read_b128 v[116:119], v13
v_lshlrev_b32 v30, 16, v98
v_and_b32 v31, 0xffff0000, v98
v_fmac_f32 v28, v30, v30
v_fmac_f32 v29, v31, v31
v_mfma_f32_32x32x16_bf16 v[80:95], v[108:111], v[100:103], v[80:95]
v_lshlrev_b32 v30, 16, v99
v_and_b32 v31, 0xffff0000, v99
v_fmac_f32 v28, v30, v30
v_fmac_f32 v29, v31, v31
v_mfma_f32_16x16x32_bf16 v[24:27], v[208:211], v[104:107], v[24:27]
s_waitcnt lgkmcnt(0)
v_add_u32 v10, 0x8000, v10
v_add_u32 v14, 0x8000, v14
v_mfma_f32_32x32x16_bf16 v[32:47], v[120:123], v[112:115], v[32:47]
ds_read_b128 v[96:99], v10
ds_read_b64_tr_b16 v[104:105], v16 offset:8192
v_lshlrev_b32 v30, 16, v112
v_and_b32 v31, 0xffff0000, v112
v_fmac_f32 v28, v30, v30
v_fmac_f32 v29, v31, v31
v_mfma_f32_32x32x16_bf16 v[48:63], v[124:127], v[112:115], v[48:63]
ds_read_b64_tr_b16 v[106:107], v16 offset:9216
ds_read_b64_tr_b16 v[108:109], v17 offset:8192
v_lshlrev_b32 v30, 16, v113
v_and_b32 v31, 0xffff0000, v113
v_fmac_f32 v28, v30, v30
v_fmac_f32 v29, v31, v31
v_mfma_f32_16x16x32_bf16 v[20:23], v[208:211], v[112:115], v[20:23]
v_mfma_f32_32x32x16_bf16 v[64:79], v[120:123], v[116:119], v[64:79]
ds_read_b64_tr_b16 v[110:111], v17 offset:9216
ds_read_b128 v[100:103], v14
v_lshlrev_b32 v30, 16, v114
v_and_b32 v31, 0xffff0000, v114
v_fmac_f32 v28, v30, v30
v_fmac_f32 v29, v31, v31
v_mfma_f32_32x32x16_bf16 v[80:95], v[124:127], v[116:119], v[80:95]
v_lshlrev_b32 v30, 16, v115
v_and_b32 v31, 0xffff0000, v115
v_fmac_f32 v28, v30, v30
v_fmac_f32 v29, v31, v31
v_mfma_f32_16x16x32_bf16 v[24:27], v[208:211], v[120:123], v[24:27]
s_waitcnt lgkmcnt(0)
v_add_u32 v11, 0x8000, v11
v_add_u32 v15, 0x8000, v15
v_mfma_f32_32x32x16_bf16 v[32:47], v[104:107], v[96:99], v[32:47]
ds_read_b128 v[112:115], v11
ds_read_b64_tr_b16 v[120:121], v16 offset:12288
v_lshlrev_b32 v30, 16, v96
v_and_b32 v31, 0xffff0000, v96
v_fmac_f32 v28, v30, v30
v_fmac_f32 v29, v31, v31
v_mfma_f32_32x32x16_bf16 v[48:63], v[108:111], v[96:99], v[48:63]
ds_read_b64_tr_b16 v[122:123], v16 offset:13312
ds_read_b64_tr_b16 v[124:125], v17 offset:12288
v_lshlrev_b32 v30, 16, v97
v_and_b32 v31, 0xffff0000, v97
v_fmac_f32 v28, v30, v30
v_fmac_f32 v29, v31, v31
v_mfma_f32_16x16x32_bf16 v[20:23], v[208:211], v[96:99], v[20:23]
v_mfma_f32_32x32x16_bf16 v[64:79], v[104:107], v[100:103], v[64:79]
ds_read_b64_tr_b16 v[126:127], v17 offset:13312
ds_read_b128 v[116:119], v15
v_lshlrev_b32 v30, 16, v98
v_and_b32 v31, 0xffff0000, v98
v_fmac_f32 v28, v30, v30
v_fmac_f32 v29, v31, v31
v_mfma_f32_32x32x16_bf16 v[80:95], v[108:111], v[100:103], v[80:95]
v_lshlrev_b32 v30, 16, v99
v_and_b32 v31, 0xffff0000, v99
v_fmac_f32 v28, v30, v30
v_fmac_f32 v29, v31, v31
v_mfma_f32_16x16x32_bf16 v[24:27], v[208:211], v[104:107], v[24:27]
s_waitcnt lgkmcnt(0)
s_barrier
v_add_u32 v8, 0xfffe0000, v8
v_add_u32 v16, 0xfffe0000, v16
v_add_u32 v17, 0xfffe0000, v17
v_add_u32 v12, 0xfffe0000, v12
v_mfma_f32_32x32x16_bf16 v[32:47], v[120:123], v[112:115], v[32:47]
ds_read_b128 v[96:99], v8
ds_read_b64_tr_b16 v[104:105], v16 offset:0
v_lshlrev_b32 v30, 16, v112
v_and_b32 v31, 0xffff0000, v112
v_fmac_f32 v28, v30, v30
v_fmac_f32 v29, v31, v31
v_mfma_f32_32x32x16_bf16 v[48:63], v[124:127], v[112:115], v[48:63]
ds_read_b64_tr_b16 v[106:107], v16 offset:1024
ds_read_b64_tr_b16 v[108:109], v17 offset:0
v_lshlrev_b32 v30, 16, v113
v_and_b32 v31, 0xffff0000, v113
v_fmac_f32 v28, v30, v30
v_fmac_f32 v29, v31, v31
v_mfma_f32_16x16x32_bf16 v[20:23], v[208:211], v[112:115], v[20:23]
v_mfma_f32_32x32x16_bf16 v[64:79], v[120:123], v[116:119], v[64:79]
ds_read_b64_tr_b16 v[110:111], v17 offset:1024
ds_read_b128 v[100:103], v12
v_lshlrev_b32 v30, 16, v114
v_and_b32 v31, 0xffff0000, v114
v_fmac_f32 v28, v30, v30
v_fmac_f32 v29, v31, v31
v_mfma_f32_32x32x16_bf16 v[80:95], v[124:127], v[116:119], v[80:95]
v_lshlrev_b32 v30, 16, v115
v_and_b32 v31, 0xffff0000, v115
v_fmac_f32 v28, v30, v30
v_fmac_f32 v29, v31, v31
v_mfma_f32_16x16x32_bf16 v[24:27], v[208:211], v[120:123], v[24:27]
s_waitcnt lgkmcnt(0)
v_add_u32 v9, 0xfffe0000, v9
v_add_u32 v13, 0xfffe0000, v13
v_mfma_f32_32x32x16_bf16 v[32:47], v[104:107], v[96:99], v[32:47]
ds_read_b128 v[112:115], v9
ds_read_b64_tr_b16 v[120:121], v16 offset:4096
v_lshlrev_b32 v30, 16, v96
v_and_b32 v31, 0xffff0000, v96
v_fmac_f32 v28, v30, v30
v_fmac_f32 v29, v31, v31
v_mfma_f32_32x32x16_bf16 v[48:63], v[108:111], v[96:99], v[48:63]
ds_read_b64_tr_b16 v[122:123], v16 offset:5120
ds_read_b64_tr_b16 v[124:125], v17 offset:4096
v_lshlrev_b32 v30, 16, v97
v_and_b32 v31, 0xffff0000, v97
v_fmac_f32 v28, v30, v30
v_fmac_f32 v29, v31, v31
v_mfma_f32_16x16x32_bf16 v[20:23], v[208:211], v[96:99], v[20:23]
v_mfma_f32_32x32x16_bf16 v[64:79], v[104:107], v[100:103], v[64:79]
ds_read_b64_tr_b16 v[126:127], v17 offset:5120
ds_read_b128 v[116:119], v13
v_lshlrev_b32 v30, 16, v98
v_and_b32 v31, 0xffff0000, v98
v_fmac_f32 v28, v30, v30
v_fmac_f32 v29, v31, v31
v_mfma_f32_32x32x16_bf16 v[80:95], v[108:111], v[100:103], v[80:95]
v_lshlrev_b32 v30, 16, v99
v_and_b32 v31, 0xffff0000, v99
v_fmac_f32 v28, v30, v30
v_fmac_f32 v29, v31, v31
v_mfma_f32_16x16x32_bf16 v[24:27], v[208:211], v[104:107], v[24:27]
s_waitcnt lgkmcnt(0)
v_add_u32 v10, 0xfffe0000, v10
v_add_u32 v14, 0xfffe0000, v14
v_mfma_f32_32x32x16_bf16 v[32:47], v[120:123], v[112:115], v[32:47]
ds_read_b128 v[96:99], v10
ds_read_b64_tr_b16 v[104:105], v16 offset:8192
v_lshlrev_b32 v30, 16, v112
v_and_b32 v31, 0xffff0000, v112
v_fmac_f32 v28, v30, v30
v_fmac_f32 v29, v31, v31
v_mfma_f32_32x32x16_bf16 v[48:63], v[124:127], v[112:115], v[48:63]
ds_read_b64_tr_b16 v[106:107], v16 offset:9216
ds_read_b64_tr_b16 v[108:109], v17 offset:8192
v_lshlrev_b32 v30, 16, v113
v_and_b32 v31, 0xffff0000, v113
v_fmac_f32 v28, v30, v30
v_fmac_f32 v29, v31, v31
v_mfma_f32_16x16x32_bf16 v[20:23], v[208:211], v[112:115], v[20:23]
v_mfma_f32_32x32x16_bf16 v[64:79], v[120:123], v[116:119], v[64:79]
ds_read_b64_tr_b16 v[110:111], v17 offset:9216
ds_read_b128 v[100:103], v14
v_lshlrev_b32 v30, 16, v114
v_and_b32 v31, 0xffff0000, v114
v_fmac_f32 v28, v30, v30
v_fmac_f32 v29, v31, v31
v_mfma_f32_32x32x16_bf16 v[80:95], v[124:127], v[116:119], v[80:95]
v_lshlrev_b32 v30, 16, v115
v_and_b32 v31, 0xffff0000, v115
v_fmac_f32 v28, v30, v30
v_fmac_f32 v29, v31, v31
v_mfma_f32_16x16x32_bf16 v[24:27], v[208:211], v[120:123], v[24:27]
s_waitcnt lgkmcnt(0)
v_add_u32 v11, 0xfffe0000, v11
v_add_u32 v15, 0xfffe0000, v15
v_mfma_f32_32x32x16_bf16 v[32:47], v[104:107], v[96:99], v[32:47]
ds_read_b128 v[112:115], v11
ds_read_b64_tr_b16 v[120:121], v16 offset:12288
v_lshlrev_b32 v30, 16, v96
v_and_b32 v31, 0xffff0000, v96
v_fmac_f32 v28, v30, v30
v_fmac_f32 v29, v31, v31
v_mfma_f32_32x32x16_bf16 v[48:63], v[108:111], v[96:99], v[48:63]
ds_read_b64_tr_b16 v[122:123], v16 offset:13312
ds_read_b64_tr_b16 v[124:125], v17 offset:12288
v_lshlrev_b32 v30, 16, v97
v_and_b32 v31, 0xffff0000, v97
v_fmac_f32 v28, v30, v30
v_fmac_f32 v29, v31, v31
v_mfma_f32_16x16x32_bf16 v[20:23], v[208:211], v[96:99], v[20:23]
v_mfma_f32_32x32x16_bf16 v[64:79], v[104:107], v[100:103], v[64:79]
ds_read_b64_tr_b16 v[126:127], v17 offset:13312
ds_read_b128 v[116:119], v15
v_lshlrev_b32 v30, 16, v98
v_and_b32 v31, 0xffff0000, v98
v_fmac_f32 v28, v30, v30
v_fmac_f32 v29, v31, v31
v_mfma_f32_32x32x16_bf16 v[80:95], v[108:111], v[100:103], v[80:95]
v_lshlrev_b32 v30, 16, v99
v_and_b32 v31, 0xffff0000, v99
)ASM"
R"ASM(v_fmac_f32 v28, v30, v30
v_fmac_f32 v29, v31, v31
v_mfma_f32_16x16x32_bf16 v[24:27], v[208:211], v[104:107], v[24:27]
s_waitcnt lgkmcnt(0)
s_barrier
v_add_u32 v8, 0x8000, v8
v_add_u32 v16, 0x8000, v16
v_add_u32 v17, 0x8000, v17
v_add_u32 v12, 0x8000, v12
v_mfma_f32_32x32x16_bf16 v[32:47], v[120:123], v[112:115], v[32:47]
ds_read_b128 v[96:99], v8
ds_read_b64_tr_b16 v[104:105], v16 offset:0
v_lshlrev_b32 v30, 16, v112
v_and_b32 v31, 0xffff0000, v112
v_fmac_f32 v28, v30, v30
v_fmac_f32 v29, v31, v31
v_mfma_f32_32x32x16_bf16 v[48:63], v[124:127], v[112:115], v[48:63]
ds_read_b64_tr_b16 v[106:107], v16 offset:1024
ds_read_b64_tr_b16 v[108:109], v17 offset:0
v_lshlrev_b32 v30, 16, v113
v_and_b32 v31, 0xffff0000, v113
v_fmac_f32 v28, v30, v30
v_fmac_f32 v29, v31, v31
v_mfma_f32_16x16x32_bf16 v[20:23], v[208:211], v[112:115], v[20:23]
v_mfma_f32_32x32x16_bf16 v[64:79], v[120:123], v[116:119], v[64:79]
ds_read_b64_tr_b16 v[110:111], v17 offset:1024
ds_read_b128 v[100:103], v12
v_lshlrev_b32 v30, 16, v114
v_and_b32 v31, 0xffff0000, v114
v_fmac_f32 v28, v30, v30
v_fmac_f32 v29, v31, v31
v_mfma_f32_32x32x16_bf16 v[80:95], v[124:127], v[116:119], v[80:95]
v_lshlrev_b32 v30, 16, v115
v_and_b32 v31, 0xffff0000, v115
v_fmac_f32 v28, v30, v30
v_fmac_f32 v29, v31, v31
v_mfma_f32_16x16x32_bf16 v[24:27], v[208:211], v[120:123], v[24:27]
s_waitcnt lgkmcnt(0)
v_add_u32 v9, 0x8000, v9
v_add_u32 v13, 0x8000, v13
v_mfma_f32_32x32x16_bf16 v[32:47], v[104:107], v[96:99], v[32:47]
ds_read_b128 v[112:115], v9
ds_read_b64_tr_b16 v[120:121], v16 offset:4096
v_lshlrev_b32 v30, 16, v96
v_and_b32 v31, 0xffff0000, v96
v_fmac_f32 v28, v30, v30
v_fmac_f32 v29, v31, v31
v_mfma_f32_32x32x16_bf16 v[48:63], v[108:111], v[96:99], v[48:63]
ds_read_b64_tr_b16 v[122:123], v16 offset:5120
ds_read_b64_tr_b16 v[124:125], v17 offset:4096
v_lshlrev_b32 v30, 16, v97
v_and_b32 v31, 0xffff0000, v97
v_fmac_f32 v28, v30, v30
v_fmac_f32 v29, v31, v31
v_mfma_f32_16x16x32_bf16 v[20:23], v[208:211], v[96:99], v[20:23]
v_mfma_f32_32x32x16_bf16 v[64:79], v[104:107], v[100:103], v[64:79]
ds_read_b64_tr_b16 v[126:127], v17 offset:5120
ds_read_b128 v[116:119], v13
v_lshlrev_b32 v30, 16, v98
v_and_b32 v31, 0xffff0000, v98
v_fmac_f32 v28, v30, v30
v_fmac_f32 v29, v31, v31
v_mfma_f32_32x32x16_bf16 v[80:95], v[108:111], v[100:103], v[80:95]
v_lshlrev_b32 v30, 16, v99
v_and_b32 v31, 0xffff0000, v99
v_fmac_f32 v28, v30, v30
v_fmac_f32 v29, v31, v31
v_mfma_f32_16x16x32_bf16 v[24:27], v[208:211], v[104:107], v[24:27]
s_waitcnt lgkmcnt(0)
v_add_u32 v10, 0x8000, v10
v_add_u32 v14, 0x8000, v14
v_mfma_f32_32x32x16_bf16 v[32:47], v[120:123], v[112:115], v[32:47]
ds_read_b128 v[96:99], v10
ds_read_b64_tr_b16 v[104:105], v16 offset:8192
v_lshlrev_b32 v30, 16, v112
v_and_b32 v31, 0xffff0000, v112
v_fmac_f32 v28, v30, v30
v_fmac_f32 v29, v31, v31
v_mfma_f32_32x32x16_bf16 v[48:63], v[124:127], v[112:115], v[48:63]
ds_read_b64_tr_b16 v[106:107], v16 offset:9216
ds_read_b64_tr_b16 v[108:109], v17 offset:8192
v_lshlrev_b32 v30, 16, v113
v_and_b32 v31, 0xffff0000, v113
v_fmac_f32 v28, v30, v30
v_fmac_f32 v29, v31, v31
v_mfma_f32_16x16x32_bf16 v[20:23], v[208:211], v[112:115], v[20:23]
v_mfma_f32_32x32x16_bf16 v[64:79], v[120:123], v[116:119], v[64:79]
ds_read_b64_tr_b16 v[110:111], v17 offset:9216
ds_read_b128 v[100:103], v14
v_lshlrev_b32 v30, 16, v114
v_and_b32 v31, 0xffff0000, v114
v_fmac_f32 v28, v30, v30
v_fmac_f32 v29, v31, v31
v_mfma_f32_32x32x16_bf16 v[80:95], v[124:127], v[116:119], v[80:95]
v_lshlrev_b32 v30, 16, v115
v_and_b32 v31, 0xffff0000, v115
v_fmac_f32 v28, v30, v30
v_fmac_f32 v29, v31, v31
v_mfma_f32_16x16x32_bf16 v[24:27], v[208:211], v[120:123], v[24:27]
s_waitcnt lgkmcnt(0)
v_add_u32 v11, 0x8000, v11
v_add_u32 v15, 0x8000, v15
v_mfma_f32_32x32x16_bf16 v[32:47], v[104:107], v[96:99], v[32:47]
ds_read_b128 v[112:115], v11
ds_read_b64_tr_b16 v[120:121], v16 offset:12288
v_lshlrev_b32 v30, 16, v96
v_and_b32 v31, 0xffff0000, v96
v_fmac_f32 v28, v30, v30
v_fmac_f32 v29, v31, v31
v_mfma_f32_32x32x16_bf16 v[48:63], v[108:111], v[96:99], v[48:63]
ds_read_b64_tr_b16 v[122:123], v16 offset:13312
ds_read_b64_tr_b16 v[124:125], v17 offset:12288
v_lshlrev_b32 v30, 16, v97
v_and_b32 v31, 0xffff0000, v97
v_fmac_f32 v28, v30, v30
v_fmac_f32 v29, v31, v31
v_mfma_f32_16x16x32_bf16 v[20:23], v[208:211], v[96:99], v[20:23]
v_mfma_f32_32x32x16_bf16 v[64:79], v[104:107], v[100:103], v[64:79]
ds_read_b64_tr_b16 v[126:127], v17 offset:13312
ds_read_b128 v[116:119], v15
v_lshlrev_b32 v30, 16, v98
v_and_b32 v31, 0xffff0000, v98
v_fmac_f32 v28, v30, v30
v_fmac_f32 v29, v31, v31
v_mfma_f32_32x32x16_bf16 v[80:95], v[108:111], v[100:103], v[80:95]
v_lshlrev_b32 v30, 16, v99
v_and_b32 v31, 0xffff0000, v99
v_fmac_f32 v28, v30, v30
v_fmac_f32 v29, v31, v31
v_mfma_f32_16x16x32_bf16 v[24:27], v[208:211], v[104:107], v[24:27]
s_waitcnt lgkmcnt(0)
s_barrier
v_add_u32 v8, 0x8000, v8
v_add_u32 v16, 0x8000, v16
v_add_u32 v17, 0x8000, v17
v_add_u32 v12, 0x8000, v12
v_mfma_f32_32x32x16_bf16 v[32:47], v[120:123], v[112:115], v[32:47]
ds_read_b128 v[96:99], v8
ds_read_b64_tr_b16 v[104:105], v16 offset:0
v_lshlrev_b32 v30, 16, v112
v_and_b32 v31, 0xffff0000, v112
v_fmac_f32 v28, v30, v30
v_fmac_f32 v29, v31, v31
v_mfma_f32_32x32x16_bf16 v[48:63], v[124:127], v[112:115], v[48:63]
ds_read_b64_tr_b16 v[106:107], v16 offset:1024
ds_read_b64_tr_b16 v[108:109], v17 offset:0
v_lshlrev_b32 v30, 16, v113
v_and_b32 v31, 0xffff0000, v113
v_fmac_f32 v28, v30, v30
v_fmac_f32 v29, v31, v31
v_mfma_f32_16x16x32_bf16 v[20:23], v[208:211], v[112:115], v[20:23]
v_mfma_f32_32x32x16_bf16 v[64:79], v[120:123], v[116:119], v[64:79]
ds_read_b64_tr_b16 v[110:111], v17 offset:1024
ds_read_b128 v[100:103], v12
v_lshlrev_b32 v30, 16, v114
v_and_b32 v31, 0xffff0000, v114
v_fmac_f32 v28, v30, v30
v_fmac_f32 v29, v31, v31
v_mfma_f32_32x32x16_bf16 v[80:95], v[124:127], v[116:119], v[80:95]
v_lshlrev_b32 v30, 16, v115
v_and_b32 v31, 0xffff0000, v115
v_fmac_f32 v28, v30, v30
v_fmac_f32 v29, v31, v31
v_mfma_f32_16x16x32_bf16 v[24:27], v[208:211], v[120:123], v[24:27]
s_waitcnt lgkmcnt(0)
v_add_u32 v9, 0x8000, v9
v_add_u32 v13, 0x8000, v13
v_mfma_f32_32x32x16_bf16 v[32:47], v[104:107], v[96:99], v[32:47]
ds_read_b128 v[112:115], v9
ds_read_b64_tr_b16 v[120:121], v16 offset:4096
v_lshlrev_b32 v30, 16, v96
v_and_b32 v31, 0xffff0000, v96
v_fmac_f32 v28, v30, v30
v_fmac_f32 v29, v31, v31
v_mfma_f32_32x32x16_bf16 v[48:63], v[108:111], v[96:99], v[48:63]
ds_read_b64_tr_b16 v[122:123], v16 offset:5120
ds_read_b64_tr_b16 v[124:125], v17 offset:4096
v_lshlrev_b32 v30, 16, v97
v_and_b32 v31, 0xffff0000, v97
v_fmac_f32 v28, v30, v30
v_fmac_f32 v29, v31, v31
v_mfma_f32_16x16x32_bf16 v[20:23], v[208:211], v[96:99], v[20:23]
v_mfma_f32_32x32x16_bf16 v[64:79], v[104:107], v[100:103], v[64:79]
ds_read_b64_tr_b16 v[126:127], v17 offset:5120
ds_read_b128 v[116:119], v13
v_lshlrev_b32 v30, 16, v98
v_and_b32 v31, 0xffff0000, v98
v_fmac_f32 v28, v30, v30
v_fmac_f32 v29, v31, v31
v_mfma_f32_32x32x16_bf16 v[80:95], v[108:111], v[100:103], v[80:95]
v_lshlrev_b32 v30, 16, v99
v_and_b32 v31, 0xffff0000, v99
v_fmac_f32 v28, v30, v30
v_fmac_f32 v29, v31, v31
v_mfma_f32_16x16x32_bf16 v[24:27], v[208:211], v[104:107], v[24:27]
s_waitcnt lgkmcnt(0)
v_add_u32 v10, 0x8000, v10
v_add_u32 v14, 0x8000, v14
v_mfma_f32_32x32x16_bf16 v[32:47], v[120:123], v[112:115], v[32:47]
ds_read_b128 v[96:99], v10
)ASM"
R"ASM(ds_read_b64_tr_b16 v[104:105], v16 offset:8192
v_lshlrev_b32 v30, 16, v112
v_and_b32 v31, 0xffff0000, v112
v_fmac_f32 v28, v30, v30
v_fmac_f32 v29, v31, v31
v_mfma_f32_32x32x16_bf16 v[48:63], v[124:127], v[112:115], v[48:63]
ds_read_b64_tr_b16 v[106:107], v16 offset:9216
ds_read_b64_tr_b16 v[108:109], v17 offset:8192
v_lshlrev_b32 v30, 16, v113
v_and_b32 v31, 0xffff0000, v113
v_fmac_f32 v28, v30, v30
v_fmac_f32 v29, v31, v31
v_mfma_f32_16x16x32_bf16 v[20:23], v[208:211], v[112:115], v[20:23]
v_mfma_f32_32x32x16_bf16 v[64:79], v[120:123], v[116:119], v[64:79]
ds_read_b64_tr_b16 v[110:111], v17 offset:9216
ds_read_b128 v[100:103], v14
v_lshlrev_b32 v30, 16, v114
v_and_b32 v31, 0xffff0000, v114
v_fmac_f32 v28, v30, v30
v_fmac_f32 v29, v31, v31
v_mfma_f32_32x32x16_bf16 v[80:95], v[124:127], v[116:119], v[80:95]
v_lshlrev_b32 v30, 16, v115
v_and_b32 v31, 0xffff0000, v115
v_fmac_f32 v28, v30, v30
v_fmac_f32 v29, v31, v31
v_mfma_f32_16x16x32_bf16 v[24:27], v[208:211], v[120:123], v[24:27]
s_waitcnt lgkmcnt(0)
v_add_u32 v11, 0x8000, v11
v_add_u32 v15, 0x8000, v15
v_mfma_f32_32x32x16_bf16 v[32:47], v[104:107], v[96:99], v[32:47]
ds_read_b128 v[112:115], v11
ds_read_b64_tr_b16 v[120:121], v16 offset:12288
v_lshlrev_b32 v30, 16, v96
v_and_b32 v31, 0xffff0000, v96
v_fmac_f32 v28, v30, v30
v_fmac_f32 v29, v31, v31
v_mfma_f32_32x32x16_bf16 v[48:63], v[108:111], v[96:99], v[48:63]
ds_read_b64_tr_b16 v[122:123], v16 offset:13312
ds_read_b64_tr_b16 v[124:125], v17 offset:12288
v_lshlrev_b32 v30, 16, v97
v_and_b32 v31, 0xffff0000, v97
v_fmac_f32 v28, v30, v30
v_fmac_f32 v29, v31, v31
v_mfma_f32_16x16x32_bf16 v[20:23], v[208:211], v[96:99], v[20:23]
v_mfma_f32_32x32x16_bf16 v[64:79], v[104:107], v[100:103], v[64:79]
ds_read_b64_tr_b16 v[126:127], v17 offset:13312
ds_read_b128 v[116:119], v15
v_lshlrev_b32 v30, 16, v98
v_and_b32 v31, 0xffff0000, v98
v_fmac_f32 v28, v30, v30
v_fmac_f32 v29, v31, v31
v_mfma_f32_32x32x16_bf16 v[80:95], v[108:111], v[100:103], v[80:95]
v_lshlrev_b32 v30, 16, v99
v_and_b32 v31, 0xffff0000, v99
v_fmac_f32 v28, v30, v30
v_fmac_f32 v29, v31, v31
v_mfma_f32_16x16x32_bf16 v[24:27], v[208:211], v[104:107], v[24:27]
s_waitcnt lgkmcnt(0)
s_barrier
v_add_u32 v8, 0x8000, v8
v_add_u32 v16, 0x8000, v16
v_add_u32 v17, 0x8000, v17
v_add_u32 v12, 0x8000, v12
v_mfma_f32_32x32x16_bf16 v[32:47], v[120:123], v[112:115], v[32:47]
ds_read_b128 v[96:99], v8
ds_read_b64_tr_b16 v[104:105], v16 offset:0
v_lshlrev_b32 v30, 16, v112
v_and_b32 v31, 0xffff0000, v112
v_fmac_f32 v28, v30, v30
v_fmac_f32 v29, v31, v31
v_mfma_f32_32x32x16_bf16 v[48:63], v[124:127], v[112:115], v[48:63]
ds_read_b64_tr_b16 v[106:107], v16 offset:1024
ds_read_b64_tr_b16 v[108:109], v17 offset:0
v_lshlrev_b32 v30, 16, v113
v_and_b32 v31, 0xffff0000, v113
v_fmac_f32 v28, v30, v30
v_fmac_f32 v29, v31, v31
v_mfma_f32_16x16x32_bf16 v[20:23], v[208:211], v[112:115], v[20:23]
v_mfma_f32_32x32x16_bf16 v[64:79], v[120:123], v[116:119], v[64:79]
ds_read_b64_tr_b16 v[110:111], v17 offset:1024
ds_read_b128 v[100:103], v12
v_lshlrev_b32 v30, 16, v114
v_and_b32 v31, 0xffff0000, v114
v_fmac_f32 v28, v30, v30
v_fmac_f32 v29, v31, v31
v_mfma_f32_32x32x16_bf16 v[80:95], v[124:127], v[116:119], v[80:95]
v_lshlrev_b32 v30, 16, v115
v_and_b32 v31, 0xffff0000, v115
v_fmac_f32 v28, v30, v30
v_fmac_f32 v29, v31, v31
v_mfma_f32_16x16x32_bf16 v[24:27], v[208:211], v[120:123], v[24:27]
s_waitcnt lgkmcnt(0)
v_add_u32 v9, 0x8000, v9
v_add_u32 v13, 0x8000, v13
v_mfma_f32_32x32x16_bf16 v[32:47], v[104:107], v[96:99], v[32:47]
ds_read_b128 v[112:115], v9
ds_read_b64_tr_b16 v[120:121], v16 offset:4096
v_lshlrev_b32 v30, 16, v96
v_and_b32 v31, 0xffff0000, v96
v_fmac_f32 v28, v30, v30
v_fmac_f32 v29, v31, v31
v_mfma_f32_32x32x16_bf16 v[48:63], v[108:111], v[96:99], v[48:63]
ds_read_b64_tr_b16 v[122:123], v16 offset:5120
ds_read_b64_tr_b16 v[124:125], v17 offset:4096
v_lshlrev_b32 v30, 16, v97
v_and_b32 v31, 0xffff0000, v97
v_fmac_f32 v28, v30, v30
v_fmac_f32 v29, v31, v31
v_mfma_f32_16x16x32_bf16 v[20:23], v[208:211], v[96:99], v[20:23]
v_mfma_f32_32x32x16_bf16 v[64:79], v[104:107], v[100:103], v[64:79]
ds_read_b64_tr_b16 v[126:127], v17 offset:5120
ds_read_b128 v[116:119], v13
v_lshlrev_b32 v30, 16, v98
v_and_b32 v31, 0xffff0000, v98
v_fmac_f32 v28, v30, v30
v_fmac_f32 v29, v31, v31
v_mfma_f32_32x32x16_bf16 v[80:95], v[108:111], v[100:103], v[80:95]
v_lshlrev_b32 v30, 16, v99
v_and_b32 v31, 0xffff0000, v99
v_fmac_f32 v28, v30, v30
v_fmac_f32 v29, v31, v31
v_mfma_f32_16x16x32_bf16 v[24:27], v[208:211], v[104:107], v[24:27]
s_waitcnt lgkmcnt(0)
v_add_u32 v10, 0x8000, v10
v_add_u32 v14, 0x8000, v14
v_mfma_f32_32x32x16_bf16 v[32:47], v[120:123], v[112:115], v[32:47]
ds_read_b128 v[96:99], v10
ds_read_b64_tr_b16 v[104:105], v16 offset:8192
v_lshlrev_b32 v30, 16, v112
v_and_b32 v31, 0xffff0000, v112
v_fmac_f32 v28, v30, v30
v_fmac_f32 v29, v31, v31
v_mfma_f32_32x32x16_bf16 v[48:63], v[124:127], v[112:115], v[48:63]
ds_read_b64_tr_b16 v[106:107], v16 offset:9216
ds_read_b64_tr_b16 v[108:109], v17 offset:8192
v_lshlrev_b32 v30, 16, v113
v_and_b32 v31, 0xffff0000, v113
v_fmac_f32 v28, v30, v30
v_fmac_f32 v29, v31, v31
v_mfma_f32_16x16x32_bf16 v[20:23], v[208:211], v[112:115], v[20:23]
v_mfma_f32_32x32x16_bf16 v[64:79], v[120:123], v[116:119], v[64:79]
ds_read_b64_tr_b16 v[110:111], v17 offset:9216
ds_read_b128 v[100:103], v14
v_lshlrev_b32 v30, 16, v114
v_and_b32 v31, 0xffff0000, v114
v_fmac_f32 v28, v30, v30
v_fmac_f32 v29, v31, v31
v_mfma_f32_32x32x16_bf16 v[80:95], v[124:127], v[116:119], v[80:95]
v_lshlrev_b32 v30, 16, v115
v_and_b32 v31, 0xffff0000, v115
v_fmac_f32 v28, v30, v30
v_fmac_f32 v29, v31, v31
v_mfma_f32_16x16x32_bf16 v[24:27], v[208:211], v[120:123], v[24:27]
s_waitcnt lgkmcnt(0)
v_add_u32 v11, 0x8000, v11
v_add_u32 v15, 0x8000, v15
v_mfma_f32_32x32x16_bf16 v[32:47], v[104:107], v[96:99], v[32:47]
ds_read_b128 v[112:115], v11
ds_read_b64_tr_b16 v[120:121], v16 offset:12288
v_lshlrev_b32 v30, 16, v96
v_and_b32 v31, 0xffff0000, v96
v_fmac_f32 v28, v30, v30
v_fmac_f32 v29, v31, v31
v_mfma_f32_32x32x16_bf16 v[48:63], v[108:111], v[96:99], v[48:63]
ds_read_b64_tr_b16 v[122:123], v16 offset:13312
ds_read_b64_tr_b16 v[124:125], v17 offset:12288
v_lshlrev_b32 v30, 16, v97
v_and_b32 v31, 0xffff0000, v97
v_fmac_f32 v28, v30, v30
v_fmac_f32 v29, v31, v31
v_mfma_f32_16x16x32_bf16 v[20:23], v[208:211], v[96:99], v[20:23]
v_mfma_f32_32x32x16_bf16 v[64:79], v[104:107], v[100:103], v[64:79]
ds_read_b64_tr_b16 v[126:127], v17 offset:13312
ds_read_b128 v[116:119], v15
v_lshlrev_b32 v30, 16, v98
v_and_b32 v31, 0xffff0000, v98
v_fmac_f32 v28, v30, v30
v_fmac_f32 v29, v31, v31
v_mfma_f32_32x32x16_bf16 v[80:95], v[108:111], v[100:103], v[80:95]
v_lshlrev_b32 v30, 16, v99
v_and_b32 v31, 0xffff0000, v99
v_fmac_f32 v28, v30, v30
v_fmac_f32 v29, v31, v31
v_mfma_f32_16x16x32_bf16 v[24:27], v[208:211], v[104:107], v[24:27]
s_waitcnt lgkmcnt(0)
s_barrier
v_add_u32 v8, 0x8000, v8
v_add_u32 v16, 0x8000, v16
v_add_u32 v17, 0x8000, v17
v_add_u32 v12, 0x8000, v12
v_mfma_f32_32x32x16_bf16 v[32:47], v[120:123], v[112:115], v[32:47]
ds_read_b128 v[96:99], v8
ds_read_b64_tr_b16 v[104:105], v16 offset:0
v_lshlrev_b32 v30, 16, v112
v_and_b32 v31, 0xffff0000, v112
v_fmac_f32 v28, v30, v30
v_fmac_f32 v29, v31, v31
v_mfma_f32_32x32x16_bf16 v[48:63], v[124:127], v[112:115], v[48:63]
ds_read_b64_tr_b16 v[106:107], v16 offset:1024
ds_read_b64_tr_b16 v[108:109], v17 offset:0
)ASM"
R"ASM(v_lshlrev_b32 v30, 16, v113
v_and_b32 v31, 0xffff0000, v113
v_fmac_f32 v28, v30, v30
v_fmac_f32 v29, v31, v31
v_mfma_f32_16x16x32_bf16 v[20:23], v[208:211], v[112:115], v[20:23]
v_mfma_f32_32x32x16_bf16 v[64:79], v[120:123], v[116:119], v[64:79]
ds_read_b64_tr_b16 v[110:111], v17 offset:1024
ds_read_b128 v[100:103], v12
v_lshlrev_b32 v30, 16, v114
v_and_b32 v31, 0xffff0000, v114
v_fmac_f32 v28, v30, v30
v_fmac_f32 v29, v31, v31
v_mfma_f32_32x32x16_bf16 v[80:95], v[124:127], v[116:119], v[80:95]
v_lshlrev_b32 v30, 16, v115
v_and_b32 v31, 0xffff0000, v115
v_fmac_f32 v28, v30, v30
v_fmac_f32 v29, v31, v31
v_mfma_f32_16x16x32_bf16 v[24:27], v[208:211], v[120:123], v[24:27]
s_waitcnt lgkmcnt(0)
v_add_u32 v9, 0x8000, v9
v_add_u32 v13, 0x8000, v13
v_mfma_f32_32x32x16_bf16 v[32:47], v[104:107], v[96:99], v[32:47]
ds_read_b128 v[112:115], v9
ds_read_b64_tr_b16 v[120:121], v16 offset:4096
v_lshlrev_b32 v30, 16, v96
v_and_b32 v31, 0xffff0000, v96
v_fmac_f32 v28, v30, v30
v_fmac_f32 v29, v31, v31
v_mfma_f32_32x32x16_bf16 v[48:63], v[108:111], v[96:99], v[48:63]
ds_read_b64_tr_b16 v[122:123], v16 offset:5120
ds_read_b64_tr_b16 v[124:125], v17 offset:4096
v_lshlrev_b32 v30, 16, v97
v_and_b32 v31, 0xffff0000, v97
v_fmac_f32 v28, v30, v30
v_fmac_f32 v29, v31, v31
v_mfma_f32_16x16x32_bf16 v[20:23], v[208:211], v[96:99], v[20:23]
v_mfma_f32_32x32x16_bf16 v[64:79], v[104:107], v[100:103], v[64:79]
ds_read_b64_tr_b16 v[126:127], v17 offset:5120
ds_read_b128 v[116:119], v13
v_lshlrev_b32 v30, 16, v98
v_and_b32 v31, 0xffff0000, v98
v_fmac_f32 v28, v30, v30
v_fmac_f32 v29, v31, v31
v_mfma_f32_32x32x16_bf16 v[80:95], v[108:111], v[100:103], v[80:95]
v_lshlrev_b32 v30, 16, v99
v_and_b32 v31, 0xffff0000, v99
v_fmac_f32 v28, v30, v30
v_fmac_f32 v29, v31, v31
v_mfma_f32_16x16x32_bf16 v[24:27], v[208:211], v[104:107], v[24:27]
s_waitcnt lgkmcnt(0)
v_add_u32 v10, 0x8000, v10
v_add_u32 v14, 0x8000, v14
v_mfma_f32_32x32x16_bf16 v[32:47], v[120:123], v[112:115], v[32:47]
ds_read_b128 v[96:99], v10
ds_read_b64_tr_b16 v[104:105], v16 offset:8192
v_lshlrev_b32 v30, 16, v112
v_and_b32 v31, 0xffff0000, v112
v_fmac_f32 v28, v30, v30
v_fmac_f32 v29, v31, v31
v_mfma_f32_32x32x16_bf16 v[48:63], v[124:127], v[112:115], v[48:63]
ds_read_b64_tr_b16 v[106:107], v16 offset:9216
ds_read_b64_tr_b16 v[108:109], v17 offset:8192
v_lshlrev_b32 v30, 16, v113
v_and_b32 v31, 0xffff0000, v113
v_fmac_f32 v28, v30, v30
v_fmac_f32 v29, v31, v31
v_mfma_f32_16x16x32_bf16 v[20:23], v[208:211], v[112:115], v[20:23]
v_mfma_f32_32x32x16_bf16 v[64:79], v[120:123], v[116:119], v[64:79]
ds_read_b64_tr_b16 v[110:111], v17 offset:9216
ds_read_b128 v[100:103], v14
v_lshlrev_b32 v30, 16, v114
v_and_b32 v31, 0xffff0000, v114
v_fmac_f32 v28, v30, v30
v_fmac_f32 v29, v31, v31
v_mfma_f32_32x32x16_bf16 v[80:95], v[124:127], v[116:119], v[80:95]
v_lshlrev_b32 v30, 16, v115
v_and_b32 v31, 0xffff0000, v115
v_fmac_f32 v28, v30, v30
v_fmac_f32 v29, v31, v31
v_mfma_f32_16x16x32_bf16 v[24:27], v[208:211], v[120:123], v[24:27]
s_waitcnt lgkmcnt(0)
v_add_u32 v11, 0x8000, v11
v_add_u32 v15, 0x8000, v15
v_mfma_f32_32x32x16_bf16 v[32:47], v[104:107], v[96:99], v[32:47]
ds_read_b128 v[112:115], v11
ds_read_b64_tr_b16 v[120:121], v16 offset:12288
v_lshlrev_b32 v30, 16, v96
v_and_b32 v31, 0xffff0000, v96
v_fmac_f32 v28, v30, v30
v_fmac_f32 v29, v31, v31
v_lshlrev_b32 v30, 16, v97
v_and_b32 v31, 0xffff0000, v97
v_mfma_f32_32x32x16_bf16 v[48:63], v[108:111], v[96:99], v[48:63]
ds_read_b64_tr_b16 v[122:123], v16 offset:13312
ds_read_b64_tr_b16 v[124:125], v17 offset:12288
v_fmac_f32 v28, v30, v30
v_fmac_f32 v29, v31, v31
v_lshlrev_b32 v30, 16, v98
v_and_b32 v31, 0xffff0000, v98
v_fmac_f32 v28, v30, v30
v_fmac_f32 v29, v31, v31
v_mfma_f32_16x16x32_bf16 v[20:23], v[208:211], v[96:99], v[20:23]
v_mfma_f32_32x32x16_bf16 v[64:79], v[104:107], v[100:103], v[64:79]
ds_read_b64_tr_b16 v[126:127], v17 offset:13312
ds_read_b128 v[116:119], v15
v_lshlrev_b32 v30, 16, v99
v_and_b32 v31, 0xffff0000, v99
v_fmac_f32 v28, v30, v30
v_fmac_f32 v29, v31, v31
v_mfma_f32_32x32x16_bf16 v[80:95], v[108:111], v[100:103], v[80:95]
v_mfma_f32_16x16x32_bf16 v[24:27], v[208:211], v[104:107], v[24:27]
s_waitcnt lgkmcnt(0)
v_mfma_f32_32x32x16_bf16 v[32:47], v[120:123], v[112:115], v[32:47]
v_lshlrev_b32 v30, 16, v112
v_and_b32 v31, 0xffff0000, v112
v_fmac_f32 v28, v30, v30
v_fmac_f32 v29, v31, v31
v_mfma_f32_32x32x16_bf16 v[48:63], v[124:127], v[112:115], v[48:63]
v_lshlrev_b32 v30, 16, v113
v_and_b32 v31, 0xffff0000, v113
v_fmac_f32 v28, v30, v30
v_fmac_f32 v29, v31, v31
v_mfma_f32_16x16x32_bf16 v[20:23], v[208:211], v[112:115], v[20:23]
v_mfma_f32_32x32x16_bf16 v[64:79], v[120:123], v[116:119], v[64:79]
v_lshlrev_b32 v30, 16, v114
v_and_b32 v31, 0xffff0000, v114
v_fmac_f32 v28, v30, v30
v_fmac_f32 v29, v31, v31
v_mfma_f32_32x32x16_bf16 v[80:95], v[124:127], v[116:119], v[80:95]
v_lshlrev_b32 v30, 16, v115
v_and_b32 v31, 0xffff0000, v115
v_fmac_f32 v28, v30, v30
v_fmac_f32 v29, v31, v31
v_mfma_f32_16x16x32_bf16 v[24:27], v[208:211], v[120:123], v[24:27]
s_lshl_b32 s35, s33, 6
s_lshl_b32 s36, s34, 5
s_add_u32 s35, s35, s36
s_add_u32 s35, s35, 0x2000
v_add_u32 v236, s35, v19
v_lshlrev_b32 v236, 2, v236
s_lshl_b32 s35, s34, 6
s_lshl_b32 s36, s33, 5
s_add_u32 s35, s35, s36
s_add_u32 s35, s35, 0x2000
v_add_u32 v237, s35, v19
v_lshlrev_b32 v237, 2, v237
v_xor_b32 v238, 0x80, v236
v_mov_b32 v6, -1
s_lshl_b32 s35, s34, 8
s_lshl_b32 s36, s33, 7
s_add_u32 s35, s35, s36
s_add_u32 s35, s35, 0x8000
v_lshlrev_b32 v239, 4, v18
v_add_u32 v239, s35, v239
v_xor_b32 v240, 0x80, v239
v_add_f32 v28, v28, v29
v_mov_b32 v29, v28
v_mov_b32 v2, 0x3acccccd
ds_write_b32 v237, v24 offset:1024
v_permlane32_swap_b32 v28, v29
v_mul_f32 v21, 0x3acccccd, v20
v_add_f32 v28, v28, v29
v_mul_f32 v29, v21, v21
ds_write_b32 v236, v21
v_fma_f32 v28, v28, v2, -v29
v_max_f32 v28, 0, v28
v_add_f32 v28, 0x3727c5ac, v28
v_rsq_f32 v28, v28
s_nop 0
ds_write_b32 v236, v28 offset:512
s_waitcnt lgkmcnt(0)
s_barrier
ds_read_b32 v168, v236
ds_read_b32 v172, v236 offset:512
ds_read_b128 v[136:139], v239 offset:1024
ds_read_b128 v[140:143], v239 offset:1056
ds_read_b128 v[144:147], v239 offset:1088
ds_read_b128 v[148:151], v239 offset:1120
ds_read_b32 v170, v238
ds_read_b32 v174, v238 offset:512
ds_read_b128 v[152:155], v240 offset:1024
ds_read_b128 v[156:159], v240 offset:1056
ds_read_b128 v[160:163], v240 offset:1088
ds_read_b128 v[164:167], v240 offset:1120
s_cmp_eq_u32 s30, 0
s_cbranch_scc0 Lxs_28_%=
s_lshl_b32 s35, s31, 7
s_lshl_b32 s36, s33, 6
s_add_u32 s35, s35, s36
s_lshl_b32 s36, s34, 5
s_add_u32 s35, s35, s36
s_lshl_b32 s35, s35, 2
s_add_u32 s35, s35, 0xe000
s_add_u32 s44, s20, s35
s_addc_u32 s45, s21, 0
v_lshlrev_b32 v6, 2, v19
global_store_dword v6, v20, s[44:45] sc1
s_nop 3
Lxs_28_%=:
s_waitcnt lgkmcnt(11)
)ASM"
R"ASM(v_sub_f32 v168, 0, v168
s_waitcnt lgkmcnt(10)
v_mul_f32 v172, s49, v172
s_waitcnt lgkmcnt(9)
v_pk_fma_f32 v[32:33], v[136:137], v[168:169], v[32:33] op_sel_hi:[1,0,1]
v_pk_mul_f32 v[32:33], v[32:33], v[172:173] op_sel_hi:[1,0]
v_cvt_pk_bf16_f32 v96, v32, v33
v_pk_fma_f32 v[34:35], v[138:139], v[168:169], v[34:35] op_sel_hi:[1,0,1]
v_pk_mul_f32 v[34:35], v[34:35], v[172:173] op_sel_hi:[1,0]
v_cvt_pk_bf16_f32 v97, v34, v35
s_waitcnt lgkmcnt(8)
v_pk_fma_f32 v[36:37], v[140:141], v[168:169], v[36:37] op_sel_hi:[1,0,1]
v_pk_mul_f32 v[36:37], v[36:37], v[172:173] op_sel_hi:[1,0]
v_cvt_pk_bf16_f32 v98, v36, v37
v_pk_fma_f32 v[38:39], v[142:143], v[168:169], v[38:39] op_sel_hi:[1,0,1]
v_pk_mul_f32 v[38:39], v[38:39], v[172:173] op_sel_hi:[1,0]
v_cvt_pk_bf16_f32 v99, v38, v39
s_waitcnt lgkmcnt(7)
v_pk_fma_f32 v[40:41], v[144:145], v[168:169], v[40:41] op_sel_hi:[1,0,1]
v_pk_mul_f32 v[40:41], v[40:41], v[172:173] op_sel_hi:[1,0]
v_cvt_pk_bf16_f32 v100, v40, v41
v_pk_fma_f32 v[42:43], v[146:147], v[168:169], v[42:43] op_sel_hi:[1,0,1]
v_pk_mul_f32 v[42:43], v[42:43], v[172:173] op_sel_hi:[1,0]
v_cvt_pk_bf16_f32 v101, v42, v43
s_waitcnt lgkmcnt(6)
v_pk_fma_f32 v[44:45], v[148:149], v[168:169], v[44:45] op_sel_hi:[1,0,1]
v_pk_mul_f32 v[44:45], v[44:45], v[172:173] op_sel_hi:[1,0]
v_cvt_pk_bf16_f32 v102, v44, v45
v_pk_fma_f32 v[46:47], v[150:151], v[168:169], v[46:47] op_sel_hi:[1,0,1]
v_pk_mul_f32 v[46:47], v[46:47], v[172:173] op_sel_hi:[1,0]
v_cvt_pk_bf16_f32 v103, v46, v47
v_permlane32_swap_b32 v96, v98
v_permlane32_swap_b32 v97, v99
global_store_dwordx4 v128, v[96:99], s[38:39] sc1
s_waitcnt lgkmcnt(5)
v_sub_f32 v170, 0, v170
s_waitcnt lgkmcnt(4)
v_mul_f32 v174, s49, v174
s_waitcnt lgkmcnt(3)
v_pk_fma_f32 v[48:49], v[152:153], v[168:169], v[48:49] op_sel_hi:[1,0,1]
v_pk_mul_f32 v[48:49], v[48:49], v[172:173] op_sel_hi:[1,0]
v_cvt_pk_bf16_f32 v104, v48, v49
v_pk_fma_f32 v[50:51], v[154:155], v[168:169], v[50:51] op_sel_hi:[1,0,1]
v_pk_mul_f32 v[50:51], v[50:51], v[172:173] op_sel_hi:[1,0]
v_cvt_pk_bf16_f32 v105, v50, v51
s_waitcnt lgkmcnt(2)
v_pk_fma_f32 v[52:53], v[156:157], v[168:169], v[52:53] op_sel_hi:[1,0,1]
v_pk_mul_f32 v[52:53], v[52:53], v[172:173] op_sel_hi:[1,0]
v_cvt_pk_bf16_f32 v106, v52, v53
v_pk_fma_f32 v[54:55], v[158:159], v[168:169], v[54:55] op_sel_hi:[1,0,1]
v_pk_mul_f32 v[54:55], v[54:55], v[172:173] op_sel_hi:[1,0]
v_cvt_pk_bf16_f32 v107, v54, v55
v_permlane32_swap_b32 v100, v102
v_permlane32_swap_b32 v101, v103
global_store_dwordx4 v129, v[100:103], s[38:39] sc1
s_waitcnt lgkmcnt(1)
v_pk_fma_f32 v[56:57], v[160:161], v[168:169], v[56:57] op_sel_hi:[1,0,1]
v_pk_mul_f32 v[56:57], v[56:57], v[172:173] op_sel_hi:[1,0]
v_cvt_pk_bf16_f32 v108, v56, v57
v_pk_fma_f32 v[58:59], v[162:163], v[168:169], v[58:59] op_sel_hi:[1,0,1]
v_pk_mul_f32 v[58:59], v[58:59], v[172:173] op_sel_hi:[1,0]
v_cvt_pk_bf16_f32 v109, v58, v59
s_waitcnt lgkmcnt(0)
v_pk_fma_f32 v[60:61], v[164:165], v[168:169], v[60:61] op_sel_hi:[1,0,1]
v_pk_mul_f32 v[60:61], v[60:61], v[172:173] op_sel_hi:[1,0]
v_cvt_pk_bf16_f32 v110, v60, v61
v_pk_fma_f32 v[62:63], v[166:167], v[168:169], v[62:63] op_sel_hi:[1,0,1]
v_pk_mul_f32 v[62:63], v[62:63], v[172:173] op_sel_hi:[1,0]
v_cvt_pk_bf16_f32 v111, v62, v63
v_permlane32_swap_b32 v104, v106
v_permlane32_swap_b32 v105, v107
global_store_dwordx4 v130, v[104:107], s[38:39] sc1
v_pk_fma_f32 v[64:65], v[136:137], v[170:171], v[64:65] op_sel_hi:[1,0,1]
v_pk_mul_f32 v[64:65], v[64:65], v[174:175] op_sel_hi:[1,0]
v_cvt_pk_bf16_f32 v96, v64, v65
v_pk_fma_f32 v[66:67], v[138:139], v[170:171], v[66:67] op_sel_hi:[1,0,1]
v_pk_mul_f32 v[66:67], v[66:67], v[174:175] op_sel_hi:[1,0]
v_cvt_pk_bf16_f32 v97, v66, v67
v_pk_fma_f32 v[68:69], v[140:141], v[170:171], v[68:69] op_sel_hi:[1,0,1]
v_pk_mul_f32 v[68:69], v[68:69], v[174:175] op_sel_hi:[1,0]
v_cvt_pk_bf16_f32 v98, v68, v69
v_pk_fma_f32 v[70:71], v[142:143], v[170:171], v[70:71] op_sel_hi:[1,0,1]
v_pk_mul_f32 v[70:71], v[70:71], v[174:175] op_sel_hi:[1,0]
v_cvt_pk_bf16_f32 v99, v70, v71
v_permlane32_swap_b32 v108, v110
v_permlane32_swap_b32 v109, v111
global_store_dwordx4 v131, v[108:111], s[38:39] sc1
v_pk_fma_f32 v[72:73], v[144:145], v[170:171], v[72:73] op_sel_hi:[1,0,1]
v_pk_mul_f32 v[72:73], v[72:73], v[174:175] op_sel_hi:[1,0]
v_cvt_pk_bf16_f32 v100, v72, v73
v_pk_fma_f32 v[74:75], v[146:147], v[170:171], v[74:75] op_sel_hi:[1,0,1]
v_pk_mul_f32 v[74:75], v[74:75], v[174:175] op_sel_hi:[1,0]
v_cvt_pk_bf16_f32 v101, v74, v75
v_pk_fma_f32 v[76:77], v[148:149], v[170:171], v[76:77] op_sel_hi:[1,0,1]
v_pk_mul_f32 v[76:77], v[76:77], v[174:175] op_sel_hi:[1,0]
v_cvt_pk_bf16_f32 v102, v76, v77
v_pk_fma_f32 v[78:79], v[150:151], v[170:171], v[78:79] op_sel_hi:[1,0,1]
v_pk_mul_f32 v[78:79], v[78:79], v[174:175] op_sel_hi:[1,0]
v_cvt_pk_bf16_f32 v103, v78, v79
v_permlane32_swap_b32 v96, v98
v_permlane32_swap_b32 v97, v99
global_store_dwordx4 v128, v[96:99], s[40:41] sc1
v_pk_fma_f32 v[80:81], v[152:153], v[170:171], v[80:81] op_sel_hi:[1,0,1]
v_pk_mul_f32 v[80:81], v[80:81], v[174:175] op_sel_hi:[1,0]
v_cvt_pk_bf16_f32 v104, v80, v81
v_pk_fma_f32 v[82:83], v[154:155], v[170:171], v[82:83] op_sel_hi:[1,0,1]
v_pk_mul_f32 v[82:83], v[82:83], v[174:175] op_sel_hi:[1,0]
v_cvt_pk_bf16_f32 v105, v82, v83
v_pk_fma_f32 v[84:85], v[156:157], v[170:171], v[84:85] op_sel_hi:[1,0,1]
v_pk_mul_f32 v[84:85], v[84:85], v[174:175] op_sel_hi:[1,0]
v_cvt_pk_bf16_f32 v106, v84, v85
v_pk_fma_f32 v[86:87], v[158:159], v[170:171], v[86:87] op_sel_hi:[1,0,1]
v_pk_mul_f32 v[86:87], v[86:87], v[174:175] op_sel_hi:[1,0]
v_cvt_pk_bf16_f32 v107, v86, v87
v_permlane32_swap_b32 v100, v102
v_permlane32_swap_b32 v101, v103
global_store_dwordx4 v129, v[100:103], s[40:41] sc1
v_pk_fma_f32 v[88:89], v[160:161], v[170:171], v[88:89] op_sel_hi:[1,0,1]
v_pk_mul_f32 v[88:89], v[88:89], v[174:175] op_sel_hi:[1,0]
v_cvt_pk_bf16_f32 v108, v88, v89
v_pk_fma_f32 v[90:91], v[162:163], v[170:171], v[90:91] op_sel_hi:[1,0,1]
v_pk_mul_f32 v[90:91], v[90:91], v[174:175] op_sel_hi:[1,0]
v_cvt_pk_bf16_f32 v109, v90, v91
v_pk_fma_f32 v[92:93], v[164:165], v[170:171], v[92:93] op_sel_hi:[1,0,1]
v_pk_mul_f32 v[92:93], v[92:93], v[174:175] op_sel_hi:[1,0]
v_cvt_pk_bf16_f32 v110, v92, v93
v_pk_fma_f32 v[94:95], v[166:167], v[170:171], v[94:95] op_sel_hi:[1,0,1]
v_pk_mul_f32 v[94:95], v[94:95], v[174:175] op_sel_hi:[1,0]
v_cvt_pk_bf16_f32 v111, v94, v95
v_permlane32_swap_b32 v104, v106
v_permlane32_swap_b32 v105, v107
global_store_dwordx4 v130, v[104:107], s[40:41] sc1
v_permlane32_swap_b32 v108, v110
v_permlane32_swap_b32 v109, v111
global_store_dwordx4 v131, v[108:111], s[40:41] sc1
s_branch Lend_3_%=
s_nop 2
Lvt_27_%=:
; ---- V column tiles (columns on the lanes: V^T fragments)
s_mov_b32 s46, 0x290a00
s_mov_b32 s48, 0x1600
s_lshl_b32 s35, s31, 2
s_lshl_b32 s36, s33, 1
s_add_u32 s35, s35, s36
s_add_u32 s36, s35, s34
s_mul_i32 s36, s36, s48
s_add_u32 s36, s36, s46
s_waitcnt lgkmcnt(0)
s_add_u32 s38, s20, s36
s_addc_u32 s39, s21, 0
s_xor_b32 s36, s34, 1
s_add_u32 s36, s35, s36
s_mul_i32 s36, s36, s48
s_add_u32 s36, s36, s46
s_add_u32 s40, s20, s36
s_addc_u32 s41, s21, 0
s_sub_u32 s37, s30, 10
s_lshl_b32 s37, s37, 7
s_lshl_b32 s35, s34, 6
s_add_u32 s37, s37, s35
s_mov_b32 s48, 0x58000
v_mul_u32_u24 v2, 0x180, v18
s_xor_b32 s35, s33, 0
s_lshl_b32 s35, s35, 5
s_add_u32 s35, s35, s37
v_add_u32 v3, s35, v19
v_mul_u32_u24 v4, 0xccd, v3
v_lshrrev_b32 v4, 16, v4
v_mul_u32_u24 v5, 20, v4
v_sub_u32 v3, v3, v5
v_lshl_add_u32 v3, v3, 4, v2
v_and_b32 v5, 1, v4
v_mul_u32_u24 v5, 0x600, v5
v_add_u32 v3, v3, v5
v_lshrrev_b32 v4, 1, v4
v_mad_u32_u24 v128, v4, s48, v3
s_xor_b32 s35, s33, 1
s_lshl_b32 s35, s35, 5
s_add_u32 s35, s35, s37
v_add_u32 v3, s35, v19
v_mul_u32_u24 v4, 0xccd, v3
v_lshrrev_b32 v4, 16, v4
v_mul_u32_u24 v5, 20, v4
v_sub_u32 v3, v3, v5
v_lshl_add_u32 v3, v3, 4, v2
v_and_b32 v5, 1, v4
v_mul_u32_u24 v5, 0x600, v5
v_add_u32 v3, v3, v5
v_lshrrev_b32 v4, 1, v4
v_mad_u32_u24 v129, v4, s48, v3
s_barrier
ds_read_b128 v[96:99], v8
ds_read_b64_tr_b16 v[104:105], v16 offset:0
ds_read_b64_tr_b16 v[106:107], v16 offset:1024
ds_read_b64_tr_b16 v[108:109], v17 offset:0
ds_read_b64_tr_b16 v[110:111], v17 offset:1024
ds_read_b128 v[100:103], v12
s_waitcnt lgkmcnt(0)
v_mfma_f32_32x32x16_bf16 v[32:47], v[96:99], v[104:107], 0
ds_read_b128 v[112:115], v9
)ASM"
R"ASM(ds_read_b64_tr_b16 v[120:121], v16 offset:4096
v_lshlrev_b32 v30, 16, v96
v_and_b32 v31, 0xffff0000, v96
v_fmac_f32 v28, v30, v30
v_fmac_f32 v29, v31, v31
v_mfma_f32_32x32x16_bf16 v[48:63], v[96:99], v[108:111], 0
ds_read_b64_tr_b16 v[122:123], v16 offset:5120
ds_read_b64_tr_b16 v[124:125], v17 offset:4096
v_lshlrev_b32 v30, 16, v97
v_and_b32 v31, 0xffff0000, v97
v_fmac_f32 v28, v30, v30
v_fmac_f32 v29, v31, v31
v_mfma_f32_16x16x32_bf16 v[20:23], v[208:211], v[96:99], 0
v_mfma_f32_32x32x16_bf16 v[64:79], v[100:103], v[104:107], 0
ds_read_b64_tr_b16 v[126:127], v17 offset:5120
ds_read_b128 v[116:119], v13
v_lshlrev_b32 v30, 16, v98
v_and_b32 v31, 0xffff0000, v98
v_fmac_f32 v28, v30, v30
v_fmac_f32 v29, v31, v31
v_mfma_f32_32x32x16_bf16 v[80:95], v[100:103], v[108:111], 0
v_lshlrev_b32 v30, 16, v99
v_and_b32 v31, 0xffff0000, v99
v_fmac_f32 v28, v30, v30
v_fmac_f32 v29, v31, v31
v_mfma_f32_16x16x32_bf16 v[24:27], v[208:211], v[104:107], 0
s_waitcnt lgkmcnt(0)
v_mfma_f32_32x32x16_bf16 v[32:47], v[112:115], v[120:123], v[32:47]
ds_read_b128 v[96:99], v10
ds_read_b64_tr_b16 v[104:105], v16 offset:8192
v_lshlrev_b32 v30, 16, v112
v_and_b32 v31, 0xffff0000, v112
v_fmac_f32 v28, v30, v30
v_fmac_f32 v29, v31, v31
v_mfma_f32_32x32x16_bf16 v[48:63], v[112:115], v[124:127], v[48:63]
ds_read_b64_tr_b16 v[106:107], v16 offset:9216
ds_read_b64_tr_b16 v[108:109], v17 offset:8192
v_lshlrev_b32 v30, 16, v113
v_and_b32 v31, 0xffff0000, v113
v_fmac_f32 v28, v30, v30
v_fmac_f32 v29, v31, v31
v_mfma_f32_16x16x32_bf16 v[20:23], v[208:211], v[112:115], v[20:23]
v_mfma_f32_32x32x16_bf16 v[64:79], v[116:119], v[120:123], v[64:79]
ds_read_b64_tr_b16 v[110:111], v17 offset:9216
ds_read_b128 v[100:103], v14
v_lshlrev_b32 v30, 16, v114
v_and_b32 v31, 0xffff0000, v114
v_fmac_f32 v28, v30, v30
v_fmac_f32 v29, v31, v31
v_mfma_f32_32x32x16_bf16 v[80:95], v[116:119], v[124:127], v[80:95]
v_lshlrev_b32 v30, 16, v115
v_and_b32 v31, 0xffff0000, v115
v_fmac_f32 v28, v30, v30
v_fmac_f32 v29, v31, v31
v_mfma_f32_16x16x32_bf16 v[24:27], v[208:211], v[120:123], v[24:27]
s_waitcnt lgkmcnt(0)
v_mfma_f32_32x32x16_bf16 v[32:47], v[96:99], v[104:107], v[32:47]
ds_read_b128 v[112:115], v11
ds_read_b64_tr_b16 v[120:121], v16 offset:12288
v_lshlrev_b32 v30, 16, v96
v_and_b32 v31, 0xffff0000, v96
v_fmac_f32 v28, v30, v30
v_fmac_f32 v29, v31, v31
v_mfma_f32_32x32x16_bf16 v[48:63], v[96:99], v[108:111], v[48:63]
ds_read_b64_tr_b16 v[122:123], v16 offset:13312
ds_read_b64_tr_b16 v[124:125], v17 offset:12288
v_lshlrev_b32 v30, 16, v97
v_and_b32 v31, 0xffff0000, v97
v_fmac_f32 v28, v30, v30
v_fmac_f32 v29, v31, v31
v_mfma_f32_16x16x32_bf16 v[20:23], v[208:211], v[96:99], v[20:23]
v_mfma_f32_32x32x16_bf16 v[64:79], v[100:103], v[104:107], v[64:79]
ds_read_b64_tr_b16 v[126:127], v17 offset:13312
ds_read_b128 v[116:119], v15
v_lshlrev_b32 v30, 16, v98
v_and_b32 v31, 0xffff0000, v98
v_fmac_f32 v28, v30, v30
v_fmac_f32 v29, v31, v31
v_mfma_f32_32x32x16_bf16 v[80:95], v[100:103], v[108:111], v[80:95]
v_lshlrev_b32 v30, 16, v99
v_and_b32 v31, 0xffff0000, v99
v_fmac_f32 v28, v30, v30
v_fmac_f32 v29, v31, v31
v_mfma_f32_16x16x32_bf16 v[24:27], v[208:211], v[104:107], v[24:27]
s_waitcnt lgkmcnt(0)
s_barrier
v_add_u32 v8, 0x8000, v8
v_add_u32 v16, 0x8000, v16
v_add_u32 v17, 0x8000, v17
v_add_u32 v12, 0x8000, v12
v_mfma_f32_32x32x16_bf16 v[32:47], v[112:115], v[120:123], v[32:47]
ds_read_b128 v[96:99], v8
ds_read_b64_tr_b16 v[104:105], v16 offset:0
v_lshlrev_b32 v30, 16, v112
v_and_b32 v31, 0xffff0000, v112
v_fmac_f32 v28, v30, v30
v_fmac_f32 v29, v31, v31
v_mfma_f32_32x32x16_bf16 v[48:63], v[112:115], v[124:127], v[48:63]
ds_read_b64_tr_b16 v[106:107], v16 offset:1024
ds_read_b64_tr_b16 v[108:109], v17 offset:0
v_lshlrev_b32 v30, 16, v113
v_and_b32 v31, 0xffff0000, v113
v_fmac_f32 v28, v30, v30
v_fmac_f32 v29, v31, v31
v_mfma_f32_16x16x32_bf16 v[20:23], v[208:211], v[112:115], v[20:23]
v_mfma_f32_32x32x16_bf16 v[64:79], v[116:119], v[120:123], v[64:79]
ds_read_b64_tr_b16 v[110:111], v17 offset:1024
ds_read_b128 v[100:103], v12
v_lshlrev_b32 v30, 16, v114
v_and_b32 v31, 0xffff0000, v114
v_fmac_f32 v28, v30, v30
v_fmac_f32 v29, v31, v31
v_mfma_f32_32x32x16_bf16 v[80:95], v[116:119], v[124:127], v[80:95]
v_lshlrev_b32 v30, 16, v115
v_and_b32 v31, 0xffff0000, v115
v_fmac_f32 v28, v30, v30
v_fmac_f32 v29, v31, v31
v_mfma_f32_16x16x32_bf16 v[24:27], v[208:211], v[120:123], v[24:27]
s_waitcnt lgkmcnt(0)
v_add_u32 v9, 0x8000, v9
v_add_u32 v13, 0x8000, v13
v_mfma_f32_32x32x16_bf16 v[32:47], v[96:99], v[104:107], v[32:47]
ds_read_b128 v[112:115], v9
ds_read_b64_tr_b16 v[120:121], v16 offset:4096
v_lshlrev_b32 v30, 16, v96
v_and_b32 v31, 0xffff0000, v96
v_fmac_f32 v28, v30, v30
v_fmac_f32 v29, v31, v31
v_mfma_f32_32x32x16_bf16 v[48:63], v[96:99], v[108:111], v[48:63]
ds_read_b64_tr_b16 v[122:123], v16 offset:5120
ds_read_b64_tr_b16 v[124:125], v17 offset:4096
v_lshlrev_b32 v30, 16, v97
v_and_b32 v31, 0xffff0000, v97
v_fmac_f32 v28, v30, v30
v_fmac_f32 v29, v31, v31
v_mfma_f32_16x16x32_bf16 v[20:23], v[208:211], v[96:99], v[20:23]
v_mfma_f32_32x32x16_bf16 v[64:79], v[100:103], v[104:107], v[64:79]
ds_read_b64_tr_b16 v[126:127], v17 offset:5120
ds_read_b128 v[116:119], v13
v_lshlrev_b32 v30, 16, v98
v_and_b32 v31, 0xffff0000, v98
v_fmac_f32 v28, v30, v30
v_fmac_f32 v29, v31, v31
v_mfma_f32_32x32x16_bf16 v[80:95], v[100:103], v[108:111], v[80:95]
v_lshlrev_b32 v30, 16, v99
v_and_b32 v31, 0xffff0000, v99
v_fmac_f32 v28, v30, v30
v_fmac_f32 v29, v31, v31
v_mfma_f32_16x16x32_bf16 v[24:27], v[208:211], v[104:107], v[24:27]
s_waitcnt lgkmcnt(0)
v_add_u32 v10, 0x8000, v10
v_add_u32 v14, 0x8000, v14
v_mfma_f32_32x32x16_bf16 v[32:47], v[112:115], v[120:123], v[32:47]
ds_read_b128 v[96:99], v10
ds_read_b64_tr_b16 v[104:105], v16 offset:8192
v_lshlrev_b32 v30, 16, v112
v_and_b32 v31, 0xffff0000, v112
v_fmac_f32 v28, v30, v30
v_fmac_f32 v29, v31, v31
v_mfma_f32_32x32x16_bf16 v[48:63], v[112:115], v[124:127], v[48:63]
ds_read_b64_tr_b16 v[106:107], v16 offset:9216
ds_read_b64_tr_b16 v[108:109], v17 offset:8192
v_lshlrev_b32 v30, 16, v113
v_and_b32 v31, 0xffff0000, v113
v_fmac_f32 v28, v30, v30
v_fmac_f32 v29, v31, v31
v_mfma_f32_16x16x32_bf16 v[20:23], v[208:211], v[112:115], v[20:23]
v_mfma_f32_32x32x16_bf16 v[64:79], v[116:119], v[120:123], v[64:79]
ds_read_b64_tr_b16 v[110:111], v17 offset:9216
ds_read_b128 v[100:103], v14
v_lshlrev_b32 v30, 16, v114
v_and_b32 v31, 0xffff0000, v114
v_fmac_f32 v28, v30, v30
v_fmac_f32 v29, v31, v31
v_mfma_f32_32x32x16_bf16 v[80:95], v[116:119], v[124:127], v[80:95]
v_lshlrev_b32 v30, 16, v115
v_and_b32 v31, 0xffff0000, v115
v_fmac_f32 v28, v30, v30
v_fmac_f32 v29, v31, v31
v_mfma_f32_16x16x32_bf16 v[24:27], v[208:211], v[120:123], v[24:27]
s_waitcnt lgkmcnt(0)
v_add_u32 v11, 0x8000, v11
v_add_u32 v15, 0x8000, v15
v_mfma_f32_32x32x16_bf16 v[32:47], v[96:99], v[104:107], v[32:47]
ds_read_b128 v[112:115], v11
ds_read_b64_tr_b16 v[120:121], v16 offset:12288
v_lshlrev_b32 v30, 16, v96
v_and_b32 v31, 0xffff0000, v96
v_fmac_f32 v28, v30, v30
v_fmac_f32 v29, v31, v31
v_mfma_f32_32x32x16_bf16 v[48:63], v[96:99], v[108:111], v[48:63]
ds_read_b64_tr_b16 v[122:123], v16 offset:13312
ds_read_b64_tr_b16 v[124:125], v17 offset:12288
v_lshlrev_b32 v30, 16, v97
v_and_b32 v31, 0xffff0000, v97
v_fmac_f32 v28, v30, v30
v_fmac_f32 v29, v31, v31
v_mfma_f32_16x16x32_bf16 v[20:23], v[208:211], v[96:99], v[20:23]
v_mfma_f32_32x32x16_bf16 v[64:79], v[100:103], v[104:107], v[64:79]
ds_read_b64_tr_b16 v[126:127], v17 offset:13312
)ASM"
R"ASM(ds_read_b128 v[116:119], v15
v_lshlrev_b32 v30, 16, v98
v_and_b32 v31, 0xffff0000, v98
v_fmac_f32 v28, v30, v30
v_fmac_f32 v29, v31, v31
v_mfma_f32_32x32x16_bf16 v[80:95], v[100:103], v[108:111], v[80:95]
v_lshlrev_b32 v30, 16, v99
v_and_b32 v31, 0xffff0000, v99
v_fmac_f32 v28, v30, v30
v_fmac_f32 v29, v31, v31
v_mfma_f32_16x16x32_bf16 v[24:27], v[208:211], v[104:107], v[24:27]
s_waitcnt lgkmcnt(0)
s_barrier
v_add_u32 v8, 0x8000, v8
v_add_u32 v16, 0x8000, v16
v_add_u32 v17, 0x8000, v17
v_add_u32 v12, 0x8000, v12
v_mfma_f32_32x32x16_bf16 v[32:47], v[112:115], v[120:123], v[32:47]
ds_read_b128 v[96:99], v8
ds_read_b64_tr_b16 v[104:105], v16 offset:0
v_lshlrev_b32 v30, 16, v112
v_and_b32 v31, 0xffff0000, v112
v_fmac_f32 v28, v30, v30
v_fmac_f32 v29, v31, v31
v_mfma_f32_32x32x16_bf16 v[48:63], v[112:115], v[124:127], v[48:63]
ds_read_b64_tr_b16 v[106:107], v16 offset:1024
ds_read_b64_tr_b16 v[108:109], v17 offset:0
v_lshlrev_b32 v30, 16, v113
v_and_b32 v31, 0xffff0000, v113
v_fmac_f32 v28, v30, v30
v_fmac_f32 v29, v31, v31
v_mfma_f32_16x16x32_bf16 v[20:23], v[208:211], v[112:115], v[20:23]
v_mfma_f32_32x32x16_bf16 v[64:79], v[116:119], v[120:123], v[64:79]
ds_read_b64_tr_b16 v[110:111], v17 offset:1024
ds_read_b128 v[100:103], v12
v_lshlrev_b32 v30, 16, v114
v_and_b32 v31, 0xffff0000, v114
v_fmac_f32 v28, v30, v30
v_fmac_f32 v29, v31, v31
v_mfma_f32_32x32x16_bf16 v[80:95], v[116:119], v[124:127], v[80:95]
v_lshlrev_b32 v30, 16, v115
v_and_b32 v31, 0xffff0000, v115
v_fmac_f32 v28, v30, v30
v_fmac_f32 v29, v31, v31
v_mfma_f32_16x16x32_bf16 v[24:27], v[208:211], v[120:123], v[24:27]
s_waitcnt lgkmcnt(0)
v_add_u32 v9, 0x8000, v9
v_add_u32 v13, 0x8000, v13
v_mfma_f32_32x32x16_bf16 v[32:47], v[96:99], v[104:107], v[32:47]
ds_read_b128 v[112:115], v9
ds_read_b64_tr_b16 v[120:121], v16 offset:4096
v_lshlrev_b32 v30, 16, v96
v_and_b32 v31, 0xffff0000, v96
v_fmac_f32 v28, v30, v30
v_fmac_f32 v29, v31, v31
v_mfma_f32_32x32x16_bf16 v[48:63], v[96:99], v[108:111], v[48:63]
ds_read_b64_tr_b16 v[122:123], v16 offset:5120
ds_read_b64_tr_b16 v[124:125], v17 offset:4096
v_lshlrev_b32 v30, 16, v97
v_and_b32 v31, 0xffff0000, v97
v_fmac_f32 v28, v30, v30
v_fmac_f32 v29, v31, v31
v_mfma_f32_16x16x32_bf16 v[20:23], v[208:211], v[96:99], v[20:23]
v_mfma_f32_32x32x16_bf16 v[64:79], v[100:103], v[104:107], v[64:79]
ds_read_b64_tr_b16 v[126:127], v17 offset:5120
ds_read_b128 v[116:119], v13
v_lshlrev_b32 v30, 16, v98
v_and_b32 v31, 0xffff0000, v98
v_fmac_f32 v28, v30, v30
v_fmac_f32 v29, v31, v31
v_mfma_f32_32x32x16_bf16 v[80:95], v[100:103], v[108:111], v[80:95]
v_lshlrev_b32 v30, 16, v99
v_and_b32 v31, 0xffff0000, v99
v_fmac_f32 v28, v30, v30
v_fmac_f32 v29, v31, v31
v_mfma_f32_16x16x32_bf16 v[24:27], v[208:211], v[104:107], v[24:27]
s_waitcnt lgkmcnt(0)
v_add_u32 v10, 0x8000, v10
v_add_u32 v14, 0x8000, v14
v_mfma_f32_32x32x16_bf16 v[32:47], v[112:115], v[120:123], v[32:47]
ds_read_b128 v[96:99], v10
ds_read_b64_tr_b16 v[104:105], v16 offset:8192
v_lshlrev_b32 v30, 16, v112
v_and_b32 v31, 0xffff0000, v112
v_fmac_f32 v28, v30, v30
v_fmac_f32 v29, v31, v31
v_mfma_f32_32x32x16_bf16 v[48:63], v[112:115], v[124:127], v[48:63]
ds_read_b64_tr_b16 v[106:107], v16 offset:9216
ds_read_b64_tr_b16 v[108:109], v17 offset:8192
v_lshlrev_b32 v30, 16, v113
v_and_b32 v31, 0xffff0000, v113
v_fmac_f32 v28, v30, v30
v_fmac_f32 v29, v31, v31
v_mfma_f32_16x16x32_bf16 v[20:23], v[208:211], v[112:115], v[20:23]
v_mfma_f32_32x32x16_bf16 v[64:79], v[116:119], v[120:123], v[64:79]
ds_read_b64_tr_b16 v[110:111], v17 offset:9216
ds_read_b128 v[100:103], v14
v_lshlrev_b32 v30, 16, v114
v_and_b32 v31, 0xffff0000, v114
v_fmac_f32 v28, v30, v30
v_fmac_f32 v29, v31, v31
v_mfma_f32_32x32x16_bf16 v[80:95], v[116:119], v[124:127], v[80:95]
v_lshlrev_b32 v30, 16, v115
v_and_b32 v31, 0xffff0000, v115
v_fmac_f32 v28, v30, v30
v_fmac_f32 v29, v31, v31
v_mfma_f32_16x16x32_bf16 v[24:27], v[208:211], v[120:123], v[24:27]
s_waitcnt lgkmcnt(0)
v_add_u32 v11, 0x8000, v11
v_add_u32 v15, 0x8000, v15
v_mfma_f32_32x32x16_bf16 v[32:47], v[96:99], v[104:107], v[32:47]
ds_read_b128 v[112:115], v11
ds_read_b64_tr_b16 v[120:121], v16 offset:12288
v_lshlrev_b32 v30, 16, v96
v_and_b32 v31, 0xffff0000, v96
v_fmac_f32 v28, v30, v30
v_fmac_f32 v29, v31, v31
v_mfma_f32_32x32x16_bf16 v[48:63], v[96:99], v[108:111], v[48:63]
ds_read_b64_tr_b16 v[122:123], v16 offset:13312
ds_read_b64_tr_b16 v[124:125], v17 offset:12288
v_lshlrev_b32 v30, 16, v97
v_and_b32 v31, 0xffff0000, v97
v_fmac_f32 v28, v30, v30
v_fmac_f32 v29, v31, v31
v_mfma_f32_16x16x32_bf16 v[20:23], v[208:211], v[96:99], v[20:23]
v_mfma_f32_32x32x16_bf16 v[64:79], v[100:103], v[104:107], v[64:79]
ds_read_b64_tr_b16 v[126:127], v17 offset:13312
ds_read_b128 v[116:119], v15
v_lshlrev_b32 v30, 16, v98
v_and_b32 v31, 0xffff0000, v98
v_fmac_f32 v28, v30, v30
v_fmac_f32 v29, v31, v31
v_mfma_f32_32x32x16_bf16 v[80:95], v[100:103], v[108:111], v[80:95]
v_lshlrev_b32 v30, 16, v99
v_and_b32 v31, 0xffff0000, v99
v_fmac_f32 v28, v30, v30
v_fmac_f32 v29, v31, v31
v_mfma_f32_16x16x32_bf16 v[24:27], v[208:211], v[104:107], v[24:27]
s_waitcnt lgkmcnt(0)
s_barrier
v_add_u32 v8, 0x8000, v8
v_add_u32 v16, 0x8000, v16
v_add_u32 v17, 0x8000, v17
v_add_u32 v12, 0x8000, v12
v_mfma_f32_32x32x16_bf16 v[32:47], v[112:115], v[120:123], v[32:47]
ds_read_b128 v[96:99], v8
ds_read_b64_tr_b16 v[104:105], v16 offset:0
v_lshlrev_b32 v30, 16, v112
v_and_b32 v31, 0xffff0000, v112
v_fmac_f32 v28, v30, v30
v_fmac_f32 v29, v31, v31
v_mfma_f32_32x32x16_bf16 v[48:63], v[112:115], v[124:127], v[48:63]
ds_read_b64_tr_b16 v[106:107], v16 offset:1024
ds_read_b64_tr_b16 v[108:109], v17 offset:0
v_lshlrev_b32 v30, 16, v113
v_and_b32 v31, 0xffff0000, v113
v_fmac_f32 v28, v30, v30
v_fmac_f32 v29, v31, v31
v_mfma_f32_16x16x32_bf16 v[20:23], v[208:211], v[112:115], v[20:23]
v_mfma_f32_32x32x16_bf16 v[64:79], v[116:119], v[120:123], v[64:79]
ds_read_b64_tr_b16 v[110:111], v17 offset:1024
ds_read_b128 v[100:103], v12
v_lshlrev_b32 v30, 16, v114
v_and_b32 v31, 0xffff0000, v114
v_fmac_f32 v28, v30, v30
v_fmac_f32 v29, v31, v31
v_mfma_f32_32x32x16_bf16 v[80:95], v[116:119], v[124:127], v[80:95]
v_lshlrev_b32 v30, 16, v115
v_and_b32 v31, 0xffff0000, v115
v_fmac_f32 v28, v30, v30
v_fmac_f32 v29, v31, v31
v_mfma_f32_16x16x32_bf16 v[24:27], v[208:211], v[120:123], v[24:27]
s_waitcnt lgkmcnt(0)
v_add_u32 v9, 0x8000, v9
v_add_u32 v13, 0x8000, v13
v_mfma_f32_32x32x16_bf16 v[32:47], v[96:99], v[104:107], v[32:47]
ds_read_b128 v[112:115], v9
ds_read_b64_tr_b16 v[120:121], v16 offset:4096
v_lshlrev_b32 v30, 16, v96
v_and_b32 v31, 0xffff0000, v96
v_fmac_f32 v28, v30, v30
v_fmac_f32 v29, v31, v31
v_mfma_f32_32x32x16_bf16 v[48:63], v[96:99], v[108:111], v[48:63]
ds_read_b64_tr_b16 v[122:123], v16 offset:5120
ds_read_b64_tr_b16 v[124:125], v17 offset:4096
v_lshlrev_b32 v30, 16, v97
v_and_b32 v31, 0xffff0000, v97
v_fmac_f32 v28, v30, v30
v_fmac_f32 v29, v31, v31
v_mfma_f32_16x16x32_bf16 v[20:23], v[208:211], v[96:99], v[20:23]
v_mfma_f32_32x32x16_bf16 v[64:79], v[100:103], v[104:107], v[64:79]
ds_read_b64_tr_b16 v[126:127], v17 offset:5120
ds_read_b128 v[116:119], v13
v_lshlrev_b32 v30, 16, v98
v_and_b32 v31, 0xffff0000, v98
v_fmac_f32 v28, v30, v30
v_fmac_f32 v29, v31, v31
v_mfma_f32_32x32x16_bf16 v[80:95], v[100:103], v[108:111], v[80:95]
v_lshlrev_b32 v30, 16, v99
v_and_b32 v31, 0xffff0000, v99
)ASM"
R"ASM(v_fmac_f32 v28, v30, v30
v_fmac_f32 v29, v31, v31
v_mfma_f32_16x16x32_bf16 v[24:27], v[208:211], v[104:107], v[24:27]
s_waitcnt lgkmcnt(0)
v_add_u32 v10, 0x8000, v10
v_add_u32 v14, 0x8000, v14
v_mfma_f32_32x32x16_bf16 v[32:47], v[112:115], v[120:123], v[32:47]
ds_read_b128 v[96:99], v10
ds_read_b64_tr_b16 v[104:105], v16 offset:8192
v_lshlrev_b32 v30, 16, v112
v_and_b32 v31, 0xffff0000, v112
v_fmac_f32 v28, v30, v30
v_fmac_f32 v29, v31, v31
v_mfma_f32_32x32x16_bf16 v[48:63], v[112:115], v[124:127], v[48:63]
ds_read_b64_tr_b16 v[106:107], v16 offset:9216
ds_read_b64_tr_b16 v[108:109], v17 offset:8192
v_lshlrev_b32 v30, 16, v113
v_and_b32 v31, 0xffff0000, v113
v_fmac_f32 v28, v30, v30
v_fmac_f32 v29, v31, v31
v_mfma_f32_16x16x32_bf16 v[20:23], v[208:211], v[112:115], v[20:23]
v_mfma_f32_32x32x16_bf16 v[64:79], v[116:119], v[120:123], v[64:79]
ds_read_b64_tr_b16 v[110:111], v17 offset:9216
ds_read_b128 v[100:103], v14
v_lshlrev_b32 v30, 16, v114
v_and_b32 v31, 0xffff0000, v114
v_fmac_f32 v28, v30, v30
v_fmac_f32 v29, v31, v31
v_mfma_f32_32x32x16_bf16 v[80:95], v[116:119], v[124:127], v[80:95]
v_lshlrev_b32 v30, 16, v115
v_and_b32 v31, 0xffff0000, v115
v_fmac_f32 v28, v30, v30
v_fmac_f32 v29, v31, v31
v_mfma_f32_16x16x32_bf16 v[24:27], v[208:211], v[120:123], v[24:27]
s_waitcnt lgkmcnt(0)
v_add_u32 v11, 0x8000, v11
v_add_u32 v15, 0x8000, v15
v_mfma_f32_32x32x16_bf16 v[32:47], v[96:99], v[104:107], v[32:47]
ds_read_b128 v[112:115], v11
ds_read_b64_tr_b16 v[120:121], v16 offset:12288
v_lshlrev_b32 v30, 16, v96
v_and_b32 v31, 0xffff0000, v96
v_fmac_f32 v28, v30, v30
v_fmac_f32 v29, v31, v31
v_mfma_f32_32x32x16_bf16 v[48:63], v[96:99], v[108:111], v[48:63]
ds_read_b64_tr_b16 v[122:123], v16 offset:13312
ds_read_b64_tr_b16 v[124:125], v17 offset:12288
v_lshlrev_b32 v30, 16, v97
v_and_b32 v31, 0xffff0000, v97
v_fmac_f32 v28, v30, v30
v_fmac_f32 v29, v31, v31
v_mfma_f32_16x16x32_bf16 v[20:23], v[208:211], v[96:99], v[20:23]
v_mfma_f32_32x32x16_bf16 v[64:79], v[100:103], v[104:107], v[64:79]
ds_read_b64_tr_b16 v[126:127], v17 offset:13312
ds_read_b128 v[116:119], v15
v_lshlrev_b32 v30, 16, v98
v_and_b32 v31, 0xffff0000, v98
v_fmac_f32 v28, v30, v30
v_fmac_f32 v29, v31, v31
v_mfma_f32_32x32x16_bf16 v[80:95], v[100:103], v[108:111], v[80:95]
v_lshlrev_b32 v30, 16, v99
v_and_b32 v31, 0xffff0000, v99
v_fmac_f32 v28, v30, v30
v_fmac_f32 v29, v31, v31
v_mfma_f32_16x16x32_bf16 v[24:27], v[208:211], v[104:107], v[24:27]
s_waitcnt lgkmcnt(0)
s_barrier
v_add_u32 v8, 0x8000, v8
v_add_u32 v16, 0x8000, v16
v_add_u32 v17, 0x8000, v17
v_add_u32 v12, 0x8000, v12
v_mfma_f32_32x32x16_bf16 v[32:47], v[112:115], v[120:123], v[32:47]
ds_read_b128 v[96:99], v8
ds_read_b64_tr_b16 v[104:105], v16 offset:0
v_lshlrev_b32 v30, 16, v112
v_and_b32 v31, 0xffff0000, v112
v_fmac_f32 v28, v30, v30
v_fmac_f32 v29, v31, v31
v_mfma_f32_32x32x16_bf16 v[48:63], v[112:115], v[124:127], v[48:63]
ds_read_b64_tr_b16 v[106:107], v16 offset:1024
ds_read_b64_tr_b16 v[108:109], v17 offset:0
v_lshlrev_b32 v30, 16, v113
v_and_b32 v31, 0xffff0000, v113
v_fmac_f32 v28, v30, v30
v_fmac_f32 v29, v31, v31
v_mfma_f32_16x16x32_bf16 v[20:23], v[208:211], v[112:115], v[20:23]
v_mfma_f32_32x32x16_bf16 v[64:79], v[116:119], v[120:123], v[64:79]
ds_read_b64_tr_b16 v[110:111], v17 offset:1024
ds_read_b128 v[100:103], v12
v_lshlrev_b32 v30, 16, v114
v_and_b32 v31, 0xffff0000, v114
v_fmac_f32 v28, v30, v30
v_fmac_f32 v29, v31, v31
v_mfma_f32_32x32x16_bf16 v[80:95], v[116:119], v[124:127], v[80:95]
v_lshlrev_b32 v30, 16, v115
v_and_b32 v31, 0xffff0000, v115
v_fmac_f32 v28, v30, v30
v_fmac_f32 v29, v31, v31
v_mfma_f32_16x16x32_bf16 v[24:27], v[208:211], v[120:123], v[24:27]
s_waitcnt lgkmcnt(0)
v_add_u32 v9, 0x8000, v9
v_add_u32 v13, 0x8000, v13
v_mfma_f32_32x32x16_bf16 v[32:47], v[96:99], v[104:107], v[32:47]
ds_read_b128 v[112:115], v9
ds_read_b64_tr_b16 v[120:121], v16 offset:4096
v_lshlrev_b32 v30, 16, v96
v_and_b32 v31, 0xffff0000, v96
v_fmac_f32 v28, v30, v30
v_fmac_f32 v29, v31, v31
v_mfma_f32_32x32x16_bf16 v[48:63], v[96:99], v[108:111], v[48:63]
ds_read_b64_tr_b16 v[122:123], v16 offset:5120
ds_read_b64_tr_b16 v[124:125], v17 offset:4096
v_lshlrev_b32 v30, 16, v97
v_and_b32 v31, 0xffff0000, v97
v_fmac_f32 v28, v30, v30
v_fmac_f32 v29, v31, v31
v_mfma_f32_16x16x32_bf16 v[20:23], v[208:211], v[96:99], v[20:23]
v_mfma_f32_32x32x16_bf16 v[64:79], v[100:103], v[104:107], v[64:79]
ds_read_b64_tr_b16 v[126:127], v17 offset:5120
ds_read_b128 v[116:119], v13
v_lshlrev_b32 v30, 16, v98
v_and_b32 v31, 0xffff0000, v98
v_fmac_f32 v28, v30, v30
v_fmac_f32 v29, v31, v31
v_mfma_f32_32x32x16_bf16 v[80:95], v[100:103], v[108:111], v[80:95]
v_lshlrev_b32 v30, 16, v99
v_and_b32 v31, 0xffff0000, v99
v_fmac_f32 v28, v30, v30
v_fmac_f32 v29, v31, v31
v_mfma_f32_16x16x32_bf16 v[24:27], v[208:211], v[104:107], v[24:27]
s_waitcnt lgkmcnt(0)
v_add_u32 v10, 0x8000, v10
v_add_u32 v14, 0x8000, v14
v_mfma_f32_32x32x16_bf16 v[32:47], v[112:115], v[120:123], v[32:47]
ds_read_b128 v[96:99], v10
ds_read_b64_tr_b16 v[104:105], v16 offset:8192
v_lshlrev_b32 v30, 16, v112
v_and_b32 v31, 0xffff0000, v112
v_fmac_f32 v28, v30, v30
v_fmac_f32 v29, v31, v31
v_mfma_f32_32x32x16_bf16 v[48:63], v[112:115], v[124:127], v[48:63]
ds_read_b64_tr_b16 v[106:107], v16 offset:9216
ds_read_b64_tr_b16 v[108:109], v17 offset:8192
v_lshlrev_b32 v30, 16, v113
v_and_b32 v31, 0xffff0000, v113
v_fmac_f32 v28, v30, v30
v_fmac_f32 v29, v31, v31
v_mfma_f32_16x16x32_bf16 v[20:23], v[208:211], v[112:115], v[20:23]
v_mfma_f32_32x32x16_bf16 v[64:79], v[116:119], v[120:123], v[64:79]
ds_read_b64_tr_b16 v[110:111], v17 offset:9216
ds_read_b128 v[100:103], v14
v_lshlrev_b32 v30, 16, v114
v_and_b32 v31, 0xffff0000, v114
v_fmac_f32 v28, v30, v30
v_fmac_f32 v29, v31, v31
v_mfma_f32_32x32x16_bf16 v[80:95], v[116:119], v[124:127], v[80:95]
v_lshlrev_b32 v30, 16, v115
v_and_b32 v31, 0xffff0000, v115
v_fmac_f32 v28, v30, v30
v_fmac_f32 v29, v31, v31
v_mfma_f32_16x16x32_bf16 v[24:27], v[208:211], v[120:123], v[24:27]
s_waitcnt lgkmcnt(0)
v_add_u32 v11, 0x8000, v11
v_add_u32 v15, 0x8000, v15
v_mfma_f32_32x32x16_bf16 v[32:47], v[96:99], v[104:107], v[32:47]
ds_read_b128 v[112:115], v11
ds_read_b64_tr_b16 v[120:121], v16 offset:12288
v_lshlrev_b32 v30, 16, v96
v_and_b32 v31, 0xffff0000, v96
v_fmac_f32 v28, v30, v30
v_fmac_f32 v29, v31, v31
v_mfma_f32_32x32x16_bf16 v[48:63], v[96:99], v[108:111], v[48:63]
ds_read_b64_tr_b16 v[122:123], v16 offset:13312
ds_read_b64_tr_b16 v[124:125], v17 offset:12288
v_lshlrev_b32 v30, 16, v97
v_and_b32 v31, 0xffff0000, v97
v_fmac_f32 v28, v30, v30
v_fmac_f32 v29, v31, v31
v_mfma_f32_16x16x32_bf16 v[20:23], v[208:211], v[96:99], v[20:23]
v_mfma_f32_32x32x16_bf16 v[64:79], v[100:103], v[104:107], v[64:79]
ds_read_b64_tr_b16 v[126:127], v17 offset:13312
ds_read_b128 v[116:119], v15
v_lshlrev_b32 v30, 16, v98
v_and_b32 v31, 0xffff0000, v98
v_fmac_f32 v28, v30, v30
v_fmac_f32 v29, v31, v31
v_mfma_f32_32x32x16_bf16 v[80:95], v[100:103], v[108:111], v[80:95]
v_lshlrev_b32 v30, 16, v99
v_and_b32 v31, 0xffff0000, v99
v_fmac_f32 v28, v30, v30
v_fmac_f32 v29, v31, v31
v_mfma_f32_16x16x32_bf16 v[24:27], v[208:211], v[104:107], v[24:27]
s_waitcnt lgkmcnt(0)
s_barrier
v_add_u32 v8, 0xfffe0000, v8
v_add_u32 v16, 0xfffe0000, v16
v_add_u32 v17, 0xfffe0000, v17
v_add_u32 v12, 0xfffe0000, v12
v_mfma_f32_32x32x16_bf16 v[32:47], v[112:115], v[120:123], v[32:47]
ds_read_b128 v[96:99], v8
)ASM"
R"ASM(ds_read_b64_tr_b16 v[104:105], v16 offset:0
v_lshlrev_b32 v30, 16, v112
v_and_b32 v31, 0xffff0000, v112
v_fmac_f32 v28, v30, v30
v_fmac_f32 v29, v31, v31
v_mfma_f32_32x32x16_bf16 v[48:63], v[112:115], v[124:127], v[48:63]
ds_read_b64_tr_b16 v[106:107], v16 offset:1024
ds_read_b64_tr_b16 v[108:109], v17 offset:0
v_lshlrev_b32 v30, 16, v113
v_and_b32 v31, 0xffff0000, v113
v_fmac_f32 v28, v30, v30
v_fmac_f32 v29, v31, v31
v_mfma_f32_16x16x32_bf16 v[20:23], v[208:211], v[112:115], v[20:23]
v_mfma_f32_32x32x16_bf16 v[64:79], v[116:119], v[120:123], v[64:79]
ds_read_b64_tr_b16 v[110:111], v17 offset:1024
ds_read_b128 v[100:103], v12
v_lshlrev_b32 v30, 16, v114
v_and_b32 v31, 0xffff0000, v114
v_fmac_f32 v28, v30, v30
v_fmac_f32 v29, v31, v31
v_mfma_f32_32x32x16_bf16 v[80:95], v[116:119], v[124:127], v[80:95]
v_lshlrev_b32 v30, 16, v115
v_and_b32 v31, 0xffff0000, v115
v_fmac_f32 v28, v30, v30
v_fmac_f32 v29, v31, v31
v_mfma_f32_16x16x32_bf16 v[24:27], v[208:211], v[120:123], v[24:27]
s_waitcnt lgkmcnt(0)
v_add_u32 v9, 0xfffe0000, v9
v_add_u32 v13, 0xfffe0000, v13
v_mfma_f32_32x32x16_bf16 v[32:47], v[96:99], v[104:107], v[32:47]
ds_read_b128 v[112:115], v9
ds_read_b64_tr_b16 v[120:121], v16 offset:4096
v_lshlrev_b32 v30, 16, v96
v_and_b32 v31, 0xffff0000, v96
v_fmac_f32 v28, v30, v30
v_fmac_f32 v29, v31, v31
v_mfma_f32_32x32x16_bf16 v[48:63], v[96:99], v[108:111], v[48:63]
ds_read_b64_tr_b16 v[122:123], v16 offset:5120
ds_read_b64_tr_b16 v[124:125], v17 offset:4096
v_lshlrev_b32 v30, 16, v97
v_and_b32 v31, 0xffff0000, v97
v_fmac_f32 v28, v30, v30
v_fmac_f32 v29, v31, v31
v_mfma_f32_16x16x32_bf16 v[20:23], v[208:211], v[96:99], v[20:23]
v_mfma_f32_32x32x16_bf16 v[64:79], v[100:103], v[104:107], v[64:79]
ds_read_b64_tr_b16 v[126:127], v17 offset:5120
ds_read_b128 v[116:119], v13
v_lshlrev_b32 v30, 16, v98
v_and_b32 v31, 0xffff0000, v98
v_fmac_f32 v28, v30, v30
v_fmac_f32 v29, v31, v31
v_mfma_f32_32x32x16_bf16 v[80:95], v[100:103], v[108:111], v[80:95]
v_lshlrev_b32 v30, 16, v99
v_and_b32 v31, 0xffff0000, v99
v_fmac_f32 v28, v30, v30
v_fmac_f32 v29, v31, v31
v_mfma_f32_16x16x32_bf16 v[24:27], v[208:211], v[104:107], v[24:27]
s_waitcnt lgkmcnt(0)
v_add_u32 v10, 0xfffe0000, v10
v_add_u32 v14, 0xfffe0000, v14
v_mfma_f32_32x32x16_bf16 v[32:47], v[112:115], v[120:123], v[32:47]
ds_read_b128 v[96:99], v10
ds_read_b64_tr_b16 v[104:105], v16 offset:8192
v_lshlrev_b32 v30, 16, v112
v_and_b32 v31, 0xffff0000, v112
v_fmac_f32 v28, v30, v30
v_fmac_f32 v29, v31, v31
v_mfma_f32_32x32x16_bf16 v[48:63], v[112:115], v[124:127], v[48:63]
ds_read_b64_tr_b16 v[106:107], v16 offset:9216
ds_read_b64_tr_b16 v[108:109], v17 offset:8192
v_lshlrev_b32 v30, 16, v113
v_and_b32 v31, 0xffff0000, v113
v_fmac_f32 v28, v30, v30
v_fmac_f32 v29, v31, v31
v_mfma_f32_16x16x32_bf16 v[20:23], v[208:211], v[112:115], v[20:23]
v_mfma_f32_32x32x16_bf16 v[64:79], v[116:119], v[120:123], v[64:79]
ds_read_b64_tr_b16 v[110:111], v17 offset:9216
ds_read_b128 v[100:103], v14
v_lshlrev_b32 v30, 16, v114
v_and_b32 v31, 0xffff0000, v114
v_fmac_f32 v28, v30, v30
v_fmac_f32 v29, v31, v31
v_mfma_f32_32x32x16_bf16 v[80:95], v[116:119], v[124:127], v[80:95]
v_lshlrev_b32 v30, 16, v115
v_and_b32 v31, 0xffff0000, v115
v_fmac_f32 v28, v30, v30
v_fmac_f32 v29, v31, v31
v_mfma_f32_16x16x32_bf16 v[24:27], v[208:211], v[120:123], v[24:27]
s_waitcnt lgkmcnt(0)
v_add_u32 v11, 0xfffe0000, v11
v_add_u32 v15, 0xfffe0000, v15
v_mfma_f32_32x32x16_bf16 v[32:47], v[96:99], v[104:107], v[32:47]
ds_read_b128 v[112:115], v11
ds_read_b64_tr_b16 v[120:121], v16 offset:12288
v_lshlrev_b32 v30, 16, v96
v_and_b32 v31, 0xffff0000, v96
v_fmac_f32 v28, v30, v30
v_fmac_f32 v29, v31, v31
v_mfma_f32_32x32x16_bf16 v[48:63], v[96:99], v[108:111], v[48:63]
ds_read_b64_tr_b16 v[122:123], v16 offset:13312
ds_read_b64_tr_b16 v[124:125], v17 offset:12288
v_lshlrev_b32 v30, 16, v97
v_and_b32 v31, 0xffff0000, v97
v_fmac_f32 v28, v30, v30
v_fmac_f32 v29, v31, v31
v_mfma_f32_16x16x32_bf16 v[20:23], v[208:211], v[96:99], v[20:23]
v_mfma_f32_32x32x16_bf16 v[64:79], v[100:103], v[104:107], v[64:79]
ds_read_b64_tr_b16 v[126:127], v17 offset:13312
ds_read_b128 v[116:119], v15
v_lshlrev_b32 v30, 16, v98
v_and_b32 v31, 0xffff0000, v98
v_fmac_f32 v28, v30, v30
v_fmac_f32 v29, v31, v31
v_mfma_f32_32x32x16_bf16 v[80:95], v[100:103], v[108:111], v[80:95]
v_lshlrev_b32 v30, 16, v99
v_and_b32 v31, 0xffff0000, v99
v_fmac_f32 v28, v30, v30
v_fmac_f32 v29, v31, v31
v_mfma_f32_16x16x32_bf16 v[24:27], v[208:211], v[104:107], v[24:27]
s_waitcnt lgkmcnt(0)
s_barrier
v_add_u32 v8, 0x8000, v8
v_add_u32 v16, 0x8000, v16
v_add_u32 v17, 0x8000, v17
v_add_u32 v12, 0x8000, v12
v_mfma_f32_32x32x16_bf16 v[32:47], v[112:115], v[120:123], v[32:47]
ds_read_b128 v[96:99], v8
ds_read_b64_tr_b16 v[104:105], v16 offset:0
v_lshlrev_b32 v30, 16, v112
v_and_b32 v31, 0xffff0000, v112
v_fmac_f32 v28, v30, v30
v_fmac_f32 v29, v31, v31
v_mfma_f32_32x32x16_bf16 v[48:63], v[112:115], v[124:127], v[48:63]
ds_read_b64_tr_b16 v[106:107], v16 offset:1024
ds_read_b64_tr_b16 v[108:109], v17 offset:0
v_lshlrev_b32 v30, 16, v113
v_and_b32 v31, 0xffff0000, v113
v_fmac_f32 v28, v30, v30
v_fmac_f32 v29, v31, v31
v_mfma_f32_16x16x32_bf16 v[20:23], v[208:211], v[112:115], v[20:23]
v_mfma_f32_32x32x16_bf16 v[64:79], v[116:119], v[120:123], v[64:79]
ds_read_b64_tr_b16 v[110:111], v17 offset:1024
ds_read_b128 v[100:103], v12
v_lshlrev_b32 v30, 16, v114
v_and_b32 v31, 0xffff0000, v114
v_fmac_f32 v28, v30, v30
v_fmac_f32 v29, v31, v31
v_mfma_f32_32x32x16_bf16 v[80:95], v[116:119], v[124:127], v[80:95]
v_lshlrev_b32 v30, 16, v115
v_and_b32 v31, 0xffff0000, v115
v_fmac_f32 v28, v30, v30
v_fmac_f32 v29, v31, v31
v_mfma_f32_16x16x32_bf16 v[24:27], v[208:211], v[120:123], v[24:27]
s_waitcnt lgkmcnt(0)
v_add_u32 v9, 0x8000, v9
v_add_u32 v13, 0x8000, v13
v_mfma_f32_32x32x16_bf16 v[32:47], v[96:99], v[104:107], v[32:47]
ds_read_b128 v[112:115], v9
ds_read_b64_tr_b16 v[120:121], v16 offset:4096
v_lshlrev_b32 v30, 16, v96
v_and_b32 v31, 0xffff0000, v96
v_fmac_f32 v28, v30, v30
v_fmac_f32 v29, v31, v31
v_mfma_f32_32x32x16_bf16 v[48:63], v[96:99], v[108:111], v[48:63]
ds_read_b64_tr_b16 v[122:123], v16 offset:5120
ds_read_b64_tr_b16 v[124:125], v17 offset:4096
v_lshlrev_b32 v30, 16, v97
v_and_b32 v31, 0xffff0000, v97
v_fmac_f32 v28, v30, v30
v_fmac_f32 v29, v31, v31
v_mfma_f32_16x16x32_bf16 v[20:23], v[208:211], v[96:99], v[20:23]
v_mfma_f32_32x32x16_bf16 v[64:79], v[100:103], v[104:107], v[64:79]
ds_read_b64_tr_b16 v[126:127], v17 offset:5120
ds_read_b128 v[116:119], v13
v_lshlrev_b32 v30, 16, v98
v_and_b32 v31, 0xffff0000, v98
v_fmac_f32 v28, v30, v30
v_fmac_f32 v29, v31, v31
v_mfma_f32_32x32x16_bf16 v[80:95], v[100:103], v[108:111], v[80:95]
v_lshlrev_b32 v30, 16, v99
v_and_b32 v31, 0xffff0000, v99
v_fmac_f32 v28, v30, v30
v_fmac_f32 v29, v31, v31
v_mfma_f32_16x16x32_bf16 v[24:27], v[208:211], v[104:107], v[24:27]
s_waitcnt lgkmcnt(0)
v_add_u32 v10, 0x8000, v10
v_add_u32 v14, 0x8000, v14
v_mfma_f32_32x32x16_bf16 v[32:47], v[112:115], v[120:123], v[32:47]
ds_read_b128 v[96:99], v10
ds_read_b64_tr_b16 v[104:105], v16 offset:8192
v_lshlrev_b32 v30, 16, v112
v_and_b32 v31, 0xffff0000, v112
v_fmac_f32 v28, v30, v30
v_fmac_f32 v29, v31, v31
v_mfma_f32_32x32x16_bf16 v[48:63], v[112:115], v[124:127], v[48:63]
ds_read_b64_tr_b16 v[106:107], v16 offset:9216
ds_read_b64_tr_b16 v[108:109], v17 offset:8192
v_lshlrev_b32 v30, 16, v113
v_and_b32 v31, 0xffff0000, v113
v_fmac_f32 v28, v30, v30
)ASM"
R"ASM(v_fmac_f32 v29, v31, v31
v_mfma_f32_16x16x32_bf16 v[20:23], v[208:211], v[112:115], v[20:23]
v_mfma_f32_32x32x16_bf16 v[64:79], v[116:119], v[120:123], v[64:79]
ds_read_b64_tr_b16 v[110:111], v17 offset:9216
ds_read_b128 v[100:103], v14
v_lshlrev_b32 v30, 16, v114
v_and_b32 v31, 0xffff0000, v114
v_fmac_f32 v28, v30, v30
v_fmac_f32 v29, v31, v31
v_mfma_f32_32x32x16_bf16 v[80:95], v[116:119], v[124:127], v[80:95]
v_lshlrev_b32 v30, 16, v115
v_and_b32 v31, 0xffff0000, v115
v_fmac_f32 v28, v30, v30
v_fmac_f32 v29, v31, v31
v_mfma_f32_16x16x32_bf16 v[24:27], v[208:211], v[120:123], v[24:27]
s_waitcnt lgkmcnt(0)
v_add_u32 v11, 0x8000, v11
v_add_u32 v15, 0x8000, v15
v_mfma_f32_32x32x16_bf16 v[32:47], v[96:99], v[104:107], v[32:47]
ds_read_b128 v[112:115], v11
ds_read_b64_tr_b16 v[120:121], v16 offset:12288
v_lshlrev_b32 v30, 16, v96
v_and_b32 v31, 0xffff0000, v96
v_fmac_f32 v28, v30, v30
v_fmac_f32 v29, v31, v31
v_mfma_f32_32x32x16_bf16 v[48:63], v[96:99], v[108:111], v[48:63]
ds_read_b64_tr_b16 v[122:123], v16 offset:13312
ds_read_b64_tr_b16 v[124:125], v17 offset:12288
v_lshlrev_b32 v30, 16, v97
v_and_b32 v31, 0xffff0000, v97
v_fmac_f32 v28, v30, v30
v_fmac_f32 v29, v31, v31
v_mfma_f32_16x16x32_bf16 v[20:23], v[208:211], v[96:99], v[20:23]
v_mfma_f32_32x32x16_bf16 v[64:79], v[100:103], v[104:107], v[64:79]
ds_read_b64_tr_b16 v[126:127], v17 offset:13312
ds_read_b128 v[116:119], v15
v_lshlrev_b32 v30, 16, v98
v_and_b32 v31, 0xffff0000, v98
v_fmac_f32 v28, v30, v30
v_fmac_f32 v29, v31, v31
v_mfma_f32_32x32x16_bf16 v[80:95], v[100:103], v[108:111], v[80:95]
v_lshlrev_b32 v30, 16, v99
v_and_b32 v31, 0xffff0000, v99
v_fmac_f32 v28, v30, v30
v_fmac_f32 v29, v31, v31
v_mfma_f32_16x16x32_bf16 v[24:27], v[208:211], v[104:107], v[24:27]
s_waitcnt lgkmcnt(0)
s_barrier
v_add_u32 v8, 0x8000, v8
v_add_u32 v16, 0x8000, v16
v_add_u32 v17, 0x8000, v17
v_add_u32 v12, 0x8000, v12
v_mfma_f32_32x32x16_bf16 v[32:47], v[112:115], v[120:123], v[32:47]
ds_read_b128 v[96:99], v8
ds_read_b64_tr_b16 v[104:105], v16 offset:0
v_lshlrev_b32 v30, 16, v112
v_and_b32 v31, 0xffff0000, v112
v_fmac_f32 v28, v30, v30
v_fmac_f32 v29, v31, v31
v_mfma_f32_32x32x16_bf16 v[48:63], v[112:115], v[124:127], v[48:63]
ds_read_b64_tr_b16 v[106:107], v16 offset:1024
ds_read_b64_tr_b16 v[108:109], v17 offset:0
v_lshlrev_b32 v30, 16, v113
v_and_b32 v31, 0xffff0000, v113
v_fmac_f32 v28, v30, v30
v_fmac_f32 v29, v31, v31
v_mfma_f32_16x16x32_bf16 v[20:23], v[208:211], v[112:115], v[20:23]
v_mfma_f32_32x32x16_bf16 v[64:79], v[116:119], v[120:123], v[64:79]
ds_read_b64_tr_b16 v[110:111], v17 offset:1024
ds_read_b128 v[100:103], v12
v_lshlrev_b32 v30, 16, v114
v_and_b32 v31, 0xffff0000, v114
v_fmac_f32 v28, v30, v30
v_fmac_f32 v29, v31, v31
v_mfma_f32_32x32x16_bf16 v[80:95], v[116:119], v[124:127], v[80:95]
v_lshlrev_b32 v30, 16, v115
v_and_b32 v31, 0xffff0000, v115
v_fmac_f32 v28, v30, v30
v_fmac_f32 v29, v31, v31
v_mfma_f32_16x16x32_bf16 v[24:27], v[208:211], v[120:123], v[24:27]
s_waitcnt lgkmcnt(0)
v_add_u32 v9, 0x8000, v9
v_add_u32 v13, 0x8000, v13
v_mfma_f32_32x32x16_bf16 v[32:47], v[96:99], v[104:107], v[32:47]
ds_read_b128 v[112:115], v9
ds_read_b64_tr_b16 v[120:121], v16 offset:4096
v_lshlrev_b32 v30, 16, v96
v_and_b32 v31, 0xffff0000, v96
v_fmac_f32 v28, v30, v30
v_fmac_f32 v29, v31, v31
v_mfma_f32_32x32x16_bf16 v[48:63], v[96:99], v[108:111], v[48:63]
ds_read_b64_tr_b16 v[122:123], v16 offset:5120
ds_read_b64_tr_b16 v[124:125], v17 offset:4096
v_lshlrev_b32 v30, 16, v97
v_and_b32 v31, 0xffff0000, v97
v_fmac_f32 v28, v30, v30
v_fmac_f32 v29, v31, v31
v_mfma_f32_16x16x32_bf16 v[20:23], v[208:211], v[96:99], v[20:23]
v_mfma_f32_32x32x16_bf16 v[64:79], v[100:103], v[104:107], v[64:79]
ds_read_b64_tr_b16 v[126:127], v17 offset:5120
ds_read_b128 v[116:119], v13
v_lshlrev_b32 v30, 16, v98
v_and_b32 v31, 0xffff0000, v98
v_fmac_f32 v28, v30, v30
v_fmac_f32 v29, v31, v31
v_mfma_f32_32x32x16_bf16 v[80:95], v[100:103], v[108:111], v[80:95]
v_lshlrev_b32 v30, 16, v99
v_and_b32 v31, 0xffff0000, v99
v_fmac_f32 v28, v30, v30
v_fmac_f32 v29, v31, v31
v_mfma_f32_16x16x32_bf16 v[24:27], v[208:211], v[104:107], v[24:27]
s_waitcnt lgkmcnt(0)
v_add_u32 v10, 0x8000, v10
v_add_u32 v14, 0x8000, v14
v_mfma_f32_32x32x16_bf16 v[32:47], v[112:115], v[120:123], v[32:47]
ds_read_b128 v[96:99], v10
ds_read_b64_tr_b16 v[104:105], v16 offset:8192
v_lshlrev_b32 v30, 16, v112
v_and_b32 v31, 0xffff0000, v112
v_fmac_f32 v28, v30, v30
v_fmac_f32 v29, v31, v31
v_mfma_f32_32x32x16_bf16 v[48:63], v[112:115], v[124:127], v[48:63]
ds_read_b64_tr_b16 v[106:107], v16 offset:9216
ds_read_b64_tr_b16 v[108:109], v17 offset:8192
v_lshlrev_b32 v30, 16, v113
v_and_b32 v31, 0xffff0000, v113
v_fmac_f32 v28, v30, v30
v_fmac_f32 v29, v31, v31
v_mfma_f32_16x16x32_bf16 v[20:23], v[208:211], v[112:115], v[20:23]
v_mfma_f32_32x32x16_bf16 v[64:79], v[116:119], v[120:123], v[64:79]
ds_read_b64_tr_b16 v[110:111], v17 offset:9216
ds_read_b128 v[100:103], v14
v_lshlrev_b32 v30, 16, v114
v_and_b32 v31, 0xffff0000, v114
v_fmac_f32 v28, v30, v30
v_fmac_f32 v29, v31, v31
v_mfma_f32_32x32x16_bf16 v[80:95], v[116:119], v[124:127], v[80:95]
v_lshlrev_b32 v30, 16, v115
v_and_b32 v31, 0xffff0000, v115
v_fmac_f32 v28, v30, v30
v_fmac_f32 v29, v31, v31
v_mfma_f32_16x16x32_bf16 v[24:27], v[208:211], v[120:123], v[24:27]
s_waitcnt lgkmcnt(0)
v_add_u32 v11, 0x8000, v11
v_add_u32 v15, 0x8000, v15
v_mfma_f32_32x32x16_bf16 v[32:47], v[96:99], v[104:107], v[32:47]
ds_read_b128 v[112:115], v11
ds_read_b64_tr_b16 v[120:121], v16 offset:12288
v_lshlrev_b32 v30, 16, v96
v_and_b32 v31, 0xffff0000, v96
v_fmac_f32 v28, v30, v30
v_fmac_f32 v29, v31, v31
v_mfma_f32_32x32x16_bf16 v[48:63], v[96:99], v[108:111], v[48:63]
ds_read_b64_tr_b16 v[122:123], v16 offset:13312
ds_read_b64_tr_b16 v[124:125], v17 offset:12288
v_lshlrev_b32 v30, 16, v97
v_and_b32 v31, 0xffff0000, v97
v_fmac_f32 v28, v30, v30
v_fmac_f32 v29, v31, v31
v_mfma_f32_16x16x32_bf16 v[20:23], v[208:211], v[96:99], v[20:23]
v_mfma_f32_32x32x16_bf16 v[64:79], v[100:103], v[104:107], v[64:79]
ds_read_b64_tr_b16 v[126:127], v17 offset:13312
ds_read_b128 v[116:119], v15
v_lshlrev_b32 v30, 16, v98
v_and_b32 v31, 0xffff0000, v98
v_fmac_f32 v28, v30, v30
v_fmac_f32 v29, v31, v31
v_mfma_f32_32x32x16_bf16 v[80:95], v[100:103], v[108:111], v[80:95]
v_lshlrev_b32 v30, 16, v99
v_and_b32 v31, 0xffff0000, v99
v_fmac_f32 v28, v30, v30
v_fmac_f32 v29, v31, v31
v_mfma_f32_16x16x32_bf16 v[24:27], v[208:211], v[104:107], v[24:27]
s_waitcnt lgkmcnt(0)
s_barrier
v_add_u32 v8, 0x8000, v8
v_add_u32 v16, 0x8000, v16
v_add_u32 v17, 0x8000, v17
v_add_u32 v12, 0x8000, v12
v_mfma_f32_32x32x16_bf16 v[32:47], v[112:115], v[120:123], v[32:47]
ds_read_b128 v[96:99], v8
ds_read_b64_tr_b16 v[104:105], v16 offset:0
v_lshlrev_b32 v30, 16, v112
v_and_b32 v31, 0xffff0000, v112
v_fmac_f32 v28, v30, v30
v_fmac_f32 v29, v31, v31
v_mfma_f32_32x32x16_bf16 v[48:63], v[112:115], v[124:127], v[48:63]
ds_read_b64_tr_b16 v[106:107], v16 offset:1024
ds_read_b64_tr_b16 v[108:109], v17 offset:0
v_lshlrev_b32 v30, 16, v113
v_and_b32 v31, 0xffff0000, v113
v_fmac_f32 v28, v30, v30
v_fmac_f32 v29, v31, v31
v_mfma_f32_16x16x32_bf16 v[20:23], v[208:211], v[112:115], v[20:23]
v_mfma_f32_32x32x16_bf16 v[64:79], v[116:119], v[120:123], v[64:79]
ds_read_b64_tr_b16 v[110:111], v17 offset:1024
ds_read_b128 v[100:103], v12
v_lshlrev_b32 v30, 16, v114
v_and_b32 v31, 0xffff0000, v114
v_fmac_f32 v28, v30, v30
)ASM"
R"ASM(v_fmac_f32 v29, v31, v31
v_mfma_f32_32x32x16_bf16 v[80:95], v[116:119], v[124:127], v[80:95]
v_lshlrev_b32 v30, 16, v115
v_and_b32 v31, 0xffff0000, v115
v_fmac_f32 v28, v30, v30
v_fmac_f32 v29, v31, v31
v_mfma_f32_16x16x32_bf16 v[24:27], v[208:211], v[120:123], v[24:27]
s_waitcnt lgkmcnt(0)
v_add_u32 v9, 0x8000, v9
v_add_u32 v13, 0x8000, v13
v_mfma_f32_32x32x16_bf16 v[32:47], v[96:99], v[104:107], v[32:47]
ds_read_b128 v[112:115], v9
ds_read_b64_tr_b16 v[120:121], v16 offset:4096
v_lshlrev_b32 v30, 16, v96
v_and_b32 v31, 0xffff0000, v96
v_fmac_f32 v28, v30, v30
v_fmac_f32 v29, v31, v31
v_mfma_f32_32x32x16_bf16 v[48:63], v[96:99], v[108:111], v[48:63]
ds_read_b64_tr_b16 v[122:123], v16 offset:5120
ds_read_b64_tr_b16 v[124:125], v17 offset:4096
v_lshlrev_b32 v30, 16, v97
v_and_b32 v31, 0xffff0000, v97
v_fmac_f32 v28, v30, v30
v_fmac_f32 v29, v31, v31
v_mfma_f32_16x16x32_bf16 v[20:23], v[208:211], v[96:99], v[20:23]
v_mfma_f32_32x32x16_bf16 v[64:79], v[100:103], v[104:107], v[64:79]
ds_read_b64_tr_b16 v[126:127], v17 offset:5120
ds_read_b128 v[116:119], v13
v_lshlrev_b32 v30, 16, v98
v_and_b32 v31, 0xffff0000, v98
v_fmac_f32 v28, v30, v30
v_fmac_f32 v29, v31, v31
v_mfma_f32_32x32x16_bf16 v[80:95], v[100:103], v[108:111], v[80:95]
v_lshlrev_b32 v30, 16, v99
v_and_b32 v31, 0xffff0000, v99
v_fmac_f32 v28, v30, v30
v_fmac_f32 v29, v31, v31
v_mfma_f32_16x16x32_bf16 v[24:27], v[208:211], v[104:107], v[24:27]
s_waitcnt lgkmcnt(0)
v_add_u32 v10, 0x8000, v10
v_add_u32 v14, 0x8000, v14
v_mfma_f32_32x32x16_bf16 v[32:47], v[112:115], v[120:123], v[32:47]
ds_read_b128 v[96:99], v10
ds_read_b64_tr_b16 v[104:105], v16 offset:8192
v_lshlrev_b32 v30, 16, v112
v_and_b32 v31, 0xffff0000, v112
v_fmac_f32 v28, v30, v30
v_fmac_f32 v29, v31, v31
v_mfma_f32_32x32x16_bf16 v[48:63], v[112:115], v[124:127], v[48:63]
ds_read_b64_tr_b16 v[106:107], v16 offset:9216
ds_read_b64_tr_b16 v[108:109], v17 offset:8192
v_lshlrev_b32 v30, 16, v113
v_and_b32 v31, 0xffff0000, v113
v_fmac_f32 v28, v30, v30
v_fmac_f32 v29, v31, v31
v_mfma_f32_16x16x32_bf16 v[20:23], v[208:211], v[112:115], v[20:23]
v_mfma_f32_32x32x16_bf16 v[64:79], v[116:119], v[120:123], v[64:79]
ds_read_b64_tr_b16 v[110:111], v17 offset:9216
ds_read_b128 v[100:103], v14
v_lshlrev_b32 v30, 16, v114
v_and_b32 v31, 0xffff0000, v114
v_fmac_f32 v28, v30, v30
v_fmac_f32 v29, v31, v31
v_mfma_f32_32x32x16_bf16 v[80:95], v[116:119], v[124:127], v[80:95]
v_lshlrev_b32 v30, 16, v115
v_and_b32 v31, 0xffff0000, v115
v_fmac_f32 v28, v30, v30
v_fmac_f32 v29, v31, v31
v_mfma_f32_16x16x32_bf16 v[24:27], v[208:211], v[120:123], v[24:27]
s_waitcnt lgkmcnt(0)
v_add_u32 v11, 0x8000, v11
v_add_u32 v15, 0x8000, v15
v_mfma_f32_32x32x16_bf16 v[32:47], v[96:99], v[104:107], v[32:47]
ds_read_b128 v[112:115], v11
ds_read_b64_tr_b16 v[120:121], v16 offset:12288
v_lshlrev_b32 v30, 16, v96
v_and_b32 v31, 0xffff0000, v96
v_fmac_f32 v28, v30, v30
v_fmac_f32 v29, v31, v31
v_mfma_f32_32x32x16_bf16 v[48:63], v[96:99], v[108:111], v[48:63]
ds_read_b64_tr_b16 v[122:123], v16 offset:13312
ds_read_b64_tr_b16 v[124:125], v17 offset:12288
v_lshlrev_b32 v30, 16, v97
v_and_b32 v31, 0xffff0000, v97
v_fmac_f32 v28, v30, v30
v_fmac_f32 v29, v31, v31
v_mfma_f32_16x16x32_bf16 v[20:23], v[208:211], v[96:99], v[20:23]
v_mfma_f32_32x32x16_bf16 v[64:79], v[100:103], v[104:107], v[64:79]
ds_read_b64_tr_b16 v[126:127], v17 offset:13312
ds_read_b128 v[116:119], v15
v_lshlrev_b32 v30, 16, v98
v_and_b32 v31, 0xffff0000, v98
v_fmac_f32 v28, v30, v30
v_fmac_f32 v29, v31, v31
v_mfma_f32_32x32x16_bf16 v[80:95], v[100:103], v[108:111], v[80:95]
v_lshlrev_b32 v30, 16, v99
v_and_b32 v31, 0xffff0000, v99
v_fmac_f32 v28, v30, v30
v_fmac_f32 v29, v31, v31
v_mfma_f32_16x16x32_bf16 v[24:27], v[208:211], v[104:107], v[24:27]
s_waitcnt lgkmcnt(0)
s_barrier
v_add_u32 v8, 0x8000, v8
v_add_u32 v16, 0x8000, v16
v_add_u32 v17, 0x8000, v17
v_add_u32 v12, 0x8000, v12
v_mfma_f32_32x32x16_bf16 v[32:47], v[112:115], v[120:123], v[32:47]
ds_read_b128 v[96:99], v8
ds_read_b64_tr_b16 v[104:105], v16 offset:0
v_lshlrev_b32 v30, 16, v112
v_and_b32 v31, 0xffff0000, v112
v_fmac_f32 v28, v30, v30
v_fmac_f32 v29, v31, v31
v_mfma_f32_32x32x16_bf16 v[48:63], v[112:115], v[124:127], v[48:63]
ds_read_b64_tr_b16 v[106:107], v16 offset:1024
ds_read_b64_tr_b16 v[108:109], v17 offset:0
v_lshlrev_b32 v30, 16, v113
v_and_b32 v31, 0xffff0000, v113
v_fmac_f32 v28, v30, v30
v_fmac_f32 v29, v31, v31
v_mfma_f32_16x16x32_bf16 v[20:23], v[208:211], v[112:115], v[20:23]
v_mfma_f32_32x32x16_bf16 v[64:79], v[116:119], v[120:123], v[64:79]
ds_read_b64_tr_b16 v[110:111], v17 offset:1024
ds_read_b128 v[100:103], v12
v_lshlrev_b32 v30, 16, v114
v_and_b32 v31, 0xffff0000, v114
v_fmac_f32 v28, v30, v30
v_fmac_f32 v29, v31, v31
v_mfma_f32_32x32x16_bf16 v[80:95], v[116:119], v[124:127], v[80:95]
v_lshlrev_b32 v30, 16, v115
v_and_b32 v31, 0xffff0000, v115
v_fmac_f32 v28, v30, v30
v_fmac_f32 v29, v31, v31
v_mfma_f32_16x16x32_bf16 v[24:27], v[208:211], v[120:123], v[24:27]
s_waitcnt lgkmcnt(0)
v_add_u32 v9, 0x8000, v9
v_add_u32 v13, 0x8000, v13
v_mfma_f32_32x32x16_bf16 v[32:47], v[96:99], v[104:107], v[32:47]
ds_read_b128 v[112:115], v9
ds_read_b64_tr_b16 v[120:121], v16 offset:4096
v_lshlrev_b32 v30, 16, v96
v_and_b32 v31, 0xffff0000, v96
v_fmac_f32 v28, v30, v30
v_fmac_f32 v29, v31, v31
v_mfma_f32_32x32x16_bf16 v[48:63], v[96:99], v[108:111], v[48:63]
ds_read_b64_tr_b16 v[122:123], v16 offset:5120
ds_read_b64_tr_b16 v[124:125], v17 offset:4096
v_lshlrev_b32 v30, 16, v97
v_and_b32 v31, 0xffff0000, v97
v_fmac_f32 v28, v30, v30
v_fmac_f32 v29, v31, v31
v_mfma_f32_16x16x32_bf16 v[20:23], v[208:211], v[96:99], v[20:23]
v_mfma_f32_32x32x16_bf16 v[64:79], v[100:103], v[104:107], v[64:79]
ds_read_b64_tr_b16 v[126:127], v17 offset:5120
ds_read_b128 v[116:119], v13
v_lshlrev_b32 v30, 16, v98
v_and_b32 v31, 0xffff0000, v98
v_fmac_f32 v28, v30, v30
v_fmac_f32 v29, v31, v31
v_mfma_f32_32x32x16_bf16 v[80:95], v[100:103], v[108:111], v[80:95]
v_lshlrev_b32 v30, 16, v99
v_and_b32 v31, 0xffff0000, v99
v_fmac_f32 v28, v30, v30
v_fmac_f32 v29, v31, v31
v_mfma_f32_16x16x32_bf16 v[24:27], v[208:211], v[104:107], v[24:27]
s_waitcnt lgkmcnt(0)
v_add_u32 v10, 0x8000, v10
v_add_u32 v14, 0x8000, v14
v_mfma_f32_32x32x16_bf16 v[32:47], v[112:115], v[120:123], v[32:47]
ds_read_b128 v[96:99], v10
ds_read_b64_tr_b16 v[104:105], v16 offset:8192
v_lshlrev_b32 v30, 16, v112
v_and_b32 v31, 0xffff0000, v112
v_fmac_f32 v28, v30, v30
v_fmac_f32 v29, v31, v31
v_mfma_f32_32x32x16_bf16 v[48:63], v[112:115], v[124:127], v[48:63]
ds_read_b64_tr_b16 v[106:107], v16 offset:9216
ds_read_b64_tr_b16 v[108:109], v17 offset:8192
v_lshlrev_b32 v30, 16, v113
v_and_b32 v31, 0xffff0000, v113
v_fmac_f32 v28, v30, v30
v_fmac_f32 v29, v31, v31
v_mfma_f32_16x16x32_bf16 v[20:23], v[208:211], v[112:115], v[20:23]
v_mfma_f32_32x32x16_bf16 v[64:79], v[116:119], v[120:123], v[64:79]
ds_read_b64_tr_b16 v[110:111], v17 offset:9216
ds_read_b128 v[100:103], v14
v_lshlrev_b32 v30, 16, v114
v_and_b32 v31, 0xffff0000, v114
v_fmac_f32 v28, v30, v30
v_fmac_f32 v29, v31, v31
v_mfma_f32_32x32x16_bf16 v[80:95], v[116:119], v[124:127], v[80:95]
v_lshlrev_b32 v30, 16, v115
v_and_b32 v31, 0xffff0000, v115
v_fmac_f32 v28, v30, v30
v_fmac_f32 v29, v31, v31
v_mfma_f32_16x16x32_bf16 v[24:27], v[208:211], v[120:123], v[24:27]
s_waitcnt lgkmcnt(0)
v_add_u32 v11, 0x8000, v11
v_add_u32 v15, 0x8000, v15
v_mfma_f32_32x32x16_bf16 v[32:47], v[96:99], v[104:107], v[32:47]
)ASM"
R"ASM(ds_read_b128 v[112:115], v11
ds_read_b64_tr_b16 v[120:121], v16 offset:12288
v_lshlrev_b32 v30, 16, v96
v_and_b32 v31, 0xffff0000, v96
v_fmac_f32 v28, v30, v30
v_fmac_f32 v29, v31, v31
v_lshlrev_b32 v30, 16, v97
v_and_b32 v31, 0xffff0000, v97
v_mfma_f32_32x32x16_bf16 v[48:63], v[96:99], v[108:111], v[48:63]
ds_read_b64_tr_b16 v[122:123], v16 offset:13312
ds_read_b64_tr_b16 v[124:125], v17 offset:12288
v_fmac_f32 v28, v30, v30
v_fmac_f32 v29, v31, v31
v_lshlrev_b32 v30, 16, v98
v_and_b32 v31, 0xffff0000, v98
v_fmac_f32 v28, v30, v30
v_fmac_f32 v29, v31, v31
v_mfma_f32_16x16x32_bf16 v[20:23], v[208:211], v[96:99], v[20:23]
v_mfma_f32_32x32x16_bf16 v[64:79], v[100:103], v[104:107], v[64:79]
ds_read_b64_tr_b16 v[126:127], v17 offset:13312
ds_read_b128 v[116:119], v15
v_lshlrev_b32 v30, 16, v99
v_and_b32 v31, 0xffff0000, v99
v_fmac_f32 v28, v30, v30
v_fmac_f32 v29, v31, v31
v_mfma_f32_32x32x16_bf16 v[80:95], v[100:103], v[108:111], v[80:95]
v_mfma_f32_16x16x32_bf16 v[24:27], v[208:211], v[104:107], v[24:27]
s_waitcnt lgkmcnt(0)
v_mfma_f32_32x32x16_bf16 v[32:47], v[112:115], v[120:123], v[32:47]
v_lshlrev_b32 v30, 16, v112
v_and_b32 v31, 0xffff0000, v112
v_fmac_f32 v28, v30, v30
v_fmac_f32 v29, v31, v31
v_mfma_f32_32x32x16_bf16 v[48:63], v[112:115], v[124:127], v[48:63]
v_lshlrev_b32 v30, 16, v113
v_and_b32 v31, 0xffff0000, v113
v_fmac_f32 v28, v30, v30
v_fmac_f32 v29, v31, v31
v_mfma_f32_16x16x32_bf16 v[20:23], v[208:211], v[112:115], v[20:23]
v_mfma_f32_32x32x16_bf16 v[64:79], v[116:119], v[120:123], v[64:79]
v_lshlrev_b32 v30, 16, v114
v_and_b32 v31, 0xffff0000, v114
v_fmac_f32 v28, v30, v30
v_fmac_f32 v29, v31, v31
v_mfma_f32_32x32x16_bf16 v[80:95], v[116:119], v[124:127], v[80:95]
v_lshlrev_b32 v30, 16, v115
v_and_b32 v31, 0xffff0000, v115
v_fmac_f32 v28, v30, v30
v_fmac_f32 v29, v31, v31
v_mfma_f32_16x16x32_bf16 v[24:27], v[208:211], v[120:123], v[24:27]
s_lshl_b32 s35, s33, 6
s_lshl_b32 s36, s34, 5
s_add_u32 s35, s35, s36
s_add_u32 s35, s35, 0x2000
v_add_u32 v236, s35, v19
v_lshlrev_b32 v236, 2, v236
s_lshl_b32 s35, s34, 6
s_lshl_b32 s36, s33, 5
s_add_u32 s35, s35, s36
s_add_u32 s35, s35, 0x2000
v_add_u32 v237, s35, v19
v_lshlrev_b32 v237, 2, v237
v_xor_b32 v238, 0x80, v237
v_mov_b32 v6, -1
s_lshl_b32 s35, s33, 8
s_lshl_b32 s36, s34, 7
s_add_u32 s35, s35, s36
s_add_u32 s35, s35, 0x8000
v_lshlrev_b32 v239, 4, v18
v_add_u32 v239, s35, v239
v_xor_b32 v240, 0x80, v239
v_add_f32 v28, v28, v29
v_mov_b32 v29, v28
v_mov_b32 v2, 0x3acccccd
ds_write_b32 v237, v24 offset:1024
v_permlane32_swap_b32 v28, v29
v_mul_f32 v21, 0x3acccccd, v20
v_add_f32 v28, v28, v29
v_mul_f32 v29, v21, v21
ds_write_b32 v236, v21
v_fma_f32 v28, v28, v2, -v29
v_max_f32 v28, 0, v28
v_add_f32 v28, 0x3727c5ac, v28
v_rsq_f32 v28, v28
s_nop 0
ds_write_b32 v236, v28 offset:512
s_waitcnt lgkmcnt(0)
s_barrier
ds_read_b32 v200, v237 offset:1024
ds_read_b128 v[136:139], v239 offset:0
ds_read_b128 v[168:171], v239 offset:512
ds_read_b128 v[140:143], v239 offset:32
ds_read_b128 v[172:175], v239 offset:544
ds_read_b128 v[144:147], v239 offset:64
ds_read_b128 v[176:179], v239 offset:576
ds_read_b128 v[148:151], v239 offset:96
ds_read_b128 v[180:183], v239 offset:608
ds_read_b32 v202, v238 offset:1024
ds_read_b128 v[152:155], v240 offset:0
ds_read_b128 v[184:187], v240 offset:512
ds_read_b128 v[156:159], v240 offset:32
ds_read_b128 v[188:191], v240 offset:544
ds_read_b128 v[160:163], v240 offset:64
ds_read_b128 v[192:195], v240 offset:576
ds_read_b128 v[164:167], v240 offset:96
ds_read_b128 v[196:199], v240 offset:608
s_waitcnt lgkmcnt(15)
v_sub_f32 v200, 0, v200
v_pk_fma_f32 v[32:33], v[136:137], v[200:201], v[32:33] op_sel_hi:[1,0,1]
v_pk_mul_f32 v[32:33], v[32:33], v[168:169]
v_cvt_pk_bf16_f32 v96, v32, v33
v_pk_fma_f32 v[34:35], v[138:139], v[200:201], v[34:35] op_sel_hi:[1,0,1]
v_pk_mul_f32 v[34:35], v[34:35], v[170:171]
v_cvt_pk_bf16_f32 v97, v34, v35
s_waitcnt lgkmcnt(14)
v_pk_fma_f32 v[36:37], v[140:141], v[200:201], v[36:37] op_sel_hi:[1,0,1]
s_waitcnt lgkmcnt(13)
v_pk_mul_f32 v[36:37], v[36:37], v[172:173]
v_cvt_pk_bf16_f32 v98, v36, v37
v_pk_fma_f32 v[38:39], v[142:143], v[200:201], v[38:39] op_sel_hi:[1,0,1]
v_pk_mul_f32 v[38:39], v[38:39], v[174:175]
v_cvt_pk_bf16_f32 v99, v38, v39
global_store_dwordx4 v128, v[96:99], s[38:39] offset:0 sc1
s_waitcnt lgkmcnt(12)
v_pk_fma_f32 v[40:41], v[144:145], v[200:201], v[40:41] op_sel_hi:[1,0,1]
s_waitcnt lgkmcnt(11)
v_pk_mul_f32 v[40:41], v[40:41], v[176:177]
v_cvt_pk_bf16_f32 v100, v40, v41
v_pk_fma_f32 v[42:43], v[146:147], v[200:201], v[42:43] op_sel_hi:[1,0,1]
v_pk_mul_f32 v[42:43], v[42:43], v[178:179]
v_cvt_pk_bf16_f32 v101, v42, v43
s_waitcnt lgkmcnt(10)
v_pk_fma_f32 v[44:45], v[148:149], v[200:201], v[44:45] op_sel_hi:[1,0,1]
s_waitcnt lgkmcnt(9)
v_pk_mul_f32 v[44:45], v[44:45], v[180:181]
v_cvt_pk_bf16_f32 v102, v44, v45
v_pk_fma_f32 v[46:47], v[150:151], v[200:201], v[46:47] op_sel_hi:[1,0,1]
v_pk_mul_f32 v[46:47], v[46:47], v[182:183]
v_cvt_pk_bf16_f32 v103, v46, v47
global_store_dwordx4 v128, v[100:103], s[38:39] offset:768 sc1
s_waitcnt lgkmcnt(8)
v_sub_f32 v202, 0, v202
v_pk_fma_f32 v[48:49], v[136:137], v[202:203], v[48:49] op_sel_hi:[1,0,1]
v_pk_mul_f32 v[48:49], v[48:49], v[168:169]
v_cvt_pk_bf16_f32 v104, v48, v49
v_pk_fma_f32 v[50:51], v[138:139], v[202:203], v[50:51] op_sel_hi:[1,0,1]
v_pk_mul_f32 v[50:51], v[50:51], v[170:171]
v_cvt_pk_bf16_f32 v105, v50, v51
v_pk_fma_f32 v[52:53], v[140:141], v[202:203], v[52:53] op_sel_hi:[1,0,1]
v_pk_mul_f32 v[52:53], v[52:53], v[172:173]
v_cvt_pk_bf16_f32 v106, v52, v53
v_pk_fma_f32 v[54:55], v[142:143], v[202:203], v[54:55] op_sel_hi:[1,0,1]
v_pk_mul_f32 v[54:55], v[54:55], v[174:175]
v_cvt_pk_bf16_f32 v107, v54, v55
global_store_dwordx4 v129, v[104:107], s[38:39] offset:0 sc1
v_pk_fma_f32 v[56:57], v[144:145], v[202:203], v[56:57] op_sel_hi:[1,0,1]
v_pk_mul_f32 v[56:57], v[56:57], v[176:177]
v_cvt_pk_bf16_f32 v108, v56, v57
v_pk_fma_f32 v[58:59], v[146:147], v[202:203], v[58:59] op_sel_hi:[1,0,1]
v_pk_mul_f32 v[58:59], v[58:59], v[178:179]
v_cvt_pk_bf16_f32 v109, v58, v59
v_pk_fma_f32 v[60:61], v[148:149], v[202:203], v[60:61] op_sel_hi:[1,0,1]
v_pk_mul_f32 v[60:61], v[60:61], v[180:181]
v_cvt_pk_bf16_f32 v110, v60, v61
v_pk_fma_f32 v[62:63], v[150:151], v[202:203], v[62:63] op_sel_hi:[1,0,1]
v_pk_mul_f32 v[62:63], v[62:63], v[182:183]
v_cvt_pk_bf16_f32 v111, v62, v63
global_store_dwordx4 v129, v[108:111], s[38:39] offset:768 sc1
s_waitcnt lgkmcnt(7)
v_pk_fma_f32 v[64:65], v[152:153], v[200:201], v[64:65] op_sel_hi:[1,0,1]
s_waitcnt lgkmcnt(6)
v_pk_mul_f32 v[64:65], v[64:65], v[184:185]
v_cvt_pk_bf16_f32 v96, v64, v65
v_pk_fma_f32 v[66:67], v[154:155], v[200:201], v[66:67] op_sel_hi:[1,0,1]
v_pk_mul_f32 v[66:67], v[66:67], v[186:187]
v_cvt_pk_bf16_f32 v97, v66, v67
s_waitcnt lgkmcnt(5)
v_pk_fma_f32 v[68:69], v[156:157], v[200:201], v[68:69] op_sel_hi:[1,0,1]
s_waitcnt lgkmcnt(4)
v_pk_mul_f32 v[68:69], v[68:69], v[188:189]
v_cvt_pk_bf16_f32 v98, v68, v69
v_pk_fma_f32 v[70:71], v[158:159], v[200:201], v[70:71] op_sel_hi:[1,0,1]
v_pk_mul_f32 v[70:71], v[70:71], v[190:191]
v_cvt_pk_bf16_f32 v99, v70, v71
global_store_dwordx4 v128, v[96:99], s[40:41] offset:0 sc1
s_waitcnt lgkmcnt(3)
v_pk_fma_f32 v[72:73], v[160:161], v[200:201], v[72:73] op_sel_hi:[1,0,1]
s_waitcnt lgkmcnt(2)
v_pk_mul_f32 v[72:73], v[72:73], v[192:193]
v_cvt_pk_bf16_f32 v100, v72, v73
v_pk_fma_f32 v[74:75], v[162:163], v[200:201], v[74:75] op_sel_hi:[1,0,1]
v_pk_mul_f32 v[74:75], v[74:75], v[194:195]
v_cvt_pk_bf16_f32 v101, v74, v75
s_waitcnt lgkmcnt(1)
v_pk_fma_f32 v[76:77], v[164:165], v[200:201], v[76:77] op_sel_hi:[1,0,1]
s_waitcnt lgkmcnt(0)
v_pk_mul_f32 v[76:77], v[76:77], v[196:197]
v_cvt_pk_bf16_f32 v102, v76, v77
v_pk_fma_f32 v[78:79], v[166:167], v[200:201], v[78:79] op_sel_hi:[1,0,1]
v_pk_mul_f32 v[78:79], v[78:79], v[198:199]
)ASM"
R"ASM(v_cvt_pk_bf16_f32 v103, v78, v79
global_store_dwordx4 v128, v[100:103], s[40:41] offset:768 sc1
v_pk_fma_f32 v[80:81], v[152:153], v[202:203], v[80:81] op_sel_hi:[1,0,1]
v_pk_mul_f32 v[80:81], v[80:81], v[184:185]
v_cvt_pk_bf16_f32 v104, v80, v81
v_pk_fma_f32 v[82:83], v[154:155], v[202:203], v[82:83] op_sel_hi:[1,0,1]
v_pk_mul_f32 v[82:83], v[82:83], v[186:187]
v_cvt_pk_bf16_f32 v105, v82, v83
v_pk_fma_f32 v[84:85], v[156:157], v[202:203], v[84:85] op_sel_hi:[1,0,1]
v_pk_mul_f32 v[84:85], v[84:85], v[188:189]
v_cvt_pk_bf16_f32 v106, v84, v85
v_pk_fma_f32 v[86:87], v[158:159], v[202:203], v[86:87] op_sel_hi:[1,0,1]
v_pk_mul_f32 v[86:87], v[86:87], v[190:191]
v_cvt_pk_bf16_f32 v107, v86, v87
global_store_dwordx4 v129, v[104:107], s[40:41] offset:0 sc1
v_pk_fma_f32 v[88:89], v[160:161], v[202:203], v[88:89] op_sel_hi:[1,0,1]
v_pk_mul_f32 v[88:89], v[88:89], v[192:193]
v_cvt_pk_bf16_f32 v108, v88, v89
v_pk_fma_f32 v[90:91], v[162:163], v[202:203], v[90:91] op_sel_hi:[1,0,1]
v_pk_mul_f32 v[90:91], v[90:91], v[194:195]
v_cvt_pk_bf16_f32 v109, v90, v91
v_pk_fma_f32 v[92:93], v[164:165], v[202:203], v[92:93] op_sel_hi:[1,0,1]
v_pk_mul_f32 v[92:93], v[92:93], v[196:197]
v_cvt_pk_bf16_f32 v110, v92, v93
v_pk_fma_f32 v[94:95], v[166:167], v[202:203], v[94:95] op_sel_hi:[1,0,1]
v_pk_mul_f32 v[94:95], v[94:95], v[198:199]
v_cvt_pk_bf16_f32 v111, v94, v95
global_store_dwordx4 v129, v[108:111], s[40:41] offset:768 sc1
s_branch Lend_3_%=
s_nop 2
Lend_3_%=:
k1_code_end:

)ASM"
  :: "s"(__builtin_amdgcn_kernarg_segment_ptr()), "s"(blockIdx.x), "v"(threadIdx.x), "s"((unsigned)(size_t)lds) : "v0", "v2", "v3", "v4", "v5", "v6", "v7", "v8", "v9", "v10", "v11", "v12", "v13", "v14", "v15", "v16", "v17", "v18", "v19", "v20", "v21", "v22", "v23", "v24", "v25", "v26", "v27", "v28", "v29", "v30", "v31", "v32", "v33", "v34", "v35", "v36", "v37", "v38", "v39", "v40", "v41", "v42", "v43", "v44", "v45", "v46", "v47", "v48", "v49", "v50", "v51", "v52", "v53", "v54", "v55", "v56", "v57", "v58", "v59", "v60", "v61", "v62", "v63", "v64", "v65", "v66", "v67", "v68", "v69", "v70", "v71", "v72", "v73", "v74", "v75", "v76", "v77", "v78", "v79", "v80", "v81", "v82", "v83", "v84", "v85", "v86", "v87", "v88", "v89", "v90", "v91", "v92", "v93", "v94", "v95", "v96", "v97", "v98", "v99", "v100", "v101", "v102", "v103", "v104", "v105", "v106", "v107", "v108", "v109", "v110", "v111", "v112", "v113", "v114", "v115", "v116", "v117", "v118", "v119", "v120", "v121", "v122", "v123", "v124", "v125", "v126", "v127", "v128", "v129", "v130", "v131", "v132", "v133", "v134", "v135", "v136", "v137", "v138", "v139", "v140", "v141", "v142", "v143", "v144", "v145", "v146", "v147", "v148", "v149", "v150", "v151", "v152", "v153", "v154", "v155", "v156", "v157", "v158", "v159", "v160", "v161", "v162", "v163", "v164", "v165", "v166", "v167", "v168", "v169", "v170", "v171", "v172", "v173", "v174", "v175", "v176", "v177", "v178", "v179", "v180", "v181", "v182", "v183", "v184", "v185", "v186", "v187", "v188", "v189", "v190", "v191", "v192", "v193", "v194", "v195", "v196", "v197", "v198", "v199", "v200", "v201", "v202", "v203", "v204", "v205", "v206", "v207", "v208", "v209", "v210", "v211", "v212", "v213", "v214", "v215", "v216", "v217", "v218", "v219", "v220", "v221", "v222", "v223", "v224", "v225", "v226", "v227", "v228", "v229", "v230", "v231", "v232", "v233", "v234", "v235", "v236", "v237", "v238", "v239", "v240", "v241", "s4", "s5", "s6", "s7", "s8", "s9", "s10", "s11", "s12", "s13", "s14", "s15", "s16", "s17", "s18", "s19", "s20", "s21", "s22", "s23", "s24", "s25", "s26", "s27", "s28", "s29", "s30", "s31", "s33", "s34", "s35", "s36", "s37", "s38", "s39", "s40", "s41", "s42", "s43", "s44", "s45", "s46", "s47", "s48", "s49", "s50", "s51", "s52", "s53", "s54", "s55", "s56", "s57", "s58", "s59", "s60", "s61", "s62", "s63", "s64", "s65", "s66", "s67", "s68", "s69", "s70", "s71", "s72", "s73", "s74", "s75", "s76", "s77", "s78", "s79", "s80", "s81", "s82", "s83", "s84", "s85", "s86", "s87", "s88", "s89", "s90", "s91", "s92", "s93", "s94", "s95", "s96", "s97", "s98", "s99", "vcc", "scc", "memory");
}

extern "C" __global__ void __launch_bounds__(256, 1) k2_attn_ffn(char* ws, const void* x, void* out) {
  __shared__ __attribute__((aligned(16))) char lds[163840];
  asm volatile(
R"ASM(k2_code_begin:
s_load_dwordx4 s[20:23], %0, 0
s_load_dwordx2 s[26:27], %0, 16
s_mov_b32 s28, %1
v_mov_b32 v0, %2
; ==== prologue: indices, first Q / K / V fragments
v_lshrrev_b32 v6, 6, v0
v_and_b32 v0, 63, v0
v_readfirstlane_b32 s29, v6
s_and_b32 s30, s28, 7
s_lshr_b32 s31, s28, 3
s_lshr_b32 s35, s31, 4
s_lshl_b32 s98, s30, 1
s_add_u32 s98, s98, s35
s_and_b32 s33, s31, 15
s_and_b32 s34, s31, 7
v_and_b32 v6, 31, v0
v_lshrrev_b32 v7, 5, v0
s_lshr_b32 s37, s29, 1
s_lshl_b32 s35, s37, 10
s_lshl_b32 s36, s37, 3
s_add_u32 s36, s36, 0x800
v_lshlrev_b32 v8, 4, v6
v_add_u32 v2, s36, v8
v_lshl_add_u32 v1, v7, 9, v8
v_add_u32 v1, s35, v1
s_mul_i32 s35, s37, 0x600
s_add_u32 s35, s35, 0xa00
v_min_u32 v8, 21, v6
v_mad_u32_u24 v8, v7, 24, v8
v_lshlrev_b32 v3, 4, v8
v_add_u32 v3, s35, v3
v_mov_b32 v8, 0xb50
v_lshlrev_b32 v4, 4, v0
v_cmp_lt_u32 vcc, 31, v0
s_and_b32 s36, s29, 1
s_lshl_b32 s35, s98, 6
s_lshl_b32 s37, s36, 5
s_add_u32 s37, s37, s35
v_cndmask_b32 v2, v2, v8, vcc
s_mul_i32 s37, s37, 0x1600
s_add_u32 s48, s37, 0x290000
s_lshl_b32 s37, s33, 2
s_add_u32 s35, s35, s37
s_lshl_b32 s36, s36, 1
s_add_u32 s37, s35, s36
s_mul_i32 s37, s37, 0xa00
s_add_u32 s37, s37, 0x10000
s_xor_b32 s36, s36, 2
s_add_u32 s35, s35, s36
s_mul_i32 s35, s35, 0xa00
s_add_u32 s35, s35, 0x10000
v_mov_b32 v14, 0
v_mov_b32 v15, 0
v_mov_b32 v20, 0
v_mov_b32 v21, 0
v_mov_b32 v26, 0
v_mov_b32 v27, 0
v_mov_b32 v32, 0
v_mov_b32 v33, 0
s_waitcnt lgkmcnt(0)
s_add_u32 s40, s20, s37
s_addc_u32 s41, s21, 0
s_add_u32 s38, s20, s48
s_addc_u32 s39, s21, 0
s_add_u32 s42, s20, s35
s_addc_u32 s43, s21, 0
global_load_dwordx4 v[10:13], v1, s[40:41] offset:0
v_mov_b32 v5, 0xdddd
s_mov_b32 s49, 0x7a1200
v_mov_b32 v34, 0
v_mov_b32 v35, 0
v_mov_b32 v36, 0
v_mov_b32 v37, 0
v_mov_b32 v38, 0
v_mov_b32 v39, 0
v_mov_b32 v40, 0
v_mov_b32 v41, 0
v_mov_b32 v42, 0
global_load_dwordx4 v[146:149], v1, s[38:39]
v_mov_b32 v43, 0
v_mov_b32 v44, 0
v_mov_b32 v45, 0
v_mov_b32 v46, 0
v_mov_b32 v47, 0
v_mov_b32 v48, 0
v_mov_b32 v49, 0
v_mov_b32 v50, 0
v_mov_b32 v51, 0
v_mov_b32 v52, 0
v_mov_b32 v53, 0
global_load_dwordx2 v[150:151], v2, s[38:39]
v_mov_b32 v54, 0
v_mov_b32 v55, 0
v_mov_b32 v56, 0
v_mov_b32 v57, 0
v_mov_b32 v58, 0
v_mov_b32 v59, 0
v_mov_b32 v60, 0
v_mov_b32 v61, 0
v_mov_b32 v62, 0
v_mov_b32 v63, 0
v_mov_b32 v64, 0
s_mov_b32 exec_hi, 0
global_load_dwordx2 v[14:15], v2, s[40:41] offset:0
global_load_dwordx2 v[20:21], v2, s[40:41] offset:2560
global_load_dwordx2 v[26:27], v2, s[42:43] offset:0
global_load_dwordx2 v[32:33], v2, s[42:43] offset:2560
s_mov_b64 exec, -1
v_mov_b32 v65, 0
v_mov_b32 v66, 0
v_mov_b32 v67, 0
v_mov_b32 v68, 0
v_mov_b32 v69, 0
v_mov_b32 v70, 0
v_mov_b32 v71, 0
v_mov_b32 v72, 0
v_mov_b32 v73, 0
v_mov_b32 v74, 0
v_mov_b32 v75, 0
global_load_dwordx4 v[16:19], v1, s[40:41] offset:2560
v_mov_b32 v76, 0
v_mov_b32 v77, 0
v_mov_b32 v78, 0
v_mov_b32 v79, 0
v_mov_b32 v80, 0
v_mov_b32 v81, 0
v_mov_b32 v82, 0
v_mov_b32 v83, 0
v_mov_b32 v84, 0
v_mov_b32 v85, 0
v_mov_b32 v86, 0
global_load_dwordx4 v[152:155], v3, s[38:39]
v_mov_b32 v87, 0
v_mov_b32 v88, 0
v_mov_b32 v89, 0
v_mov_b32 v90, 0
v_mov_b32 v91, 0
v_mov_b32 v92, 0
v_mov_b32 v93, 0
v_mov_b32 v94, 0
v_mov_b32 v95, 0
v_mov_b32 v96, 0
v_mov_b32 v97, 0
global_load_dwordx4 v[22:25], v1, s[42:43] offset:0
s_lshl_b32 s57, s30, 4
s_lshr_b32 s35, s31, 3
s_lshl_b32 s36, s35, 2
s_add_u32 s57, s57, s36
s_lshr_b32 s36, s30, 1
s_lshl_b32 s36, s36, 2
s_add_u32 s55, s36, s35
s_and_b32 s36, s30, 1
s_lshl_b32 s36, s36, 3
s_add_u32 s59, s36, s34
s_mul_i32 s56, s59, 0x1af
global_load_dwordx4 v[156:159], v3, s[38:39] offset:768
s_lshr_b32 s56, s56, 6
s_and_b32 s35, s34, 0
s_lshl_b32 s35, s35, 1
s_add_u32 s54, s29, s35
s_and_b32 s54, s54, 3
s_and_b32 s63, s59, 0
s_mul_i32 s63, s63, 0x2800
s_mul_i32 s62, s29, 14
s_cmp_eq_u32 s29, 3
s_cselect_b32 s35, 1, 0
s_sub_u32 s62, s62, s35
s_mul_i32 s35, s59, 0x23000
global_load_dwordx4 v[28:31], v1, s[42:43] offset:2560
s_add_u32 s38, s38, 0x1600
s_addc_u32 s39, s39, 0
s_add_u32 s35, s35, 0xaf4000
s_add_u32 s52, s20, s35
s_addc_u32 s53, s21, 0
v_mov_b32 v208, -1
v_mov_b32 v209, -1
v_mov_b32 v210, -1
v_mov_b32 v211, -1
v_lshlrev_b32 v212, 8, v0
s_mul_i32 s51, s29, 0x2a00
s_add_u32 s51, s51, 0x18800
v_add_u32 v205, s51, v4
s_xor_b32 s58, s29, 1
global_load_dwordx4 v[160:163], v1, s[38:39]
s_mul_i32 s58, s58, 0x2a00
s_add_u32 s58, s58, 0x18800
v_add_u32 v206, s58, v4
s_lshl_b32 s51, s33, 2
s_and_b32 s58, s29, 1
s_lshl_b32 s58, s58, 1
s_add_u32 s51, s51, s58
s_mul_i32 s51, s51, 0xa000
s_mul_i32 s58, s98, 0x500
s_add_u32 s51, s51, s58
s_add_u32 s51, s51, 0xd24000
global_load_dwordx2 v[164:165], v2, s[38:39]
s_lshr_b32 s58, s29, 1
s_lshl_b32 s99, s58, 9
s_add_u32 s99, s99, s51
)ASM"
R"ASM(s_add_u32 s4, s20, s99
s_addc_u32 s5, s21, 0
s_add_u32 s6, s4, 0xa000
s_addc_u32 s7, s5, 0
s_mul_i32 s99, s58, 0xa000
s_add_u32 s99, s99, s51
s_add_u32 s99, s99, 0x400
s_add_u32 s8, s20, s99
s_addc_u32 s9, s21, 0
v_bfe_u32 v8, v0, 4, 1
v_mul_u32_u24 v8, 0x5000, v8
global_load_dwordx4 v[166:169], v3, s[38:39]
v_and_b32 v9, 15, v0
v_lshl_add_u32 v204, v9, 4, v8
v_lshrrev_b32 v9, 5, v0
v_lshl_add_u32 v203, v9, 8, v204
s_and_b32 s51, s29, 1
s_lshl_b32 s51, s51, 10
s_add_u32 s51, s51, 0x23000
s_lshl_b32 s99, s58, 3
s_add_u32 s99, s99, s51
v_add_u32 v214, s99, v4
s_lshl_b32 s99, s58, 9
global_load_dwordx4 v[170:173], v3, s[38:39] offset:768
s_add_u32 s38, s38, 0x1600
s_addc_u32 s39, s39, 0
s_add_u32 s99, s99, s51
v_and_b32 v9, 0x1f0, v4
v_add_u32 v215, s99, v9
v_bfe_u32 v8, v0, 1, 2
v_mul_u32_u24 v8, 0x500, v8
v_and_b32 v9, 1, v0
v_lshl_add_u32 v8, v9, 9, v8
s_mul_i32 s51, s57, 0x5000
s_mul_i32 s58, s29, 0x1400
s_add_u32 s51, s51, s58
s_add_u32 s51, s51, 0xd2e000
global_load_dwordx4 v[174:177], v1, s[38:39]
v_add_u32 v207, s51, v8
s_mul_i32 s51, s29, 0x2a00
s_add_u32 s51, s51, 0x1b0d0
v_mov_b32 v202, s51
v_mov_b32 v8, 0
ds_write_b32 v202, v8
ds_write_b32 v202, v8 offset:4
s_add_u32 s51, s54, 1
s_and_b32 s51, s51, 3
s_add_u32 s51, s51, s57
s_mul_i32 s51, s51, 20
global_load_dwordx2 v[178:179], v2, s[38:39]
s_mul_i32 s58, s29, 5
s_add_u32 s51, s51, s58
s_lshl_b32 s51, s51, 10
s_add_u32 s51, s51, 0xd24800
s_add_u32 s10, s20, s51
s_addc_u32 s11, s21, 0
s_add_u32 s51, s54, 2
s_and_b32 s51, s51, 3
s_add_u32 s51, s51, s57
s_mul_i32 s51, s51, 20
s_mul_i32 s58, s29, 5
s_add_u32 s51, s51, s58
global_load_dwordx4 v[180:183], v3, s[38:39]
s_lshl_b32 s51, s51, 10
s_add_u32 s51, s51, 0xd24800
s_add_u32 s12, s20, s51
s_addc_u32 s13, s21, 0
s_add_u32 s51, s54, 3
s_and_b32 s51, s51, 3
s_add_u32 s51, s51, s57
s_mul_i32 s51, s51, 20
s_mul_i32 s58, s29, 5
s_add_u32 s51, s51, s58
s_lshl_b32 s51, s51, 10
s_add_u32 s51, s51, 0xd24800
global_load_dwordx4 v[184:187], v3, s[38:39] offset:768
s_add_u32 s38, s38, 0x1600
s_addc_u32 s39, s39, 0
s_add_u32 s14, s20, s51
s_addc_u32 s15, s21, 0
s_add_u32 s51, s54, 4
s_and_b32 s51, s51, 3
s_add_u32 s51, s51, s57
s_mul_i32 s51, s51, 20
s_mul_i32 s58, s29, 5
s_add_u32 s51, s51, s58
s_lshl_b32 s51, s51, 10
s_add_u32 s51, s51, 0xd24800
s_add_u32 s16, s20, s51
s_addc_u32 s17, s21, 0
s_waitcnt lgkmcnt(0)
s_barrier
; ==== attention: 32 key tiles x 4 query tiles per wave. S^T = K Q^T (32x32x16 + 32x32x8), p = 2^s in place, O^T += V^T p (sparse MFMA picks the upper halves of the f32 p).
;      between the MFMAs: K/V prefetch, weight prefetch (W1 -> LDS, Wo and W2 -> registers), preset of the hand-off buffers
s_waitcnt vmcnt(18)
v_mfma_f32_32x32x16_bf16 v[98:113], v[146:149], v[10:13], 0
s_waitcnt vmcnt(16)
v_mfma_f32_32x32x8_bf16 v[98:113], v[150:151], v[14:15], v[98:113]
s_waitcnt vmcnt(12)
v_mfma_f32_32x32x16_bf16 v[114:129], v[146:149], v[16:19], 0
global_load_dwordx4 v[188:191], v1, s[38:39]
s_nop 8
v_exp_f32 v98, v98
v_exp_f32 v99, v99
v_exp_f32 v100, v100
v_exp_f32 v101, v101
v_mfma_f32_32x32x8_bf16 v[114:129], v[150:151], v[20:21], v[114:129]
v_exp_f32 v102, v102
v_exp_f32 v103, v103
v_exp_f32 v104, v104
v_exp_f32 v105, v105
v_exp_f32 v106, v106
v_exp_f32 v107, v107
v_exp_f32 v108, v108
v_exp_f32 v109, v109
s_waitcnt vmcnt(12)
v_smfmac_f32_32x32x32_bf16 v[34:49], v[152:155], v[98:105], v5
global_load_dwordx2 v[192:193], v2, s[38:39]
s_mul_i32 s48, s29, 0x6400
s_min_u32 s48, s48, 0x12400
s_add_u32 s60, s52, s48
s_addc_u32 s61, s53, 0
s_mov_b32 m0, s48
v_exp_f32 v110, v110
v_exp_f32 v111, v111
v_exp_f32 v112, v112
v_exp_f32 v113, v113
s_waitcnt vmcnt(12)
v_mfma_f32_32x32x16_bf16 v[130:145], v[146:149], v[22:25], 0
global_load_lds_dwordx4 v4, s[60:61] offset:0
v_exp_f32 v114, v114
v_exp_f32 v115, v115
v_exp_f32 v116, v116
v_exp_f32 v117, v117
v_mfma_f32_32x32x8_bf16 v[130:145], v[150:151], v[26:27], v[130:145]
v_exp_f32 v118, v118
v_exp_f32 v119, v119
v_exp_f32 v120, v120
v_exp_f32 v121, v121
s_waitcnt vmcnt(12)
v_smfmac_f32_32x32x32_bf16 v[34:49], v[156:159], v[106:113], v5
global_load_lds_dwordx4 v4, s[60:61] offset:1024
v_exp_f32 v122, v122
v_exp_f32 v123, v123
v_exp_f32 v124, v124
v_exp_f32 v125, v125
v_smfmac_f32_32x32x32_bf16 v[50:65], v[152:155], v[114:121], v5
global_load_dwordx4 v[194:197], v3, s[38:39]
v_exp_f32 v126, v126
v_exp_f32 v127, v127
v_exp_f32 v128, v128
v_exp_f32 v129, v129
s_waitcnt vmcnt(13)
v_mfma_f32_32x32x16_bf16 v[98:113], v[146:149], v[28:31], 0
global_load_dwordx4 v[198:201], v3, s[38:39] offset:768
s_add_u32 s38, s38, 0x1600
s_addc_u32 s39, s39, 0
v_exp_f32 v130, v130
v_exp_f32 v131, v131
v_exp_f32 v132, v132
v_exp_f32 v133, v133
v_mfma_f32_32x32x8_bf16 v[98:113], v[150:151], v[32:33], v[98:113]
v_exp_f32 v134, v134
v_exp_f32 v135, v135
v_exp_f32 v136, v136
v_exp_f32 v137, v137
v_smfmac_f32_32x32x32_bf16 v[50:65], v[156:159], v[122:129], v5
v_exp_f32 v138, v138
v_exp_f32 v139, v139
v_exp_f32 v140, v140
v_exp_f32 v141, v141
v_smfmac_f32_32x32x32_bf16 v[66:81], v[152:155], v[130:137], v5
s_waitcnt vmcnt(10)
v_exp_f32 v142, v142
v_exp_f32 v143, v143
v_exp_f32 v144, v144
v_exp_f32 v145, v145
v_mfma_f32_32x32x16_bf16 v[114:129], v[160:163], v[10:13], 0
global_load_lds_dwordx4 v4, s[60:61] offset:2048
v_exp_f32 v98, v98
v_exp_f32 v99, v99
v_exp_f32 v100, v100
v_exp_f32 v101, v101
v_mfma_f32_32x32x8_bf16 v[114:129], v[164:165], v[14:15], v[114:129]
v_exp_f32 v102, v102
v_exp_f32 v103, v103
v_exp_f32 v104, v104
v_exp_f32 v105, v105
v_smfmac_f32_32x32x32_bf16 v[66:81], v[156:159], v[138:145], v5
v_exp_f32 v106, v106
v_exp_f32 v107, v107
v_exp_f32 v108, v108
v_exp_f32 v109, v109
v_smfmac_f32_32x32x32_bf16 v[82:97], v[152:155], v[98:105], v5
global_load_lds_dwordx4 v4, s[60:61] offset:3072
v_exp_f32 v110, v110
v_exp_f32 v111, v111
v_exp_f32 v112, v112
v_exp_f32 v113, v113
v_mfma_f32_32x32x16_bf16 v[130:145], v[160:163], v[16:19], 0
)ASM"
R"ASM(global_load_dwordx4 v[146:149], v1, s[38:39]
v_exp_f32 v114, v114
v_exp_f32 v115, v115
v_exp_f32 v116, v116
v_exp_f32 v117, v117
v_mfma_f32_32x32x8_bf16 v[130:145], v[164:165], v[20:21], v[130:145]
v_exp_f32 v118, v118
v_exp_f32 v119, v119
v_exp_f32 v120, v120
v_exp_f32 v121, v121
v_smfmac_f32_32x32x32_bf16 v[82:97], v[156:159], v[106:113], v5
v_exp_f32 v122, v122
v_exp_f32 v123, v123
v_exp_f32 v124, v124
v_exp_f32 v125, v125
v_smfmac_f32_32x32x32_bf16 v[34:49], v[166:169], v[114:121], v5
global_load_dwordx2 v[150:151], v2, s[38:39]
s_add_u32 s60, s60, 0x1000
s_addc_u32 s61, s61, 0
s_add_u32 m0, s48, 0x1000
v_exp_f32 v126, v126
v_exp_f32 v127, v127
v_exp_f32 v128, v128
v_exp_f32 v129, v129
v_mfma_f32_32x32x16_bf16 v[98:113], v[160:163], v[22:25], 0
v_exp_f32 v130, v130
v_exp_f32 v131, v131
v_exp_f32 v132, v132
v_exp_f32 v133, v133
v_mfma_f32_32x32x8_bf16 v[98:113], v[164:165], v[26:27], v[98:113]
v_exp_f32 v134, v134
v_exp_f32 v135, v135
v_exp_f32 v136, v136
v_exp_f32 v137, v137
v_smfmac_f32_32x32x32_bf16 v[34:49], v[170:173], v[122:129], v5
global_load_lds_dwordx4 v4, s[60:61] offset:0
v_exp_f32 v138, v138
v_exp_f32 v139, v139
v_exp_f32 v140, v140
v_exp_f32 v141, v141
v_smfmac_f32_32x32x32_bf16 v[50:65], v[166:169], v[130:137], v5
global_load_dwordx4 v[152:155], v3, s[38:39]
v_exp_f32 v142, v142
v_exp_f32 v143, v143
v_exp_f32 v144, v144
v_exp_f32 v145, v145
v_mfma_f32_32x32x16_bf16 v[114:129], v[160:163], v[28:31], 0
global_load_dwordx4 v[156:159], v3, s[38:39] offset:768
s_add_u32 s38, s38, 0x1600
s_addc_u32 s39, s39, 0
v_exp_f32 v98, v98
v_exp_f32 v99, v99
v_exp_f32 v100, v100
v_exp_f32 v101, v101
v_mfma_f32_32x32x8_bf16 v[114:129], v[164:165], v[32:33], v[114:129]
v_exp_f32 v102, v102
v_exp_f32 v103, v103
v_exp_f32 v104, v104
v_exp_f32 v105, v105
v_smfmac_f32_32x32x32_bf16 v[50:65], v[170:173], v[138:145], v5
global_load_lds_dwordx4 v4, s[60:61] offset:1024
v_exp_f32 v106, v106
v_exp_f32 v107, v107
v_exp_f32 v108, v108
v_exp_f32 v109, v109
v_smfmac_f32_32x32x32_bf16 v[66:81], v[166:169], v[98:105], v5
s_waitcnt vmcnt(14)
v_exp_f32 v110, v110
v_exp_f32 v111, v111
v_exp_f32 v112, v112
v_exp_f32 v113, v113
v_mfma_f32_32x32x16_bf16 v[130:145], v[174:177], v[10:13], 0
v_exp_f32 v114, v114
v_exp_f32 v115, v115
v_exp_f32 v116, v116
v_exp_f32 v117, v117
v_mfma_f32_32x32x8_bf16 v[130:145], v[178:179], v[14:15], v[130:145]
v_exp_f32 v118, v118
v_exp_f32 v119, v119
v_exp_f32 v120, v120
v_exp_f32 v121, v121
v_smfmac_f32_32x32x32_bf16 v[66:81], v[170:173], v[106:113], v5
global_load_lds_dwordx4 v4, s[60:61] offset:2048
v_exp_f32 v122, v122
v_exp_f32 v123, v123
v_exp_f32 v124, v124
v_exp_f32 v125, v125
v_smfmac_f32_32x32x32_bf16 v[82:97], v[166:169], v[114:121], v5
v_exp_f32 v126, v126
v_exp_f32 v127, v127
v_exp_f32 v128, v128
v_exp_f32 v129, v129
v_mfma_f32_32x32x16_bf16 v[98:113], v[174:177], v[16:19], 0
global_load_dwordx4 v[160:163], v1, s[38:39]
v_exp_f32 v130, v130
v_exp_f32 v131, v131
v_exp_f32 v132, v132
v_exp_f32 v133, v133
v_mfma_f32_32x32x8_bf16 v[98:113], v[178:179], v[20:21], v[98:113]
v_exp_f32 v134, v134
v_exp_f32 v135, v135
v_exp_f32 v136, v136
v_exp_f32 v137, v137
v_smfmac_f32_32x32x32_bf16 v[82:97], v[170:173], v[122:129], v5
v_exp_f32 v138, v138
v_exp_f32 v139, v139
v_exp_f32 v140, v140
v_exp_f32 v141, v141
v_smfmac_f32_32x32x32_bf16 v[34:49], v[180:183], v[130:137], v5
global_load_dwordx2 v[164:165], v2, s[38:39]
v_exp_f32 v142, v142
v_exp_f32 v143, v143
v_exp_f32 v144, v144
v_exp_f32 v145, v145
v_mfma_f32_32x32x16_bf16 v[114:129], v[174:177], v[22:25], 0
global_load_lds_dwordx4 v4, s[60:61] offset:3072
v_exp_f32 v98, v98
v_exp_f32 v99, v99
v_exp_f32 v100, v100
v_exp_f32 v101, v101
v_mfma_f32_32x32x8_bf16 v[114:129], v[178:179], v[26:27], v[114:129]
v_exp_f32 v102, v102
v_exp_f32 v103, v103
v_exp_f32 v104, v104
v_exp_f32 v105, v105
v_smfmac_f32_32x32x32_bf16 v[34:49], v[184:187], v[138:145], v5
v_exp_f32 v106, v106
v_exp_f32 v107, v107
v_exp_f32 v108, v108
v_exp_f32 v109, v109
v_smfmac_f32_32x32x32_bf16 v[50:65], v[180:183], v[98:105], v5
global_load_dwordx4 v[166:169], v3, s[38:39]
v_exp_f32 v110, v110
v_exp_f32 v111, v111
v_exp_f32 v112, v112
v_exp_f32 v113, v113
v_mfma_f32_32x32x16_bf16 v[130:145], v[174:177], v[28:31], 0
global_load_dwordx4 v[170:173], v3, s[38:39] offset:768
s_add_u32 s38, s38, 0x1600
s_addc_u32 s39, s39, 0
s_add_u32 s60, s60, 0x1000
s_addc_u32 s61, s61, 0
s_add_u32 m0, s48, 0x2000
v_exp_f32 v114, v114
v_exp_f32 v115, v115
v_exp_f32 v116, v116
v_exp_f32 v117, v117
v_mfma_f32_32x32x8_bf16 v[130:145], v[178:179], v[32:33], v[130:145]
v_exp_f32 v118, v118
v_exp_f32 v119, v119
v_exp_f32 v120, v120
v_exp_f32 v121, v121
v_smfmac_f32_32x32x32_bf16 v[50:65], v[184:187], v[106:113], v5
global_load_lds_dwordx4 v4, s[60:61] offset:0
v_exp_f32 v122, v122
v_exp_f32 v123, v123
v_exp_f32 v124, v124
v_exp_f32 v125, v125
v_smfmac_f32_32x32x32_bf16 v[66:81], v[180:183], v[114:121], v5
s_waitcnt vmcnt(15)
v_exp_f32 v126, v126
v_exp_f32 v127, v127
v_exp_f32 v128, v128
v_exp_f32 v129, v129
v_mfma_f32_32x32x16_bf16 v[98:113], v[188:191], v[10:13], 0
v_exp_f32 v130, v130
v_exp_f32 v131, v131
v_exp_f32 v132, v132
v_exp_f32 v133, v133
v_mfma_f32_32x32x8_bf16 v[98:113], v[192:193], v[14:15], v[98:113]
v_exp_f32 v134, v134
v_exp_f32 v135, v135
v_exp_f32 v136, v136
v_exp_f32 v137, v137
v_smfmac_f32_32x32x32_bf16 v[66:81], v[184:187], v[122:129], v5
global_load_lds_dwordx4 v4, s[60:61] offset:1024
v_exp_f32 v138, v138
v_exp_f32 v139, v139
v_exp_f32 v140, v140
v_exp_f32 v141, v141
v_smfmac_f32_32x32x32_bf16 v[82:97], v[180:183], v[130:137], v5
global_load_lds_dwordx4 v4, s[60:61] offset:2048
v_exp_f32 v142, v142
v_exp_f32 v143, v143
v_exp_f32 v144, v144
v_exp_f32 v145, v145
v_mfma_f32_32x32x16_bf16 v[114:129], v[188:191], v[16:19], 0
global_load_dwordx4 v[174:177], v1, s[38:39]
v_exp_f32 v98, v98
v_exp_f32 v99, v99
v_exp_f32 v100, v100
v_exp_f32 v101, v101
v_mfma_f32_32x32x8_bf16 v[114:129], v[192:193], v[20:21], v[114:129]
v_exp_f32 v102, v102
v_exp_f32 v103, v103
v_exp_f32 v104, v104
v_exp_f32 v105, v105
v_smfmac_f32_32x32x32_bf16 v[82:97], v[184:187], v[138:145], v5
v_exp_f32 v106, v106
v_exp_f32 v107, v107
)ASM"
R"ASM(v_exp_f32 v108, v108
v_exp_f32 v109, v109
v_smfmac_f32_32x32x32_bf16 v[34:49], v[194:197], v[98:105], v5
global_load_dwordx2 v[178:179], v2, s[38:39]
v_exp_f32 v110, v110
v_exp_f32 v111, v111
v_exp_f32 v112, v112
v_exp_f32 v113, v113
v_mfma_f32_32x32x16_bf16 v[130:145], v[188:191], v[22:25], 0
v_exp_f32 v114, v114
v_exp_f32 v115, v115
v_exp_f32 v116, v116
v_exp_f32 v117, v117
v_mfma_f32_32x32x8_bf16 v[130:145], v[192:193], v[26:27], v[130:145]
v_exp_f32 v118, v118
v_exp_f32 v119, v119
v_exp_f32 v120, v120
v_exp_f32 v121, v121
v_smfmac_f32_32x32x32_bf16 v[34:49], v[198:201], v[106:113], v5
global_load_lds_dwordx4 v4, s[60:61] offset:3072
v_exp_f32 v122, v122
v_exp_f32 v123, v123
v_exp_f32 v124, v124
v_exp_f32 v125, v125
v_smfmac_f32_32x32x32_bf16 v[50:65], v[194:197], v[114:121], v5
global_load_dwordx4 v[180:183], v3, s[38:39]
v_exp_f32 v126, v126
v_exp_f32 v127, v127
v_exp_f32 v128, v128
v_exp_f32 v129, v129
v_mfma_f32_32x32x16_bf16 v[98:113], v[188:191], v[28:31], 0
global_load_dwordx4 v[184:187], v3, s[38:39] offset:768
s_add_u32 s38, s38, 0x1600
s_addc_u32 s39, s39, 0
s_add_u32 s60, s60, 0x1000
s_addc_u32 s61, s61, 0
s_add_u32 m0, s48, 0x3000
v_exp_f32 v130, v130
v_exp_f32 v131, v131
v_exp_f32 v132, v132
v_exp_f32 v133, v133
v_mfma_f32_32x32x8_bf16 v[98:113], v[192:193], v[32:33], v[98:113]
v_exp_f32 v134, v134
v_exp_f32 v135, v135
v_exp_f32 v136, v136
v_exp_f32 v137, v137
v_smfmac_f32_32x32x32_bf16 v[50:65], v[198:201], v[122:129], v5
v_exp_f32 v138, v138
v_exp_f32 v139, v139
v_exp_f32 v140, v140
v_exp_f32 v141, v141
v_smfmac_f32_32x32x32_bf16 v[66:81], v[194:197], v[130:137], v5
s_waitcnt vmcnt(15)
v_exp_f32 v142, v142
v_exp_f32 v143, v143
v_exp_f32 v144, v144
v_exp_f32 v145, v145
v_mfma_f32_32x32x16_bf16 v[114:129], v[146:149], v[10:13], 0
global_load_lds_dwordx4 v4, s[60:61] offset:0
v_exp_f32 v98, v98
v_exp_f32 v99, v99
v_exp_f32 v100, v100
v_exp_f32 v101, v101
v_mfma_f32_32x32x8_bf16 v[114:129], v[150:151], v[14:15], v[114:129]
v_exp_f32 v102, v102
v_exp_f32 v103, v103
v_exp_f32 v104, v104
v_exp_f32 v105, v105
v_smfmac_f32_32x32x32_bf16 v[66:81], v[198:201], v[138:145], v5
v_exp_f32 v106, v106
v_exp_f32 v107, v107
v_exp_f32 v108, v108
v_exp_f32 v109, v109
v_smfmac_f32_32x32x32_bf16 v[82:97], v[194:197], v[98:105], v5
global_load_lds_dwordx4 v4, s[60:61] offset:1024
v_exp_f32 v110, v110
v_exp_f32 v111, v111
v_exp_f32 v112, v112
v_exp_f32 v113, v113
v_mfma_f32_32x32x16_bf16 v[130:145], v[146:149], v[16:19], 0
global_load_dwordx4 v[188:191], v1, s[38:39]
v_exp_f32 v114, v114
v_exp_f32 v115, v115
v_exp_f32 v116, v116
v_exp_f32 v117, v117
v_mfma_f32_32x32x8_bf16 v[130:145], v[150:151], v[20:21], v[130:145]
v_exp_f32 v118, v118
v_exp_f32 v119, v119
v_exp_f32 v120, v120
v_exp_f32 v121, v121
v_smfmac_f32_32x32x32_bf16 v[82:97], v[198:201], v[106:113], v5
v_exp_f32 v122, v122
v_exp_f32 v123, v123
v_exp_f32 v124, v124
v_exp_f32 v125, v125
v_smfmac_f32_32x32x32_bf16 v[34:49], v[152:155], v[114:121], v5
global_load_dwordx2 v[192:193], v2, s[38:39]
v_exp_f32 v126, v126
v_exp_f32 v127, v127
v_exp_f32 v128, v128
v_exp_f32 v129, v129
v_mfma_f32_32x32x16_bf16 v[98:113], v[146:149], v[22:25], 0
v_exp_f32 v130, v130
v_exp_f32 v131, v131
v_exp_f32 v132, v132
v_exp_f32 v133, v133
v_mfma_f32_32x32x8_bf16 v[98:113], v[150:151], v[26:27], v[98:113]
v_exp_f32 v134, v134
v_exp_f32 v135, v135
v_exp_f32 v136, v136
v_exp_f32 v137, v137
v_smfmac_f32_32x32x32_bf16 v[34:49], v[156:159], v[122:129], v5
global_load_lds_dwordx4 v4, s[60:61] offset:2048
v_exp_f32 v138, v138
v_exp_f32 v139, v139
v_exp_f32 v140, v140
v_exp_f32 v141, v141
v_smfmac_f32_32x32x32_bf16 v[50:65], v[152:155], v[130:137], v5
global_load_dwordx4 v[194:197], v3, s[38:39]
v_exp_f32 v142, v142
v_exp_f32 v143, v143
v_exp_f32 v144, v144
v_exp_f32 v145, v145
v_mfma_f32_32x32x16_bf16 v[114:129], v[146:149], v[28:31], 0
global_load_dwordx4 v[198:201], v3, s[38:39] offset:768
s_add_u32 s38, s38, 0x1600
s_addc_u32 s39, s39, 0
v_exp_f32 v98, v98
v_exp_f32 v99, v99
v_exp_f32 v100, v100
v_exp_f32 v101, v101
v_mfma_f32_32x32x8_bf16 v[114:129], v[150:151], v[32:33], v[114:129]
v_exp_f32 v102, v102
v_exp_f32 v103, v103
v_exp_f32 v104, v104
v_exp_f32 v105, v105
v_smfmac_f32_32x32x32_bf16 v[50:65], v[156:159], v[138:145], v5
global_load_lds_dwordx4 v4, s[60:61] offset:3072
s_add_u32 s60, s60, 0x1000
s_addc_u32 s61, s61, 0
s_add_u32 m0, s48, 0x4000
v_exp_f32 v106, v106
v_exp_f32 v107, v107
v_exp_f32 v108, v108
v_exp_f32 v109, v109
v_smfmac_f32_32x32x32_bf16 v[66:81], v[152:155], v[98:105], v5
s_waitcnt vmcnt(16)
v_exp_f32 v110, v110
v_exp_f32 v111, v111
v_exp_f32 v112, v112
v_exp_f32 v113, v113
v_mfma_f32_32x32x16_bf16 v[130:145], v[160:163], v[10:13], 0
v_exp_f32 v114, v114
v_exp_f32 v115, v115
v_exp_f32 v116, v116
v_exp_f32 v117, v117
v_mfma_f32_32x32x8_bf16 v[130:145], v[164:165], v[14:15], v[130:145]
v_exp_f32 v118, v118
v_exp_f32 v119, v119
v_exp_f32 v120, v120
v_exp_f32 v121, v121
v_smfmac_f32_32x32x32_bf16 v[66:81], v[156:159], v[106:113], v5
global_load_lds_dwordx4 v4, s[60:61] offset:0
v_exp_f32 v122, v122
v_exp_f32 v123, v123
v_exp_f32 v124, v124
v_exp_f32 v125, v125
v_smfmac_f32_32x32x32_bf16 v[82:97], v[152:155], v[114:121], v5
v_exp_f32 v126, v126
v_exp_f32 v127, v127
v_exp_f32 v128, v128
v_exp_f32 v129, v129
v_mfma_f32_32x32x16_bf16 v[98:113], v[160:163], v[16:19], 0
global_load_dwordx4 v[146:149], v1, s[38:39]
v_exp_f32 v130, v130
v_exp_f32 v131, v131
v_exp_f32 v132, v132
v_exp_f32 v133, v133
v_mfma_f32_32x32x8_bf16 v[98:113], v[164:165], v[20:21], v[98:113]
v_exp_f32 v134, v134
v_exp_f32 v135, v135
v_exp_f32 v136, v136
v_exp_f32 v137, v137
v_smfmac_f32_32x32x32_bf16 v[82:97], v[156:159], v[122:129], v5
v_exp_f32 v138, v138
v_exp_f32 v139, v139
v_exp_f32 v140, v140
v_exp_f32 v141, v141
v_smfmac_f32_32x32x32_bf16 v[34:49], v[166:169], v[130:137], v5
global_load_dwordx2 v[150:151], v2, s[38:39]
v_exp_f32 v142, v142
v_exp_f32 v143, v143
v_exp_f32 v144, v144
v_exp_f32 v145, v145
v_mfma_f32_32x32x16_bf16 v[114:129], v[160:163], v[22:25], 0
global_load_lds_dwordx4 v4, s[60:61] offset:1024
v_exp_f32 v98, v98
v_exp_f32 v99, v99
v_exp_f32 v100, v100
v_exp_f32 v101, v101
)ASM"
R"ASM(v_mfma_f32_32x32x8_bf16 v[114:129], v[164:165], v[26:27], v[114:129]
v_exp_f32 v102, v102
v_exp_f32 v103, v103
v_exp_f32 v104, v104
v_exp_f32 v105, v105
v_smfmac_f32_32x32x32_bf16 v[34:49], v[170:173], v[138:145], v5
v_exp_f32 v106, v106
v_exp_f32 v107, v107
v_exp_f32 v108, v108
v_exp_f32 v109, v109
v_smfmac_f32_32x32x32_bf16 v[50:65], v[166:169], v[98:105], v5
global_load_dwordx4 v[152:155], v3, s[38:39]
v_exp_f32 v110, v110
v_exp_f32 v111, v111
v_exp_f32 v112, v112
v_exp_f32 v113, v113
v_mfma_f32_32x32x16_bf16 v[130:145], v[160:163], v[28:31], 0
global_load_dwordx4 v[156:159], v3, s[38:39] offset:768
s_add_u32 s38, s38, 0x1600
s_addc_u32 s39, s39, 0
v_exp_f32 v114, v114
v_exp_f32 v115, v115
v_exp_f32 v116, v116
v_exp_f32 v117, v117
v_mfma_f32_32x32x8_bf16 v[130:145], v[164:165], v[32:33], v[130:145]
v_exp_f32 v118, v118
v_exp_f32 v119, v119
v_exp_f32 v120, v120
v_exp_f32 v121, v121
v_smfmac_f32_32x32x32_bf16 v[50:65], v[170:173], v[106:113], v5
global_load_lds_dwordx4 v4, s[60:61] offset:2048
v_exp_f32 v122, v122
v_exp_f32 v123, v123
v_exp_f32 v124, v124
v_exp_f32 v125, v125
v_smfmac_f32_32x32x32_bf16 v[66:81], v[166:169], v[114:121], v5
s_waitcnt vmcnt(15)
v_exp_f32 v126, v126
v_exp_f32 v127, v127
v_exp_f32 v128, v128
v_exp_f32 v129, v129
v_mfma_f32_32x32x16_bf16 v[98:113], v[174:177], v[10:13], 0
global_load_lds_dwordx4 v4, s[60:61] offset:3072
s_add_u32 s60, s60, 0x1000
s_addc_u32 s61, s61, 0
s_add_u32 m0, s48, 0x5000
v_exp_f32 v130, v130
v_exp_f32 v131, v131
v_exp_f32 v132, v132
v_exp_f32 v133, v133
v_mfma_f32_32x32x8_bf16 v[98:113], v[178:179], v[14:15], v[98:113]
v_exp_f32 v134, v134
v_exp_f32 v135, v135
v_exp_f32 v136, v136
v_exp_f32 v137, v137
v_smfmac_f32_32x32x32_bf16 v[66:81], v[170:173], v[122:129], v5
v_exp_f32 v138, v138
v_exp_f32 v139, v139
v_exp_f32 v140, v140
v_exp_f32 v141, v141
v_smfmac_f32_32x32x32_bf16 v[82:97], v[166:169], v[130:137], v5
global_load_lds_dwordx4 v4, s[60:61] offset:0
v_exp_f32 v142, v142
v_exp_f32 v143, v143
v_exp_f32 v144, v144
v_exp_f32 v145, v145
v_mfma_f32_32x32x16_bf16 v[114:129], v[174:177], v[16:19], 0
global_load_dwordx4 v[160:163], v1, s[38:39]
v_exp_f32 v98, v98
v_exp_f32 v99, v99
v_exp_f32 v100, v100
v_exp_f32 v101, v101
v_mfma_f32_32x32x8_bf16 v[114:129], v[178:179], v[20:21], v[114:129]
v_exp_f32 v102, v102
v_exp_f32 v103, v103
v_exp_f32 v104, v104
v_exp_f32 v105, v105
v_smfmac_f32_32x32x32_bf16 v[82:97], v[170:173], v[138:145], v5
v_exp_f32 v106, v106
v_exp_f32 v107, v107
v_exp_f32 v108, v108
v_exp_f32 v109, v109
v_smfmac_f32_32x32x32_bf16 v[34:49], v[180:183], v[98:105], v5
global_load_dwordx2 v[164:165], v2, s[38:39]
s_lshl_b32 s35, s28, 2
s_add_u32 s35, s35, s29
s_mul_i32 s35, s35, 12
s_min_u32 s35, s35, 0x2f34
s_lshl_b32 s35, s35, 10
s_add_u32 s35, s35, 0xd25000
s_add_u32 s18, s20, s35
s_addc_u32 s19, s21, 0
v_exp_f32 v110, v110
v_exp_f32 v111, v111
v_exp_f32 v112, v112
v_exp_f32 v113, v113
v_mfma_f32_32x32x16_bf16 v[130:145], v[174:177], v[22:25], 0
v_exp_f32 v114, v114
v_exp_f32 v115, v115
v_exp_f32 v116, v116
v_exp_f32 v117, v117
v_mfma_f32_32x32x8_bf16 v[130:145], v[178:179], v[26:27], v[130:145]
v_exp_f32 v118, v118
v_exp_f32 v119, v119
v_exp_f32 v120, v120
v_exp_f32 v121, v121
v_smfmac_f32_32x32x32_bf16 v[34:49], v[184:187], v[106:113], v5
global_store_dwordx4 v4, v[208:211], s[18:19] offset:-4096 sc1
v_exp_f32 v122, v122
v_exp_f32 v123, v123
v_exp_f32 v124, v124
v_exp_f32 v125, v125
v_smfmac_f32_32x32x32_bf16 v[50:65], v[180:183], v[114:121], v5
global_load_dwordx4 v[166:169], v3, s[38:39]
v_exp_f32 v126, v126
v_exp_f32 v127, v127
v_exp_f32 v128, v128
v_exp_f32 v129, v129
v_mfma_f32_32x32x16_bf16 v[98:113], v[174:177], v[28:31], 0
global_load_dwordx4 v[170:173], v3, s[38:39] offset:768
s_add_u32 s38, s38, 0x1600
s_addc_u32 s39, s39, 0
v_exp_f32 v130, v130
v_exp_f32 v131, v131
v_exp_f32 v132, v132
v_exp_f32 v133, v133
v_mfma_f32_32x32x8_bf16 v[98:113], v[178:179], v[32:33], v[98:113]
v_exp_f32 v134, v134
v_exp_f32 v135, v135
v_exp_f32 v136, v136
v_exp_f32 v137, v137
v_smfmac_f32_32x32x32_bf16 v[50:65], v[184:187], v[122:129], v5
global_load_lds_dwordx4 v4, s[60:61] offset:1024
v_exp_f32 v138, v138
v_exp_f32 v139, v139
v_exp_f32 v140, v140
v_exp_f32 v141, v141
v_smfmac_f32_32x32x32_bf16 v[66:81], v[180:183], v[130:137], v5
s_waitcnt vmcnt(16)
v_exp_f32 v142, v142
v_exp_f32 v143, v143
v_exp_f32 v144, v144
v_exp_f32 v145, v145
v_mfma_f32_32x32x16_bf16 v[114:129], v[188:191], v[10:13], 0
v_exp_f32 v98, v98
v_exp_f32 v99, v99
v_exp_f32 v100, v100
v_exp_f32 v101, v101
v_mfma_f32_32x32x8_bf16 v[114:129], v[192:193], v[14:15], v[114:129]
v_exp_f32 v102, v102
v_exp_f32 v103, v103
v_exp_f32 v104, v104
v_exp_f32 v105, v105
v_smfmac_f32_32x32x32_bf16 v[66:81], v[184:187], v[138:145], v5
global_load_lds_dwordx4 v4, s[60:61] offset:2048
v_exp_f32 v106, v106
v_exp_f32 v107, v107
v_exp_f32 v108, v108
v_exp_f32 v109, v109
v_smfmac_f32_32x32x32_bf16 v[82:97], v[180:183], v[98:105], v5
v_exp_f32 v110, v110
v_exp_f32 v111, v111
v_exp_f32 v112, v112
v_exp_f32 v113, v113
v_mfma_f32_32x32x16_bf16 v[130:145], v[188:191], v[16:19], 0
global_load_dwordx4 v[174:177], v1, s[38:39]
v_exp_f32 v114, v114
v_exp_f32 v115, v115
v_exp_f32 v116, v116
v_exp_f32 v117, v117
v_mfma_f32_32x32x8_bf16 v[130:145], v[192:193], v[20:21], v[130:145]
v_exp_f32 v118, v118
v_exp_f32 v119, v119
v_exp_f32 v120, v120
v_exp_f32 v121, v121
v_smfmac_f32_32x32x32_bf16 v[82:97], v[184:187], v[106:113], v5
v_exp_f32 v122, v122
v_exp_f32 v123, v123
v_exp_f32 v124, v124
v_exp_f32 v125, v125
v_smfmac_f32_32x32x32_bf16 v[34:49], v[194:197], v[114:121], v5
global_load_dwordx2 v[178:179], v2, s[38:39]
v_exp_f32 v126, v126
v_exp_f32 v127, v127
v_exp_f32 v128, v128
v_exp_f32 v129, v129
v_mfma_f32_32x32x16_bf16 v[98:113], v[188:191], v[22:25], 0
global_load_lds_dwordx4 v4, s[60:61] offset:3072
v_exp_f32 v130, v130
v_exp_f32 v131, v131
v_exp_f32 v132, v132
v_exp_f32 v133, v133
v_mfma_f32_32x32x8_bf16 v[98:113], v[192:193], v[26:27], v[98:113]
v_exp_f32 v134, v134
v_exp_f32 v135, v135
v_exp_f32 v136, v136
v_exp_f32 v137, v137
v_smfmac_f32_32x32x32_bf16 v[34:49], v[198:201], v[122:129], v5
global_store_dwordx4 v4, v[208:211], s[18:19] offset:-3072 sc1
v_exp_f32 v138, v138
)ASM"
R"ASM(v_exp_f32 v139, v139
v_exp_f32 v140, v140
v_exp_f32 v141, v141
v_smfmac_f32_32x32x32_bf16 v[50:65], v[194:197], v[130:137], v5
global_load_dwordx4 v[180:183], v3, s[38:39]
v_exp_f32 v142, v142
v_exp_f32 v143, v143
v_exp_f32 v144, v144
v_exp_f32 v145, v145
v_mfma_f32_32x32x16_bf16 v[114:129], v[188:191], v[28:31], 0
global_load_dwordx4 v[184:187], v3, s[38:39] offset:768
s_add_u32 s38, s38, 0x1600
s_addc_u32 s39, s39, 0
s_add_u32 s60, s60, 0x1000
s_addc_u32 s61, s61, 0
s_add_u32 m0, s48, 0x6000
v_exp_f32 v98, v98
v_exp_f32 v99, v99
v_exp_f32 v100, v100
v_exp_f32 v101, v101
v_mfma_f32_32x32x8_bf16 v[114:129], v[192:193], v[32:33], v[114:129]
v_exp_f32 v102, v102
v_exp_f32 v103, v103
v_exp_f32 v104, v104
v_exp_f32 v105, v105
v_smfmac_f32_32x32x32_bf16 v[50:65], v[198:201], v[138:145], v5
global_load_lds_dwordx4 v4, s[60:61] offset:0
s_mul_i32 s35, s34, 20
s_mul_i32 s36, s29, 5
s_add_u32 s35, s35, s36
s_mul_i32 s35, s35, 0x1400
s_add_u32 s35, s35, 0x810000
s_add_u32 s60, s20, s35
s_addc_u32 s61, s21, 0
s_add_u32 s60, s60, 0x1000
s_addc_u32 s61, s61, 0
v_exp_f32 v106, v106
v_exp_f32 v107, v107
v_exp_f32 v108, v108
v_exp_f32 v109, v109
v_smfmac_f32_32x32x32_bf16 v[66:81], v[194:197], v[98:105], v5
s_waitcnt vmcnt(17)
v_exp_f32 v110, v110
v_exp_f32 v111, v111
v_exp_f32 v112, v112
v_exp_f32 v113, v113
v_mfma_f32_32x32x16_bf16 v[130:145], v[146:149], v[10:13], 0
v_exp_f32 v114, v114
v_exp_f32 v115, v115
v_exp_f32 v116, v116
v_exp_f32 v117, v117
v_mfma_f32_32x32x8_bf16 v[130:145], v[150:151], v[14:15], v[130:145]
v_exp_f32 v118, v118
v_exp_f32 v119, v119
v_exp_f32 v120, v120
v_exp_f32 v121, v121
v_smfmac_f32_32x32x32_bf16 v[66:81], v[198:201], v[106:113], v5
global_load_dwordx4 a[180:183], v4, s[60:61] offset:-4096
v_exp_f32 v122, v122
v_exp_f32 v123, v123
v_exp_f32 v124, v124
v_exp_f32 v125, v125
v_smfmac_f32_32x32x32_bf16 v[82:97], v[194:197], v[114:121], v5
v_exp_f32 v126, v126
v_exp_f32 v127, v127
v_exp_f32 v128, v128
v_exp_f32 v129, v129
v_mfma_f32_32x32x16_bf16 v[98:113], v[146:149], v[16:19], 0
global_load_dwordx4 v[188:191], v1, s[38:39]
v_exp_f32 v130, v130
v_exp_f32 v131, v131
v_exp_f32 v132, v132
v_exp_f32 v133, v133
v_mfma_f32_32x32x8_bf16 v[98:113], v[150:151], v[20:21], v[98:113]
v_exp_f32 v134, v134
v_exp_f32 v135, v135
v_exp_f32 v136, v136
v_exp_f32 v137, v137
v_smfmac_f32_32x32x32_bf16 v[82:97], v[198:201], v[122:129], v5
v_exp_f32 v138, v138
v_exp_f32 v139, v139
v_exp_f32 v140, v140
v_exp_f32 v141, v141
v_smfmac_f32_32x32x32_bf16 v[34:49], v[152:155], v[130:137], v5
global_load_dwordx2 v[192:193], v2, s[38:39]
v_exp_f32 v142, v142
v_exp_f32 v143, v143
v_exp_f32 v144, v144
v_exp_f32 v145, v145
v_mfma_f32_32x32x16_bf16 v[114:129], v[146:149], v[22:25], 0
global_load_dwordx4 a[184:187], v4, s[60:61] offset:-3072
v_exp_f32 v98, v98
v_exp_f32 v99, v99
v_exp_f32 v100, v100
v_exp_f32 v101, v101
v_mfma_f32_32x32x8_bf16 v[114:129], v[150:151], v[26:27], v[114:129]
v_exp_f32 v102, v102
v_exp_f32 v103, v103
v_exp_f32 v104, v104
v_exp_f32 v105, v105
v_smfmac_f32_32x32x32_bf16 v[34:49], v[156:159], v[138:145], v5
global_store_dwordx4 v4, v[208:211], s[18:19] offset:-2048 sc1
v_exp_f32 v106, v106
v_exp_f32 v107, v107
v_exp_f32 v108, v108
v_exp_f32 v109, v109
v_smfmac_f32_32x32x32_bf16 v[50:65], v[152:155], v[98:105], v5
global_load_dwordx4 v[194:197], v3, s[38:39]
v_exp_f32 v110, v110
v_exp_f32 v111, v111
v_exp_f32 v112, v112
v_exp_f32 v113, v113
v_mfma_f32_32x32x16_bf16 v[130:145], v[146:149], v[28:31], 0
global_load_dwordx4 v[198:201], v3, s[38:39] offset:768
s_add_u32 s38, s38, 0x1600
s_addc_u32 s39, s39, 0
v_exp_f32 v114, v114
v_exp_f32 v115, v115
v_exp_f32 v116, v116
v_exp_f32 v117, v117
v_mfma_f32_32x32x8_bf16 v[130:145], v[150:151], v[32:33], v[130:145]
v_exp_f32 v118, v118
v_exp_f32 v119, v119
v_exp_f32 v120, v120
v_exp_f32 v121, v121
v_smfmac_f32_32x32x32_bf16 v[50:65], v[156:159], v[106:113], v5
v_exp_f32 v122, v122
v_exp_f32 v123, v123
v_exp_f32 v124, v124
v_exp_f32 v125, v125
v_smfmac_f32_32x32x32_bf16 v[66:81], v[152:155], v[114:121], v5
s_waitcnt vmcnt(16)
v_exp_f32 v126, v126
v_exp_f32 v127, v127
v_exp_f32 v128, v128
v_exp_f32 v129, v129
v_mfma_f32_32x32x16_bf16 v[98:113], v[160:163], v[10:13], 0
global_load_dwordx4 a[188:191], v4, s[60:61] offset:-2048
v_exp_f32 v130, v130
v_exp_f32 v131, v131
v_exp_f32 v132, v132
v_exp_f32 v133, v133
v_mfma_f32_32x32x8_bf16 v[98:113], v[164:165], v[14:15], v[98:113]
v_exp_f32 v134, v134
v_exp_f32 v135, v135
v_exp_f32 v136, v136
v_exp_f32 v137, v137
v_smfmac_f32_32x32x32_bf16 v[66:81], v[156:159], v[122:129], v5
v_exp_f32 v138, v138
v_exp_f32 v139, v139
v_exp_f32 v140, v140
v_exp_f32 v141, v141
v_smfmac_f32_32x32x32_bf16 v[82:97], v[152:155], v[130:137], v5
global_load_dwordx4 a[192:195], v4, s[60:61] offset:-1024
v_exp_f32 v142, v142
v_exp_f32 v143, v143
v_exp_f32 v144, v144
v_exp_f32 v145, v145
v_mfma_f32_32x32x16_bf16 v[114:129], v[160:163], v[16:19], 0
global_load_dwordx4 v[146:149], v1, s[38:39]
v_exp_f32 v98, v98
v_exp_f32 v99, v99
v_exp_f32 v100, v100
v_exp_f32 v101, v101
v_mfma_f32_32x32x8_bf16 v[114:129], v[164:165], v[20:21], v[114:129]
v_exp_f32 v102, v102
v_exp_f32 v103, v103
v_exp_f32 v104, v104
v_exp_f32 v105, v105
v_smfmac_f32_32x32x32_bf16 v[82:97], v[156:159], v[138:145], v5
v_exp_f32 v106, v106
v_exp_f32 v107, v107
v_exp_f32 v108, v108
v_exp_f32 v109, v109
v_smfmac_f32_32x32x32_bf16 v[34:49], v[166:169], v[98:105], v5
global_load_dwordx2 v[150:151], v2, s[38:39]
v_exp_f32 v110, v110
v_exp_f32 v111, v111
v_exp_f32 v112, v112
v_exp_f32 v113, v113
v_mfma_f32_32x32x16_bf16 v[130:145], v[160:163], v[22:25], 0
global_load_dwordx4 a[196:199], v4, s[60:61] offset:0
v_exp_f32 v114, v114
v_exp_f32 v115, v115
v_exp_f32 v116, v116
v_exp_f32 v117, v117
v_mfma_f32_32x32x8_bf16 v[130:145], v[164:165], v[26:27], v[130:145]
v_exp_f32 v118, v118
v_exp_f32 v119, v119
v_exp_f32 v120, v120
v_exp_f32 v121, v121
v_smfmac_f32_32x32x32_bf16 v[34:49], v[170:173], v[106:113], v5
global_store_dwordx4 v4, v[208:211], s[18:19] offset:-1024 sc1
v_exp_f32 v122, v122
v_exp_f32 v123, v123
v_exp_f32 v124, v124
v_exp_f32 v125, v125
v_smfmac_f32_32x32x32_bf16 v[50:65], v[166:169], v[114:121], v5
global_load_dwordx4 v[152:155], v3, s[38:39]
v_exp_f32 v126, v126
)ASM"
R"ASM(v_exp_f32 v127, v127
v_exp_f32 v128, v128
v_exp_f32 v129, v129
v_mfma_f32_32x32x16_bf16 v[98:113], v[160:163], v[28:31], 0
global_load_dwordx4 v[156:159], v3, s[38:39] offset:768
s_add_u32 s38, s38, 0x1600
s_addc_u32 s39, s39, 0
v_exp_f32 v130, v130
v_exp_f32 v131, v131
v_exp_f32 v132, v132
v_exp_f32 v133, v133
v_mfma_f32_32x32x8_bf16 v[98:113], v[164:165], v[32:33], v[98:113]
v_exp_f32 v134, v134
v_exp_f32 v135, v135
v_exp_f32 v136, v136
v_exp_f32 v137, v137
v_smfmac_f32_32x32x32_bf16 v[50:65], v[170:173], v[122:129], v5
v_exp_f32 v138, v138
v_exp_f32 v139, v139
v_exp_f32 v140, v140
v_exp_f32 v141, v141
v_smfmac_f32_32x32x32_bf16 v[66:81], v[166:169], v[130:137], v5
s_waitcnt vmcnt(16)
v_exp_f32 v142, v142
v_exp_f32 v143, v143
v_exp_f32 v144, v144
v_exp_f32 v145, v145
v_mfma_f32_32x32x16_bf16 v[114:129], v[174:177], v[10:13], 0
global_load_dwordx4 a[204:207], v4, s[60:61] offset:1024
v_exp_f32 v98, v98
v_exp_f32 v99, v99
v_exp_f32 v100, v100
v_exp_f32 v101, v101
v_mfma_f32_32x32x8_bf16 v[114:129], v[178:179], v[14:15], v[114:129]
v_exp_f32 v102, v102
v_exp_f32 v103, v103
v_exp_f32 v104, v104
v_exp_f32 v105, v105
v_smfmac_f32_32x32x32_bf16 v[66:81], v[170:173], v[138:145], v5
v_exp_f32 v106, v106
v_exp_f32 v107, v107
v_exp_f32 v108, v108
v_exp_f32 v109, v109
v_smfmac_f32_32x32x32_bf16 v[82:97], v[166:169], v[98:105], v5
global_load_dwordx4 a[208:211], v4, s[60:61] offset:2048
v_exp_f32 v110, v110
v_exp_f32 v111, v111
v_exp_f32 v112, v112
v_exp_f32 v113, v113
v_mfma_f32_32x32x16_bf16 v[130:145], v[174:177], v[16:19], 0
global_load_dwordx4 v[160:163], v1, s[38:39]
v_exp_f32 v114, v114
v_exp_f32 v115, v115
v_exp_f32 v116, v116
v_exp_f32 v117, v117
v_mfma_f32_32x32x8_bf16 v[130:145], v[178:179], v[20:21], v[130:145]
v_exp_f32 v118, v118
v_exp_f32 v119, v119
v_exp_f32 v120, v120
v_exp_f32 v121, v121
v_smfmac_f32_32x32x32_bf16 v[82:97], v[170:173], v[106:113], v5
v_exp_f32 v122, v122
v_exp_f32 v123, v123
v_exp_f32 v124, v124
v_exp_f32 v125, v125
v_smfmac_f32_32x32x32_bf16 v[34:49], v[180:183], v[114:121], v5
global_load_dwordx2 v[164:165], v2, s[38:39]
v_exp_f32 v126, v126
v_exp_f32 v127, v127
v_exp_f32 v128, v128
v_exp_f32 v129, v129
v_mfma_f32_32x32x16_bf16 v[98:113], v[174:177], v[22:25], 0
v_exp_f32 v130, v130
v_exp_f32 v131, v131
v_exp_f32 v132, v132
v_exp_f32 v133, v133
v_mfma_f32_32x32x8_bf16 v[98:113], v[178:179], v[26:27], v[98:113]
v_exp_f32 v134, v134
v_exp_f32 v135, v135
v_exp_f32 v136, v136
v_exp_f32 v137, v137
v_smfmac_f32_32x32x32_bf16 v[34:49], v[184:187], v[122:129], v5
global_store_dwordx4 v4, v[208:211], s[18:19] offset:0 sc1
v_exp_f32 v138, v138
v_exp_f32 v139, v139
v_exp_f32 v140, v140
v_exp_f32 v141, v141
v_smfmac_f32_32x32x32_bf16 v[50:65], v[180:183], v[130:137], v5
global_load_dwordx4 v[166:169], v3, s[38:39]
v_exp_f32 v142, v142
v_exp_f32 v143, v143
v_exp_f32 v144, v144
v_exp_f32 v145, v145
v_mfma_f32_32x32x16_bf16 v[114:129], v[174:177], v[28:31], 0
global_load_dwordx4 v[170:173], v3, s[38:39] offset:768
s_add_u32 s38, s38, 0x1600
s_addc_u32 s39, s39, 0
v_exp_f32 v98, v98
v_exp_f32 v99, v99
v_exp_f32 v100, v100
v_exp_f32 v101, v101
v_mfma_f32_32x32x8_bf16 v[114:129], v[178:179], v[32:33], v[114:129]
v_exp_f32 v102, v102
v_exp_f32 v103, v103
v_exp_f32 v104, v104
v_exp_f32 v105, v105
v_smfmac_f32_32x32x32_bf16 v[50:65], v[184:187], v[138:145], v5
global_load_dwordx4 a[212:215], v4, s[60:61] offset:3072
s_add_u32 s60, s60, 0x2000
s_addc_u32 s61, s61, 0
v_exp_f32 v106, v106
v_exp_f32 v107, v107
v_exp_f32 v108, v108
v_exp_f32 v109, v109
v_smfmac_f32_32x32x32_bf16 v[66:81], v[180:183], v[98:105], v5
s_waitcnt vmcnt(16)
v_exp_f32 v110, v110
v_exp_f32 v111, v111
v_exp_f32 v112, v112
v_exp_f32 v113, v113
v_mfma_f32_32x32x16_bf16 v[130:145], v[188:191], v[10:13], 0
v_exp_f32 v114, v114
v_exp_f32 v115, v115
v_exp_f32 v116, v116
v_exp_f32 v117, v117
v_mfma_f32_32x32x8_bf16 v[130:145], v[192:193], v[14:15], v[130:145]
v_exp_f32 v118, v118
v_exp_f32 v119, v119
v_exp_f32 v120, v120
v_exp_f32 v121, v121
v_smfmac_f32_32x32x32_bf16 v[66:81], v[184:187], v[106:113], v5
global_load_dwordx4 a[216:219], v4, s[60:61] offset:-4096
v_exp_f32 v122, v122
v_exp_f32 v123, v123
v_exp_f32 v124, v124
v_exp_f32 v125, v125
v_smfmac_f32_32x32x32_bf16 v[82:97], v[180:183], v[114:121], v5
global_load_dwordx4 a[220:223], v4, s[60:61] offset:-3072
v_exp_f32 v126, v126
v_exp_f32 v127, v127
v_exp_f32 v128, v128
v_exp_f32 v129, v129
v_mfma_f32_32x32x16_bf16 v[98:113], v[188:191], v[16:19], 0
global_load_dwordx4 v[174:177], v1, s[38:39]
v_exp_f32 v130, v130
v_exp_f32 v131, v131
v_exp_f32 v132, v132
v_exp_f32 v133, v133
v_mfma_f32_32x32x8_bf16 v[98:113], v[192:193], v[20:21], v[98:113]
v_exp_f32 v134, v134
v_exp_f32 v135, v135
v_exp_f32 v136, v136
v_exp_f32 v137, v137
v_smfmac_f32_32x32x32_bf16 v[82:97], v[184:187], v[122:129], v5
v_exp_f32 v138, v138
v_exp_f32 v139, v139
v_exp_f32 v140, v140
v_exp_f32 v141, v141
v_smfmac_f32_32x32x32_bf16 v[34:49], v[194:197], v[130:137], v5
global_load_dwordx2 v[178:179], v2, s[38:39]
v_exp_f32 v142, v142
v_exp_f32 v143, v143
v_exp_f32 v144, v144
v_exp_f32 v145, v145
v_mfma_f32_32x32x16_bf16 v[114:129], v[188:191], v[22:25], 0
v_exp_f32 v98, v98
v_exp_f32 v99, v99
v_exp_f32 v100, v100
v_exp_f32 v101, v101
v_mfma_f32_32x32x8_bf16 v[114:129], v[192:193], v[26:27], v[114:129]
v_exp_f32 v102, v102
v_exp_f32 v103, v103
v_exp_f32 v104, v104
v_exp_f32 v105, v105
v_smfmac_f32_32x32x32_bf16 v[34:49], v[198:201], v[138:145], v5
global_store_dwordx4 v4, v[208:211], s[18:19] offset:1024 sc1
v_exp_f32 v106, v106
v_exp_f32 v107, v107
v_exp_f32 v108, v108
v_exp_f32 v109, v109
v_smfmac_f32_32x32x32_bf16 v[50:65], v[194:197], v[98:105], v5
global_load_dwordx4 v[180:183], v3, s[38:39]
v_exp_f32 v110, v110
v_exp_f32 v111, v111
v_exp_f32 v112, v112
v_exp_f32 v113, v113
v_mfma_f32_32x32x16_bf16 v[130:145], v[188:191], v[28:31], 0
global_load_dwordx4 v[184:187], v3, s[38:39] offset:768
s_add_u32 s38, s38, 0x1600
s_addc_u32 s39, s39, 0
v_exp_f32 v114, v114
v_exp_f32 v115, v115
v_exp_f32 v116, v116
v_exp_f32 v117, v117
v_mfma_f32_32x32x8_bf16 v[130:145], v[192:193], v[32:33], v[130:145]
v_exp_f32 v118, v118
v_exp_f32 v119, v119
v_exp_f32 v120, v120
v_exp_f32 v121, v121
v_smfmac_f32_32x32x32_bf16 v[50:65], v[198:201], v[106:113], v5
)ASM"
R"ASM(global_load_dwordx4 a[228:231], v4, s[60:61] offset:-2048
v_exp_f32 v122, v122
v_exp_f32 v123, v123
v_exp_f32 v124, v124
v_exp_f32 v125, v125
v_smfmac_f32_32x32x32_bf16 v[66:81], v[194:197], v[114:121], v5
s_waitcnt vmcnt(16)
v_exp_f32 v126, v126
v_exp_f32 v127, v127
v_exp_f32 v128, v128
v_exp_f32 v129, v129
v_mfma_f32_32x32x16_bf16 v[98:113], v[146:149], v[10:13], 0
v_exp_f32 v130, v130
v_exp_f32 v131, v131
v_exp_f32 v132, v132
v_exp_f32 v133, v133
v_mfma_f32_32x32x8_bf16 v[98:113], v[150:151], v[14:15], v[98:113]
v_exp_f32 v134, v134
v_exp_f32 v135, v135
v_exp_f32 v136, v136
v_exp_f32 v137, v137
v_smfmac_f32_32x32x32_bf16 v[66:81], v[198:201], v[122:129], v5
global_load_dwordx4 a[232:235], v4, s[60:61] offset:-1024
v_exp_f32 v138, v138
v_exp_f32 v139, v139
v_exp_f32 v140, v140
v_exp_f32 v141, v141
v_smfmac_f32_32x32x32_bf16 v[82:97], v[194:197], v[130:137], v5
v_exp_f32 v142, v142
v_exp_f32 v143, v143
v_exp_f32 v144, v144
v_exp_f32 v145, v145
v_mfma_f32_32x32x16_bf16 v[114:129], v[146:149], v[16:19], 0
global_load_dwordx4 v[188:191], v1, s[38:39]
v_exp_f32 v98, v98
v_exp_f32 v99, v99
v_exp_f32 v100, v100
v_exp_f32 v101, v101
v_mfma_f32_32x32x8_bf16 v[114:129], v[150:151], v[20:21], v[114:129]
v_exp_f32 v102, v102
v_exp_f32 v103, v103
v_exp_f32 v104, v104
v_exp_f32 v105, v105
v_smfmac_f32_32x32x32_bf16 v[82:97], v[198:201], v[138:145], v5
v_exp_f32 v106, v106
v_exp_f32 v107, v107
v_exp_f32 v108, v108
v_exp_f32 v109, v109
v_smfmac_f32_32x32x32_bf16 v[34:49], v[152:155], v[98:105], v5
global_load_dwordx2 v[192:193], v2, s[38:39]
v_exp_f32 v110, v110
v_exp_f32 v111, v111
v_exp_f32 v112, v112
v_exp_f32 v113, v113
v_mfma_f32_32x32x16_bf16 v[130:145], v[146:149], v[22:25], 0
global_load_dwordx4 a[236:239], v4, s[60:61] offset:0
v_exp_f32 v114, v114
v_exp_f32 v115, v115
v_exp_f32 v116, v116
v_exp_f32 v117, v117
v_mfma_f32_32x32x8_bf16 v[130:145], v[150:151], v[26:27], v[130:145]
v_exp_f32 v118, v118
v_exp_f32 v119, v119
v_exp_f32 v120, v120
v_exp_f32 v121, v121
v_smfmac_f32_32x32x32_bf16 v[34:49], v[156:159], v[106:113], v5
global_store_dwordx4 v4, v[208:211], s[18:19] offset:2048 sc1
v_exp_f32 v122, v122
v_exp_f32 v123, v123
v_exp_f32 v124, v124
v_exp_f32 v125, v125
v_smfmac_f32_32x32x32_bf16 v[50:65], v[152:155], v[114:121], v5
global_load_dwordx4 v[194:197], v3, s[38:39]
v_exp_f32 v126, v126
v_exp_f32 v127, v127
v_exp_f32 v128, v128
v_exp_f32 v129, v129
v_mfma_f32_32x32x16_bf16 v[98:113], v[146:149], v[28:31], 0
global_load_dwordx4 v[198:201], v3, s[38:39] offset:768
s_add_u32 s38, s38, 0x1600
s_addc_u32 s39, s39, 0
v_exp_f32 v130, v130
v_exp_f32 v131, v131
v_exp_f32 v132, v132
v_exp_f32 v133, v133
v_mfma_f32_32x32x8_bf16 v[98:113], v[150:151], v[32:33], v[98:113]
v_exp_f32 v134, v134
v_exp_f32 v135, v135
v_exp_f32 v136, v136
v_exp_f32 v137, v137
v_smfmac_f32_32x32x32_bf16 v[50:65], v[156:159], v[122:129], v5
global_load_dwordx4 a[240:243], v4, s[60:61] offset:1024
v_exp_f32 v138, v138
v_exp_f32 v139, v139
v_exp_f32 v140, v140
v_exp_f32 v141, v141
v_smfmac_f32_32x32x32_bf16 v[66:81], v[152:155], v[130:137], v5
s_waitcnt vmcnt(17)
v_exp_f32 v142, v142
v_exp_f32 v143, v143
v_exp_f32 v144, v144
v_exp_f32 v145, v145
v_mfma_f32_32x32x16_bf16 v[114:129], v[160:163], v[10:13], 0
v_exp_f32 v98, v98
v_exp_f32 v99, v99
v_exp_f32 v100, v100
v_exp_f32 v101, v101
v_mfma_f32_32x32x8_bf16 v[114:129], v[164:165], v[14:15], v[114:129]
v_exp_f32 v102, v102
v_exp_f32 v103, v103
v_exp_f32 v104, v104
v_exp_f32 v105, v105
v_smfmac_f32_32x32x32_bf16 v[66:81], v[156:159], v[138:145], v5
global_load_dwordx4 a[244:247], v4, s[60:61] offset:2048
v_exp_f32 v106, v106
v_exp_f32 v107, v107
v_exp_f32 v108, v108
v_exp_f32 v109, v109
v_smfmac_f32_32x32x32_bf16 v[82:97], v[152:155], v[98:105], v5
v_exp_f32 v110, v110
v_exp_f32 v111, v111
v_exp_f32 v112, v112
v_exp_f32 v113, v113
v_mfma_f32_32x32x16_bf16 v[130:145], v[160:163], v[16:19], 0
global_load_dwordx4 v[146:149], v1, s[38:39]
v_exp_f32 v114, v114
v_exp_f32 v115, v115
v_exp_f32 v116, v116
v_exp_f32 v117, v117
v_mfma_f32_32x32x8_bf16 v[130:145], v[164:165], v[20:21], v[130:145]
v_exp_f32 v118, v118
v_exp_f32 v119, v119
v_exp_f32 v120, v120
v_exp_f32 v121, v121
v_smfmac_f32_32x32x32_bf16 v[82:97], v[156:159], v[106:113], v5
v_exp_f32 v122, v122
v_exp_f32 v123, v123
v_exp_f32 v124, v124
v_exp_f32 v125, v125
v_smfmac_f32_32x32x32_bf16 v[34:49], v[166:169], v[114:121], v5
global_load_dwordx2 v[150:151], v2, s[38:39]
v_exp_f32 v126, v126
v_exp_f32 v127, v127
v_exp_f32 v128, v128
v_exp_f32 v129, v129
v_mfma_f32_32x32x16_bf16 v[98:113], v[160:163], v[22:25], 0
global_load_dwordx4 a[252:255], v4, s[60:61] offset:3072
v_exp_f32 v130, v130
v_exp_f32 v131, v131
v_exp_f32 v132, v132
v_exp_f32 v133, v133
v_mfma_f32_32x32x8_bf16 v[98:113], v[164:165], v[26:27], v[98:113]
v_exp_f32 v134, v134
v_exp_f32 v135, v135
v_exp_f32 v136, v136
v_exp_f32 v137, v137
v_smfmac_f32_32x32x32_bf16 v[34:49], v[170:173], v[122:129], v5
global_store_dwordx4 v4, v[208:211], s[18:19] offset:3072 sc1
v_exp_f32 v138, v138
v_exp_f32 v139, v139
v_exp_f32 v140, v140
v_exp_f32 v141, v141
v_smfmac_f32_32x32x32_bf16 v[50:65], v[166:169], v[130:137], v5
global_load_dwordx4 v[152:155], v3, s[38:39]
v_exp_f32 v142, v142
v_exp_f32 v143, v143
v_exp_f32 v144, v144
v_exp_f32 v145, v145
v_mfma_f32_32x32x16_bf16 v[114:129], v[160:163], v[28:31], 0
global_load_dwordx4 v[156:159], v3, s[38:39] offset:768
s_add_u32 s38, s38, 0x1600
s_addc_u32 s39, s39, 0
s_add_u32 s60, s60, 0x2000
s_addc_u32 s61, s61, 0
v_exp_f32 v98, v98
v_exp_f32 v99, v99
v_exp_f32 v100, v100
v_exp_f32 v101, v101
v_mfma_f32_32x32x8_bf16 v[114:129], v[164:165], v[32:33], v[114:129]
v_exp_f32 v102, v102
v_exp_f32 v103, v103
v_exp_f32 v104, v104
v_exp_f32 v105, v105
v_smfmac_f32_32x32x32_bf16 v[50:65], v[170:173], v[138:145], v5
v_exp_f32 v106, v106
v_exp_f32 v107, v107
v_exp_f32 v108, v108
v_exp_f32 v109, v109
v_smfmac_f32_32x32x32_bf16 v[66:81], v[166:169], v[98:105], v5
s_waitcnt vmcnt(16)
v_exp_f32 v110, v110
v_exp_f32 v111, v111
v_exp_f32 v112, v112
v_exp_f32 v113, v113
v_mfma_f32_32x32x16_bf16 v[130:145], v[174:177], v[10:13], 0
global_load_dwordx4 v[216:219], v4, s[60:61] offset:-4096
v_exp_f32 v114, v114
v_exp_f32 v115, v115
v_exp_f32 v116, v116
v_exp_f32 v117, v117
)ASM"
R"ASM(v_mfma_f32_32x32x8_bf16 v[130:145], v[178:179], v[14:15], v[130:145]
v_exp_f32 v118, v118
v_exp_f32 v119, v119
v_exp_f32 v120, v120
v_exp_f32 v121, v121
v_smfmac_f32_32x32x32_bf16 v[66:81], v[170:173], v[106:113], v5
v_exp_f32 v122, v122
v_exp_f32 v123, v123
v_exp_f32 v124, v124
v_exp_f32 v125, v125
v_smfmac_f32_32x32x32_bf16 v[82:97], v[166:169], v[114:121], v5
global_load_dwordx4 v[220:223], v4, s[60:61] offset:-3072
v_exp_f32 v126, v126
v_exp_f32 v127, v127
v_exp_f32 v128, v128
v_exp_f32 v129, v129
v_mfma_f32_32x32x16_bf16 v[98:113], v[174:177], v[16:19], 0
global_load_dwordx4 v[160:163], v1, s[38:39]
v_exp_f32 v130, v130
v_exp_f32 v131, v131
v_exp_f32 v132, v132
v_exp_f32 v133, v133
v_mfma_f32_32x32x8_bf16 v[98:113], v[178:179], v[20:21], v[98:113]
v_exp_f32 v134, v134
v_exp_f32 v135, v135
v_exp_f32 v136, v136
v_exp_f32 v137, v137
v_smfmac_f32_32x32x32_bf16 v[82:97], v[170:173], v[122:129], v5
v_exp_f32 v138, v138
v_exp_f32 v139, v139
v_exp_f32 v140, v140
v_exp_f32 v141, v141
v_smfmac_f32_32x32x32_bf16 v[34:49], v[180:183], v[130:137], v5
global_load_dwordx2 v[164:165], v2, s[38:39]
v_exp_f32 v142, v142
v_exp_f32 v143, v143
v_exp_f32 v144, v144
v_exp_f32 v145, v145
v_mfma_f32_32x32x16_bf16 v[114:129], v[174:177], v[22:25], 0
global_load_dwordx4 v[224:227], v4, s[60:61] offset:-2048
s_add_u32 s18, s18, 0x2000
s_addc_u32 s19, s19, 0
v_exp_f32 v98, v98
v_exp_f32 v99, v99
v_exp_f32 v100, v100
v_exp_f32 v101, v101
v_mfma_f32_32x32x8_bf16 v[114:129], v[178:179], v[26:27], v[114:129]
v_exp_f32 v102, v102
v_exp_f32 v103, v103
v_exp_f32 v104, v104
v_exp_f32 v105, v105
v_smfmac_f32_32x32x32_bf16 v[34:49], v[184:187], v[138:145], v5
global_store_dwordx4 v4, v[208:211], s[18:19] offset:-4096 sc1
v_exp_f32 v106, v106
v_exp_f32 v107, v107
v_exp_f32 v108, v108
v_exp_f32 v109, v109
v_smfmac_f32_32x32x32_bf16 v[50:65], v[180:183], v[98:105], v5
global_load_dwordx4 v[166:169], v3, s[38:39]
v_exp_f32 v110, v110
v_exp_f32 v111, v111
v_exp_f32 v112, v112
v_exp_f32 v113, v113
v_mfma_f32_32x32x16_bf16 v[130:145], v[174:177], v[28:31], 0
global_load_dwordx4 v[170:173], v3, s[38:39] offset:768
s_add_u32 s38, s38, 0x1600
s_addc_u32 s39, s39, 0
v_exp_f32 v114, v114
v_exp_f32 v115, v115
v_exp_f32 v116, v116
v_exp_f32 v117, v117
v_mfma_f32_32x32x8_bf16 v[130:145], v[178:179], v[32:33], v[130:145]
v_exp_f32 v118, v118
v_exp_f32 v119, v119
v_exp_f32 v120, v120
v_exp_f32 v121, v121
v_smfmac_f32_32x32x32_bf16 v[50:65], v[184:187], v[106:113], v5
v_exp_f32 v122, v122
v_exp_f32 v123, v123
v_exp_f32 v124, v124
v_exp_f32 v125, v125
v_smfmac_f32_32x32x32_bf16 v[66:81], v[180:183], v[114:121], v5
s_waitcnt vmcnt(16)
v_exp_f32 v126, v126
v_exp_f32 v127, v127
v_exp_f32 v128, v128
v_exp_f32 v129, v129
v_mfma_f32_32x32x16_bf16 v[98:113], v[188:191], v[10:13], 0
global_load_dwordx4 v[228:231], v4, s[60:61] offset:-1024
v_exp_f32 v130, v130
v_exp_f32 v131, v131
v_exp_f32 v132, v132
v_exp_f32 v133, v133
v_mfma_f32_32x32x8_bf16 v[98:113], v[192:193], v[14:15], v[98:113]
v_exp_f32 v134, v134
v_exp_f32 v135, v135
v_exp_f32 v136, v136
v_exp_f32 v137, v137
v_smfmac_f32_32x32x32_bf16 v[66:81], v[184:187], v[122:129], v5
v_exp_f32 v138, v138
v_exp_f32 v139, v139
v_exp_f32 v140, v140
v_exp_f32 v141, v141
v_smfmac_f32_32x32x32_bf16 v[82:97], v[180:183], v[130:137], v5
global_load_dwordx4 v[236:239], v4, s[60:61] offset:0
v_exp_f32 v142, v142
v_exp_f32 v143, v143
v_exp_f32 v144, v144
v_exp_f32 v145, v145
v_mfma_f32_32x32x16_bf16 v[114:129], v[188:191], v[16:19], 0
global_load_dwordx4 v[174:177], v1, s[38:39]
v_exp_f32 v98, v98
v_exp_f32 v99, v99
v_exp_f32 v100, v100
v_exp_f32 v101, v101
v_mfma_f32_32x32x8_bf16 v[114:129], v[192:193], v[20:21], v[114:129]
v_exp_f32 v102, v102
v_exp_f32 v103, v103
v_exp_f32 v104, v104
v_exp_f32 v105, v105
v_smfmac_f32_32x32x32_bf16 v[82:97], v[184:187], v[138:145], v5
v_exp_f32 v106, v106
v_exp_f32 v107, v107
v_exp_f32 v108, v108
v_exp_f32 v109, v109
v_smfmac_f32_32x32x32_bf16 v[34:49], v[194:197], v[98:105], v5
global_load_dwordx2 v[178:179], v2, s[38:39]
v_exp_f32 v110, v110
v_exp_f32 v111, v111
v_exp_f32 v112, v112
v_exp_f32 v113, v113
v_mfma_f32_32x32x16_bf16 v[130:145], v[188:191], v[22:25], 0
v_exp_f32 v114, v114
v_exp_f32 v115, v115
v_exp_f32 v116, v116
v_exp_f32 v117, v117
v_mfma_f32_32x32x8_bf16 v[130:145], v[192:193], v[26:27], v[130:145]
v_exp_f32 v118, v118
v_exp_f32 v119, v119
v_exp_f32 v120, v120
v_exp_f32 v121, v121
v_smfmac_f32_32x32x32_bf16 v[34:49], v[198:201], v[106:113], v5
global_store_dwordx4 v4, v[208:211], s[18:19] offset:-3072 sc1
v_exp_f32 v122, v122
v_exp_f32 v123, v123
v_exp_f32 v124, v124
v_exp_f32 v125, v125
v_smfmac_f32_32x32x32_bf16 v[50:65], v[194:197], v[114:121], v5
global_load_dwordx4 v[180:183], v3, s[38:39]
v_exp_f32 v126, v126
v_exp_f32 v127, v127
v_exp_f32 v128, v128
v_exp_f32 v129, v129
v_mfma_f32_32x32x16_bf16 v[98:113], v[188:191], v[28:31], 0
global_load_dwordx4 v[184:187], v3, s[38:39] offset:768
s_add_u32 s38, s38, 0x1600
s_addc_u32 s39, s39, 0
v_exp_f32 v130, v130
v_exp_f32 v131, v131
v_exp_f32 v132, v132
v_exp_f32 v133, v133
v_mfma_f32_32x32x8_bf16 v[98:113], v[192:193], v[32:33], v[98:113]
v_exp_f32 v134, v134
v_exp_f32 v135, v135
v_exp_f32 v136, v136
v_exp_f32 v137, v137
v_smfmac_f32_32x32x32_bf16 v[50:65], v[198:201], v[122:129], v5
global_load_dwordx4 v[240:243], v4, s[60:61] offset:1024
v_exp_f32 v138, v138
v_exp_f32 v139, v139
v_exp_f32 v140, v140
v_exp_f32 v141, v141
v_smfmac_f32_32x32x32_bf16 v[66:81], v[194:197], v[130:137], v5
s_waitcnt vmcnt(16)
v_exp_f32 v142, v142
v_exp_f32 v143, v143
v_exp_f32 v144, v144
v_exp_f32 v145, v145
v_mfma_f32_32x32x16_bf16 v[114:129], v[146:149], v[10:13], 0
v_exp_f32 v98, v98
v_exp_f32 v99, v99
v_exp_f32 v100, v100
v_exp_f32 v101, v101
v_mfma_f32_32x32x8_bf16 v[114:129], v[150:151], v[14:15], v[114:129]
v_exp_f32 v102, v102
v_exp_f32 v103, v103
v_exp_f32 v104, v104
v_exp_f32 v105, v105
v_smfmac_f32_32x32x32_bf16 v[66:81], v[198:201], v[138:145], v5
global_load_dwordx4 v[244:247], v4, s[60:61] offset:2048
v_exp_f32 v106, v106
v_exp_f32 v107, v107
v_exp_f32 v108, v108
v_exp_f32 v109, v109
v_smfmac_f32_32x32x32_bf16 v[82:97], v[194:197], v[98:105], v5
global_load_dwordx4 v[248:251], v4, s[60:61] offset:3072
v_exp_f32 v110, v110
v_exp_f32 v111, v111
v_exp_f32 v112, v112
v_exp_f32 v113, v113
)ASM"
R"ASM(v_mfma_f32_32x32x16_bf16 v[130:145], v[146:149], v[16:19], 0
global_load_dwordx4 v[188:191], v1, s[38:39]
v_exp_f32 v114, v114
v_exp_f32 v115, v115
v_exp_f32 v116, v116
v_exp_f32 v117, v117
v_mfma_f32_32x32x8_bf16 v[130:145], v[150:151], v[20:21], v[130:145]
v_exp_f32 v118, v118
v_exp_f32 v119, v119
v_exp_f32 v120, v120
v_exp_f32 v121, v121
v_smfmac_f32_32x32x32_bf16 v[82:97], v[198:201], v[106:113], v5
v_exp_f32 v122, v122
v_exp_f32 v123, v123
v_exp_f32 v124, v124
v_exp_f32 v125, v125
v_smfmac_f32_32x32x32_bf16 v[34:49], v[152:155], v[114:121], v5
global_load_dwordx2 v[192:193], v2, s[38:39]
v_exp_f32 v126, v126
v_exp_f32 v127, v127
v_exp_f32 v128, v128
v_exp_f32 v129, v129
v_mfma_f32_32x32x16_bf16 v[98:113], v[146:149], v[22:25], 0
v_exp_f32 v130, v130
v_exp_f32 v131, v131
v_exp_f32 v132, v132
v_exp_f32 v133, v133
v_mfma_f32_32x32x8_bf16 v[98:113], v[150:151], v[26:27], v[98:113]
v_exp_f32 v134, v134
v_exp_f32 v135, v135
v_exp_f32 v136, v136
v_exp_f32 v137, v137
v_smfmac_f32_32x32x32_bf16 v[34:49], v[156:159], v[122:129], v5
global_store_dwordx4 v4, v[208:211], s[18:19] offset:-2048 sc1
v_exp_f32 v138, v138
v_exp_f32 v139, v139
v_exp_f32 v140, v140
v_exp_f32 v141, v141
v_smfmac_f32_32x32x32_bf16 v[50:65], v[152:155], v[130:137], v5
global_load_dwordx4 v[194:197], v3, s[38:39]
v_exp_f32 v142, v142
v_exp_f32 v143, v143
v_exp_f32 v144, v144
v_exp_f32 v145, v145
v_mfma_f32_32x32x16_bf16 v[114:129], v[146:149], v[28:31], 0
global_load_dwordx4 v[198:201], v3, s[38:39] offset:768
s_add_u32 s38, s38, 0x1600
s_addc_u32 s39, s39, 0
s_add_u32 s60, s60, 0x2000
s_addc_u32 s61, s61, 0
v_exp_f32 v98, v98
v_exp_f32 v99, v99
v_exp_f32 v100, v100
v_exp_f32 v101, v101
v_mfma_f32_32x32x8_bf16 v[114:129], v[150:151], v[32:33], v[114:129]
v_exp_f32 v102, v102
v_exp_f32 v103, v103
v_exp_f32 v104, v104
v_exp_f32 v105, v105
v_smfmac_f32_32x32x32_bf16 v[50:65], v[156:159], v[138:145], v5
global_load_dwordx4 v[252:255], v4, s[60:61] offset:-4096
s_mul_i32 s35, s29, 0x1400
s_add_u32 s35, s35, 0x8000
s_add_u32 s60, s20, s35
s_addc_u32 s61, s21, 0
s_add_u32 s60, s60, 0x1000
s_addc_u32 s61, s61, 0
v_exp_f32 v106, v106
v_exp_f32 v107, v107
v_exp_f32 v108, v108
v_exp_f32 v109, v109
v_smfmac_f32_32x32x32_bf16 v[66:81], v[152:155], v[98:105], v5
s_waitcnt vmcnt(16)
v_exp_f32 v110, v110
v_exp_f32 v111, v111
v_exp_f32 v112, v112
v_exp_f32 v113, v113
v_mfma_f32_32x32x16_bf16 v[130:145], v[160:163], v[10:13], 0
v_exp_f32 v114, v114
v_exp_f32 v115, v115
v_exp_f32 v116, v116
v_exp_f32 v117, v117
v_mfma_f32_32x32x8_bf16 v[130:145], v[164:165], v[14:15], v[130:145]
v_exp_f32 v118, v118
v_exp_f32 v119, v119
v_exp_f32 v120, v120
v_exp_f32 v121, v121
v_smfmac_f32_32x32x32_bf16 v[66:81], v[156:159], v[106:113], v5
global_load_dwordx4 a[176:179], v4, s[60:61] offset:-4096
v_exp_f32 v122, v122
v_exp_f32 v123, v123
v_exp_f32 v124, v124
v_exp_f32 v125, v125
v_smfmac_f32_32x32x32_bf16 v[82:97], v[152:155], v[114:121], v5
v_exp_f32 v126, v126
v_exp_f32 v127, v127
v_exp_f32 v128, v128
v_exp_f32 v129, v129
v_mfma_f32_32x32x16_bf16 v[98:113], v[160:163], v[16:19], 0
global_load_dwordx4 v[146:149], v1, s[38:39]
v_exp_f32 v130, v130
v_exp_f32 v131, v131
v_exp_f32 v132, v132
v_exp_f32 v133, v133
v_mfma_f32_32x32x8_bf16 v[98:113], v[164:165], v[20:21], v[98:113]
v_exp_f32 v134, v134
v_exp_f32 v135, v135
v_exp_f32 v136, v136
v_exp_f32 v137, v137
v_smfmac_f32_32x32x32_bf16 v[82:97], v[156:159], v[122:129], v5
v_exp_f32 v138, v138
v_exp_f32 v139, v139
v_exp_f32 v140, v140
v_exp_f32 v141, v141
v_smfmac_f32_32x32x32_bf16 v[34:49], v[166:169], v[130:137], v5
global_load_dwordx2 v[150:151], v2, s[38:39]
v_exp_f32 v142, v142
v_exp_f32 v143, v143
v_exp_f32 v144, v144
v_exp_f32 v145, v145
v_mfma_f32_32x32x16_bf16 v[114:129], v[160:163], v[22:25], 0
global_load_dwordx4 a[200:203], v4, s[60:61] offset:-3072
v_exp_f32 v98, v98
v_exp_f32 v99, v99
v_exp_f32 v100, v100
v_exp_f32 v101, v101
v_mfma_f32_32x32x8_bf16 v[114:129], v[164:165], v[26:27], v[114:129]
v_exp_f32 v102, v102
v_exp_f32 v103, v103
v_exp_f32 v104, v104
v_exp_f32 v105, v105
v_smfmac_f32_32x32x32_bf16 v[34:49], v[170:173], v[138:145], v5
global_store_dwordx4 v4, v[208:211], s[18:19] offset:-1024 sc1
v_exp_f32 v106, v106
v_exp_f32 v107, v107
v_exp_f32 v108, v108
v_exp_f32 v109, v109
v_smfmac_f32_32x32x32_bf16 v[50:65], v[166:169], v[98:105], v5
global_load_dwordx4 v[152:155], v3, s[38:39]
v_exp_f32 v110, v110
v_exp_f32 v111, v111
v_exp_f32 v112, v112
v_exp_f32 v113, v113
v_mfma_f32_32x32x16_bf16 v[130:145], v[160:163], v[28:31], 0
global_load_dwordx4 v[156:159], v3, s[38:39] offset:768
s_add_u32 s38, s38, 0x1600
s_addc_u32 s39, s39, 0
v_exp_f32 v114, v114
v_exp_f32 v115, v115
v_exp_f32 v116, v116
v_exp_f32 v117, v117
v_mfma_f32_32x32x8_bf16 v[130:145], v[164:165], v[32:33], v[130:145]
v_exp_f32 v118, v118
v_exp_f32 v119, v119
v_exp_f32 v120, v120
v_exp_f32 v121, v121
v_smfmac_f32_32x32x32_bf16 v[50:65], v[170:173], v[106:113], v5
global_load_dwordx4 a[224:227], v4, s[60:61] offset:-2048
v_exp_f32 v122, v122
v_exp_f32 v123, v123
v_exp_f32 v124, v124
v_exp_f32 v125, v125
v_smfmac_f32_32x32x32_bf16 v[66:81], v[166:169], v[114:121], v5
s_waitcnt vmcnt(17)
v_exp_f32 v126, v126
v_exp_f32 v127, v127
v_exp_f32 v128, v128
v_exp_f32 v129, v129
v_mfma_f32_32x32x16_bf16 v[98:113], v[174:177], v[10:13], 0
v_exp_f32 v130, v130
v_exp_f32 v131, v131
v_exp_f32 v132, v132
v_exp_f32 v133, v133
v_mfma_f32_32x32x8_bf16 v[98:113], v[178:179], v[14:15], v[98:113]
v_exp_f32 v134, v134
v_exp_f32 v135, v135
v_exp_f32 v136, v136
v_exp_f32 v137, v137
v_smfmac_f32_32x32x32_bf16 v[66:81], v[170:173], v[122:129], v5
global_load_dwordx4 a[248:251], v4, s[60:61] offset:-1024
v_exp_f32 v138, v138
v_exp_f32 v139, v139
v_exp_f32 v140, v140
v_exp_f32 v141, v141
v_smfmac_f32_32x32x32_bf16 v[82:97], v[166:169], v[130:137], v5
v_exp_f32 v142, v142
v_exp_f32 v143, v143
v_exp_f32 v144, v144
v_exp_f32 v145, v145
v_mfma_f32_32x32x16_bf16 v[114:129], v[174:177], v[16:19], 0
global_load_dwordx4 v[160:163], v1, s[38:39]
v_exp_f32 v98, v98
v_exp_f32 v99, v99
v_exp_f32 v100, v100
v_exp_f32 v101, v101
v_mfma_f32_32x32x8_bf16 v[114:129], v[178:179], v[20:21], v[114:129]
v_exp_f32 v102, v102
v_exp_f32 v103, v103
v_exp_f32 v104, v104
v_exp_f32 v105, v105
)ASM"
R"ASM(v_smfmac_f32_32x32x32_bf16 v[82:97], v[170:173], v[138:145], v5
v_exp_f32 v106, v106
v_exp_f32 v107, v107
v_exp_f32 v108, v108
v_exp_f32 v109, v109
v_smfmac_f32_32x32x32_bf16 v[34:49], v[180:183], v[98:105], v5
global_load_dwordx2 v[164:165], v2, s[38:39]
v_exp_f32 v110, v110
v_exp_f32 v111, v111
v_exp_f32 v112, v112
v_exp_f32 v113, v113
v_mfma_f32_32x32x16_bf16 v[130:145], v[174:177], v[22:25], 0
global_load_dwordx4 v[232:235], v4, s[60:61] offset:0
s_lshl_b32 s35, s28, 2
s_add_u32 s35, s35, s29
s_mul_i32 s35, s35, 5
s_lshr_b32 s35, s35, 1
s_min_u32 s35, s35, 0x9fd
s_lshl_b32 s35, s35, 10
s_add_u32 s18, s26, s35
s_addc_u32 s19, s27, 0
v_exp_f32 v114, v114
v_exp_f32 v115, v115
v_exp_f32 v116, v116
v_exp_f32 v117, v117
v_mfma_f32_32x32x8_bf16 v[130:145], v[178:179], v[26:27], v[130:145]
v_exp_f32 v118, v118
v_exp_f32 v119, v119
v_exp_f32 v120, v120
v_exp_f32 v121, v121
v_smfmac_f32_32x32x32_bf16 v[34:49], v[184:187], v[106:113], v5
global_store_dwordx4 v4, v[208:211], s[18:19] offset:0 sc1
v_exp_f32 v122, v122
v_exp_f32 v123, v123
v_exp_f32 v124, v124
v_exp_f32 v125, v125
v_smfmac_f32_32x32x32_bf16 v[50:65], v[180:183], v[114:121], v5
global_load_dwordx4 v[166:169], v3, s[38:39]
v_exp_f32 v126, v126
v_exp_f32 v127, v127
v_exp_f32 v128, v128
v_exp_f32 v129, v129
v_mfma_f32_32x32x16_bf16 v[98:113], v[174:177], v[28:31], 0
global_load_dwordx4 v[170:173], v3, s[38:39] offset:768
s_add_u32 s38, s38, 0x1600
s_addc_u32 s39, s39, 0
s_mul_i32 s35, s34, 54
s_add_u32 s35, s35, s62
s_mul_i32 s35, s35, 0x1400
s_add_u32 s35, s35, 0x8d8000
s_add_u32 s60, s20, s35
s_addc_u32 s61, s21, 0
s_add_u32 s60, s60, 0x1000
s_addc_u32 s61, s61, 0
v_exp_f32 v130, v130
v_exp_f32 v131, v131
v_exp_f32 v132, v132
v_exp_f32 v133, v133
v_mfma_f32_32x32x8_bf16 v[98:113], v[178:179], v[32:33], v[98:113]
v_exp_f32 v134, v134
v_exp_f32 v135, v135
v_exp_f32 v136, v136
v_exp_f32 v137, v137
v_smfmac_f32_32x32x32_bf16 v[50:65], v[184:187], v[122:129], v5
v_exp_f32 v138, v138
v_exp_f32 v139, v139
v_exp_f32 v140, v140
v_exp_f32 v141, v141
v_smfmac_f32_32x32x32_bf16 v[66:81], v[180:183], v[130:137], v5
s_waitcnt vmcnt(16)
v_exp_f32 v142, v142
v_exp_f32 v143, v143
v_exp_f32 v144, v144
v_exp_f32 v145, v145
v_mfma_f32_32x32x16_bf16 v[114:129], v[188:191], v[10:13], 0
global_load_dwordx4 a[0:3], v4, s[60:61] offset:-4096
v_exp_f32 v98, v98
v_exp_f32 v99, v99
v_exp_f32 v100, v100
v_exp_f32 v101, v101
v_mfma_f32_32x32x8_bf16 v[114:129], v[192:193], v[14:15], v[114:129]
v_exp_f32 v102, v102
v_exp_f32 v103, v103
v_exp_f32 v104, v104
v_exp_f32 v105, v105
v_smfmac_f32_32x32x32_bf16 v[66:81], v[184:187], v[138:145], v5
v_exp_f32 v106, v106
v_exp_f32 v107, v107
v_exp_f32 v108, v108
v_exp_f32 v109, v109
v_smfmac_f32_32x32x32_bf16 v[82:97], v[180:183], v[98:105], v5
global_load_dwordx4 a[4:7], v4, s[60:61] offset:-3072
v_exp_f32 v110, v110
v_exp_f32 v111, v111
v_exp_f32 v112, v112
v_exp_f32 v113, v113
v_mfma_f32_32x32x16_bf16 v[130:145], v[188:191], v[16:19], 0
global_load_dwordx4 v[174:177], v1, s[38:39]
v_exp_f32 v114, v114
v_exp_f32 v115, v115
v_exp_f32 v116, v116
v_exp_f32 v117, v117
v_mfma_f32_32x32x8_bf16 v[130:145], v[192:193], v[20:21], v[130:145]
v_exp_f32 v118, v118
v_exp_f32 v119, v119
v_exp_f32 v120, v120
v_exp_f32 v121, v121
v_smfmac_f32_32x32x32_bf16 v[82:97], v[184:187], v[106:113], v5
v_exp_f32 v122, v122
v_exp_f32 v123, v123
v_exp_f32 v124, v124
v_exp_f32 v125, v125
v_smfmac_f32_32x32x32_bf16 v[34:49], v[194:197], v[114:121], v5
global_load_dwordx2 v[178:179], v2, s[38:39]
v_exp_f32 v126, v126
v_exp_f32 v127, v127
v_exp_f32 v128, v128
v_exp_f32 v129, v129
v_mfma_f32_32x32x16_bf16 v[98:113], v[188:191], v[22:25], 0
global_load_dwordx4 a[8:11], v4, s[60:61] offset:-2048
v_exp_f32 v130, v130
v_exp_f32 v131, v131
v_exp_f32 v132, v132
v_exp_f32 v133, v133
v_mfma_f32_32x32x8_bf16 v[98:113], v[192:193], v[26:27], v[98:113]
v_exp_f32 v134, v134
v_exp_f32 v135, v135
v_exp_f32 v136, v136
v_exp_f32 v137, v137
v_smfmac_f32_32x32x32_bf16 v[34:49], v[198:201], v[122:129], v5
global_store_dwordx4 v4, v[208:211], s[18:19] offset:1024 sc1
v_exp_f32 v138, v138
v_exp_f32 v139, v139
v_exp_f32 v140, v140
v_exp_f32 v141, v141
v_smfmac_f32_32x32x32_bf16 v[50:65], v[194:197], v[130:137], v5
global_load_dwordx4 v[180:183], v3, s[38:39]
v_exp_f32 v142, v142
v_exp_f32 v143, v143
v_exp_f32 v144, v144
v_exp_f32 v145, v145
v_mfma_f32_32x32x16_bf16 v[114:129], v[188:191], v[28:31], 0
global_load_dwordx4 v[184:187], v3, s[38:39] offset:768
s_add_u32 s38, s38, 0x1600
s_addc_u32 s39, s39, 0
v_exp_f32 v98, v98
v_exp_f32 v99, v99
v_exp_f32 v100, v100
v_exp_f32 v101, v101
v_mfma_f32_32x32x8_bf16 v[114:129], v[192:193], v[32:33], v[114:129]
v_exp_f32 v102, v102
v_exp_f32 v103, v103
v_exp_f32 v104, v104
v_exp_f32 v105, v105
v_smfmac_f32_32x32x32_bf16 v[50:65], v[198:201], v[138:145], v5
v_exp_f32 v106, v106
v_exp_f32 v107, v107
v_exp_f32 v108, v108
v_exp_f32 v109, v109
v_smfmac_f32_32x32x32_bf16 v[66:81], v[194:197], v[98:105], v5
s_waitcnt vmcnt(16)
v_exp_f32 v110, v110
v_exp_f32 v111, v111
v_exp_f32 v112, v112
v_exp_f32 v113, v113
v_mfma_f32_32x32x16_bf16 v[130:145], v[146:149], v[10:13], 0
global_load_dwordx4 a[12:15], v4, s[60:61] offset:-1024
v_exp_f32 v114, v114
v_exp_f32 v115, v115
v_exp_f32 v116, v116
v_exp_f32 v117, v117
v_mfma_f32_32x32x8_bf16 v[130:145], v[150:151], v[14:15], v[130:145]
v_exp_f32 v118, v118
v_exp_f32 v119, v119
v_exp_f32 v120, v120
v_exp_f32 v121, v121
v_smfmac_f32_32x32x32_bf16 v[66:81], v[198:201], v[106:113], v5
v_exp_f32 v122, v122
v_exp_f32 v123, v123
v_exp_f32 v124, v124
v_exp_f32 v125, v125
v_smfmac_f32_32x32x32_bf16 v[82:97], v[194:197], v[114:121], v5
global_load_dwordx4 a[16:19], v4, s[60:61] offset:0
v_exp_f32 v126, v126
v_exp_f32 v127, v127
v_exp_f32 v128, v128
v_exp_f32 v129, v129
v_mfma_f32_32x32x16_bf16 v[98:113], v[146:149], v[16:19], 0
global_load_dwordx4 v[188:191], v1, s[38:39]
v_exp_f32 v130, v130
v_exp_f32 v131, v131
v_exp_f32 v132, v132
v_exp_f32 v133, v133
v_mfma_f32_32x32x8_bf16 v[98:113], v[150:151], v[20:21], v[98:113]
v_exp_f32 v134, v134
v_exp_f32 v135, v135
v_exp_f32 v136, v136
v_exp_f32 v137, v137
v_smfmac_f32_32x32x32_bf16 v[82:97], v[198:201], v[122:129], v5
v_exp_f32 v138, v138
)ASM"
R"ASM(v_exp_f32 v139, v139
v_exp_f32 v140, v140
v_exp_f32 v141, v141
v_smfmac_f32_32x32x32_bf16 v[34:49], v[152:155], v[130:137], v5
global_load_dwordx2 v[192:193], v2, s[38:39]
v_exp_f32 v142, v142
v_exp_f32 v143, v143
v_exp_f32 v144, v144
v_exp_f32 v145, v145
v_mfma_f32_32x32x16_bf16 v[114:129], v[146:149], v[22:25], 0
v_exp_f32 v98, v98
v_exp_f32 v99, v99
v_exp_f32 v100, v100
v_exp_f32 v101, v101
v_mfma_f32_32x32x8_bf16 v[114:129], v[150:151], v[26:27], v[114:129]
v_exp_f32 v102, v102
v_exp_f32 v103, v103
v_exp_f32 v104, v104
v_exp_f32 v105, v105
v_smfmac_f32_32x32x32_bf16 v[34:49], v[156:159], v[138:145], v5
global_store_dwordx4 v4, v[208:211], s[18:19] offset:2048 sc1
v_exp_f32 v106, v106
v_exp_f32 v107, v107
v_exp_f32 v108, v108
v_exp_f32 v109, v109
v_smfmac_f32_32x32x32_bf16 v[50:65], v[152:155], v[98:105], v5
global_load_dwordx4 v[194:197], v3, s[38:39]
v_exp_f32 v110, v110
v_exp_f32 v111, v111
v_exp_f32 v112, v112
v_exp_f32 v113, v113
v_mfma_f32_32x32x16_bf16 v[130:145], v[146:149], v[28:31], 0
global_load_dwordx4 v[198:201], v3, s[38:39] offset:768
s_add_u32 s38, s38, 0x1600
s_addc_u32 s39, s39, 0
v_exp_f32 v114, v114
v_exp_f32 v115, v115
v_exp_f32 v116, v116
v_exp_f32 v117, v117
v_mfma_f32_32x32x8_bf16 v[130:145], v[150:151], v[32:33], v[130:145]
v_exp_f32 v118, v118
v_exp_f32 v119, v119
v_exp_f32 v120, v120
v_exp_f32 v121, v121
v_smfmac_f32_32x32x32_bf16 v[50:65], v[156:159], v[106:113], v5
global_load_dwordx4 a[20:23], v4, s[60:61] offset:1024
v_exp_f32 v122, v122
v_exp_f32 v123, v123
v_exp_f32 v124, v124
v_exp_f32 v125, v125
v_smfmac_f32_32x32x32_bf16 v[66:81], v[152:155], v[114:121], v5
s_waitcnt vmcnt(16)
v_exp_f32 v126, v126
v_exp_f32 v127, v127
v_exp_f32 v128, v128
v_exp_f32 v129, v129
v_mfma_f32_32x32x16_bf16 v[98:113], v[160:163], v[10:13], 0
v_exp_f32 v130, v130
v_exp_f32 v131, v131
v_exp_f32 v132, v132
v_exp_f32 v133, v133
v_mfma_f32_32x32x8_bf16 v[98:113], v[164:165], v[14:15], v[98:113]
v_exp_f32 v134, v134
v_exp_f32 v135, v135
v_exp_f32 v136, v136
v_exp_f32 v137, v137
v_smfmac_f32_32x32x32_bf16 v[66:81], v[156:159], v[122:129], v5
global_load_dwordx4 a[24:27], v4, s[60:61] offset:2048
v_exp_f32 v138, v138
v_exp_f32 v139, v139
v_exp_f32 v140, v140
v_exp_f32 v141, v141
v_smfmac_f32_32x32x32_bf16 v[82:97], v[152:155], v[130:137], v5
global_load_dwordx4 a[28:31], v4, s[60:61] offset:3072
v_exp_f32 v142, v142
v_exp_f32 v143, v143
v_exp_f32 v144, v144
v_exp_f32 v145, v145
v_mfma_f32_32x32x16_bf16 v[114:129], v[160:163], v[16:19], 0
global_load_dwordx4 v[146:149], v1, s[38:39]
v_exp_f32 v98, v98
v_exp_f32 v99, v99
v_exp_f32 v100, v100
v_exp_f32 v101, v101
v_mfma_f32_32x32x8_bf16 v[114:129], v[164:165], v[20:21], v[114:129]
v_exp_f32 v102, v102
v_exp_f32 v103, v103
v_exp_f32 v104, v104
v_exp_f32 v105, v105
v_smfmac_f32_32x32x32_bf16 v[82:97], v[156:159], v[138:145], v5
v_exp_f32 v106, v106
v_exp_f32 v107, v107
v_exp_f32 v108, v108
v_exp_f32 v109, v109
v_smfmac_f32_32x32x32_bf16 v[34:49], v[166:169], v[98:105], v5
global_load_dwordx2 v[150:151], v2, s[38:39]
s_add_u32 s60, s60, 0x2000
s_addc_u32 s61, s61, 0
v_exp_f32 v110, v110
v_exp_f32 v111, v111
v_exp_f32 v112, v112
v_exp_f32 v113, v113
v_mfma_f32_32x32x16_bf16 v[130:145], v[160:163], v[22:25], 0
v_exp_f32 v114, v114
v_exp_f32 v115, v115
v_exp_f32 v116, v116
v_exp_f32 v117, v117
v_mfma_f32_32x32x8_bf16 v[130:145], v[164:165], v[26:27], v[130:145]
v_exp_f32 v118, v118
v_exp_f32 v119, v119
v_exp_f32 v120, v120
v_exp_f32 v121, v121
v_smfmac_f32_32x32x32_bf16 v[34:49], v[170:173], v[106:113], v5
global_load_dwordx4 a[32:35], v4, s[60:61] offset:-4096
v_exp_f32 v122, v122
v_exp_f32 v123, v123
v_exp_f32 v124, v124
v_exp_f32 v125, v125
v_smfmac_f32_32x32x32_bf16 v[50:65], v[166:169], v[114:121], v5
global_load_dwordx4 v[152:155], v3, s[38:39]
v_exp_f32 v126, v126
v_exp_f32 v127, v127
v_exp_f32 v128, v128
v_exp_f32 v129, v129
v_mfma_f32_32x32x16_bf16 v[98:113], v[160:163], v[28:31], 0
global_load_dwordx4 v[156:159], v3, s[38:39] offset:768
s_add_u32 s38, s38, 0x1600
s_addc_u32 s39, s39, 0
v_exp_f32 v130, v130
v_exp_f32 v131, v131
v_exp_f32 v132, v132
v_exp_f32 v133, v133
v_mfma_f32_32x32x8_bf16 v[98:113], v[164:165], v[32:33], v[98:113]
v_exp_f32 v134, v134
v_exp_f32 v135, v135
v_exp_f32 v136, v136
v_exp_f32 v137, v137
v_smfmac_f32_32x32x32_bf16 v[50:65], v[170:173], v[122:129], v5
v_exp_f32 v138, v138
v_exp_f32 v139, v139
v_exp_f32 v140, v140
v_exp_f32 v141, v141
v_smfmac_f32_32x32x32_bf16 v[66:81], v[166:169], v[130:137], v5
s_waitcnt vmcnt(15)
v_exp_f32 v142, v142
v_exp_f32 v143, v143
v_exp_f32 v144, v144
v_exp_f32 v145, v145
v_mfma_f32_32x32x16_bf16 v[114:129], v[174:177], v[10:13], 0
global_load_dwordx4 a[36:39], v4, s[60:61] offset:-3072
v_exp_f32 v98, v98
v_exp_f32 v99, v99
v_exp_f32 v100, v100
v_exp_f32 v101, v101
v_mfma_f32_32x32x8_bf16 v[114:129], v[178:179], v[14:15], v[114:129]
v_exp_f32 v102, v102
v_exp_f32 v103, v103
v_exp_f32 v104, v104
v_exp_f32 v105, v105
v_smfmac_f32_32x32x32_bf16 v[66:81], v[170:173], v[138:145], v5
v_exp_f32 v106, v106
v_exp_f32 v107, v107
v_exp_f32 v108, v108
v_exp_f32 v109, v109
v_smfmac_f32_32x32x32_bf16 v[82:97], v[166:169], v[98:105], v5
global_load_dwordx4 a[40:43], v4, s[60:61] offset:-2048
v_exp_f32 v110, v110
v_exp_f32 v111, v111
v_exp_f32 v112, v112
v_exp_f32 v113, v113
v_mfma_f32_32x32x16_bf16 v[130:145], v[174:177], v[16:19], 0
global_load_dwordx4 v[160:163], v1, s[38:39]
v_exp_f32 v114, v114
v_exp_f32 v115, v115
v_exp_f32 v116, v116
v_exp_f32 v117, v117
v_mfma_f32_32x32x8_bf16 v[130:145], v[178:179], v[20:21], v[130:145]
v_exp_f32 v118, v118
v_exp_f32 v119, v119
v_exp_f32 v120, v120
v_exp_f32 v121, v121
v_smfmac_f32_32x32x32_bf16 v[82:97], v[170:173], v[106:113], v5
v_exp_f32 v122, v122
v_exp_f32 v123, v123
v_exp_f32 v124, v124
v_exp_f32 v125, v125
v_smfmac_f32_32x32x32_bf16 v[34:49], v[180:183], v[114:121], v5
global_load_dwordx2 v[164:165], v2, s[38:39]
v_exp_f32 v126, v126
v_exp_f32 v127, v127
v_exp_f32 v128, v128
v_exp_f32 v129, v129
v_mfma_f32_32x32x16_bf16 v[98:113], v[174:177], v[22:25], 0
v_exp_f32 v130, v130
v_exp_f32 v131, v131
v_exp_f32 v132, v132
v_exp_f32 v133, v133
v_mfma_f32_32x32x8_bf16 v[98:113], v[178:179], v[26:27], v[98:113]
v_exp_f32 v134, v134
v_exp_f32 v135, v135
)ASM"
R"ASM(v_exp_f32 v136, v136
v_exp_f32 v137, v137
v_smfmac_f32_32x32x32_bf16 v[34:49], v[184:187], v[122:129], v5
global_load_dwordx4 a[44:47], v4, s[60:61] offset:-1024
v_exp_f32 v138, v138
v_exp_f32 v139, v139
v_exp_f32 v140, v140
v_exp_f32 v141, v141
v_smfmac_f32_32x32x32_bf16 v[50:65], v[180:183], v[130:137], v5
global_load_dwordx4 v[166:169], v3, s[38:39]
v_exp_f32 v142, v142
v_exp_f32 v143, v143
v_exp_f32 v144, v144
v_exp_f32 v145, v145
v_mfma_f32_32x32x16_bf16 v[114:129], v[174:177], v[28:31], 0
global_load_dwordx4 v[170:173], v3, s[38:39] offset:768
s_add_u32 s38, s38, 0x1600
s_addc_u32 s39, s39, 0
v_exp_f32 v98, v98
v_exp_f32 v99, v99
v_exp_f32 v100, v100
v_exp_f32 v101, v101
v_mfma_f32_32x32x8_bf16 v[114:129], v[178:179], v[32:33], v[114:129]
v_exp_f32 v102, v102
v_exp_f32 v103, v103
v_exp_f32 v104, v104
v_exp_f32 v105, v105
v_smfmac_f32_32x32x32_bf16 v[50:65], v[184:187], v[138:145], v5
global_load_dwordx4 a[48:51], v4, s[60:61] offset:0
v_exp_f32 v106, v106
v_exp_f32 v107, v107
v_exp_f32 v108, v108
v_exp_f32 v109, v109
v_smfmac_f32_32x32x32_bf16 v[66:81], v[180:183], v[98:105], v5
s_waitcnt vmcnt(16)
v_exp_f32 v110, v110
v_exp_f32 v111, v111
v_exp_f32 v112, v112
v_exp_f32 v113, v113
v_mfma_f32_32x32x16_bf16 v[130:145], v[188:191], v[10:13], 0
v_exp_f32 v114, v114
v_exp_f32 v115, v115
v_exp_f32 v116, v116
v_exp_f32 v117, v117
v_mfma_f32_32x32x8_bf16 v[130:145], v[192:193], v[14:15], v[130:145]
v_exp_f32 v118, v118
v_exp_f32 v119, v119
v_exp_f32 v120, v120
v_exp_f32 v121, v121
v_smfmac_f32_32x32x32_bf16 v[66:81], v[184:187], v[106:113], v5
global_load_dwordx4 a[52:55], v4, s[60:61] offset:1024
v_exp_f32 v122, v122
v_exp_f32 v123, v123
v_exp_f32 v124, v124
v_exp_f32 v125, v125
v_smfmac_f32_32x32x32_bf16 v[82:97], v[180:183], v[114:121], v5
v_exp_f32 v126, v126
v_exp_f32 v127, v127
v_exp_f32 v128, v128
v_exp_f32 v129, v129
v_mfma_f32_32x32x16_bf16 v[98:113], v[188:191], v[16:19], 0
global_load_dwordx4 v[174:177], v1, s[38:39]
v_exp_f32 v130, v130
v_exp_f32 v131, v131
v_exp_f32 v132, v132
v_exp_f32 v133, v133
v_mfma_f32_32x32x8_bf16 v[98:113], v[192:193], v[20:21], v[98:113]
v_exp_f32 v134, v134
v_exp_f32 v135, v135
v_exp_f32 v136, v136
v_exp_f32 v137, v137
v_smfmac_f32_32x32x32_bf16 v[82:97], v[184:187], v[122:129], v5
v_exp_f32 v138, v138
v_exp_f32 v139, v139
v_exp_f32 v140, v140
v_exp_f32 v141, v141
v_smfmac_f32_32x32x32_bf16 v[34:49], v[194:197], v[130:137], v5
global_load_dwordx2 v[178:179], v2, s[38:39]
v_exp_f32 v142, v142
v_exp_f32 v143, v143
v_exp_f32 v144, v144
v_exp_f32 v145, v145
v_mfma_f32_32x32x16_bf16 v[114:129], v[188:191], v[22:25], 0
global_load_dwordx4 a[56:59], v4, s[60:61] offset:2048
v_exp_f32 v98, v98
v_exp_f32 v99, v99
v_exp_f32 v100, v100
v_exp_f32 v101, v101
v_mfma_f32_32x32x8_bf16 v[114:129], v[192:193], v[26:27], v[114:129]
v_exp_f32 v102, v102
v_exp_f32 v103, v103
v_exp_f32 v104, v104
v_exp_f32 v105, v105
v_smfmac_f32_32x32x32_bf16 v[34:49], v[198:201], v[138:145], v5
v_exp_f32 v106, v106
v_exp_f32 v107, v107
v_exp_f32 v108, v108
v_exp_f32 v109, v109
v_smfmac_f32_32x32x32_bf16 v[50:65], v[194:197], v[98:105], v5
global_load_dwordx4 v[180:183], v3, s[38:39]
v_exp_f32 v110, v110
v_exp_f32 v111, v111
v_exp_f32 v112, v112
v_exp_f32 v113, v113
v_mfma_f32_32x32x16_bf16 v[130:145], v[188:191], v[28:31], 0
global_load_dwordx4 v[184:187], v3, s[38:39] offset:768
s_add_u32 s38, s38, 0x1600
s_addc_u32 s39, s39, 0
v_exp_f32 v114, v114
v_exp_f32 v115, v115
v_exp_f32 v116, v116
v_exp_f32 v117, v117
v_mfma_f32_32x32x8_bf16 v[130:145], v[192:193], v[32:33], v[130:145]
v_exp_f32 v118, v118
v_exp_f32 v119, v119
v_exp_f32 v120, v120
v_exp_f32 v121, v121
v_smfmac_f32_32x32x32_bf16 v[50:65], v[198:201], v[106:113], v5
global_load_dwordx4 a[60:63], v4, s[60:61] offset:3072
s_add_u32 s60, s60, 0x2000
s_addc_u32 s61, s61, 0
v_exp_f32 v122, v122
v_exp_f32 v123, v123
v_exp_f32 v124, v124
v_exp_f32 v125, v125
v_smfmac_f32_32x32x32_bf16 v[66:81], v[194:197], v[114:121], v5
s_waitcnt vmcnt(15)
v_exp_f32 v126, v126
v_exp_f32 v127, v127
v_exp_f32 v128, v128
v_exp_f32 v129, v129
v_mfma_f32_32x32x16_bf16 v[98:113], v[146:149], v[10:13], 0
global_load_dwordx4 a[64:67], v4, s[60:61] offset:-4096
v_exp_f32 v130, v130
v_exp_f32 v131, v131
v_exp_f32 v132, v132
v_exp_f32 v133, v133
v_mfma_f32_32x32x8_bf16 v[98:113], v[150:151], v[14:15], v[98:113]
v_exp_f32 v134, v134
v_exp_f32 v135, v135
v_exp_f32 v136, v136
v_exp_f32 v137, v137
v_smfmac_f32_32x32x32_bf16 v[66:81], v[198:201], v[122:129], v5
v_exp_f32 v138, v138
v_exp_f32 v139, v139
v_exp_f32 v140, v140
v_exp_f32 v141, v141
v_smfmac_f32_32x32x32_bf16 v[82:97], v[194:197], v[130:137], v5
global_load_dwordx4 a[68:71], v4, s[60:61] offset:-3072
v_exp_f32 v142, v142
v_exp_f32 v143, v143
v_exp_f32 v144, v144
v_exp_f32 v145, v145
v_mfma_f32_32x32x16_bf16 v[114:129], v[146:149], v[16:19], 0
global_load_dwordx4 v[188:191], v1, s[38:39]
v_exp_f32 v98, v98
v_exp_f32 v99, v99
v_exp_f32 v100, v100
v_exp_f32 v101, v101
v_mfma_f32_32x32x8_bf16 v[114:129], v[150:151], v[20:21], v[114:129]
v_exp_f32 v102, v102
v_exp_f32 v103, v103
v_exp_f32 v104, v104
v_exp_f32 v105, v105
v_smfmac_f32_32x32x32_bf16 v[82:97], v[198:201], v[138:145], v5
v_exp_f32 v106, v106
v_exp_f32 v107, v107
v_exp_f32 v108, v108
v_exp_f32 v109, v109
v_smfmac_f32_32x32x32_bf16 v[34:49], v[152:155], v[98:105], v5
global_load_dwordx2 v[192:193], v2, s[38:39]
v_exp_f32 v110, v110
v_exp_f32 v111, v111
v_exp_f32 v112, v112
v_exp_f32 v113, v113
v_mfma_f32_32x32x16_bf16 v[130:145], v[146:149], v[22:25], 0
v_exp_f32 v114, v114
v_exp_f32 v115, v115
v_exp_f32 v116, v116
v_exp_f32 v117, v117
v_mfma_f32_32x32x8_bf16 v[130:145], v[150:151], v[26:27], v[130:145]
v_exp_f32 v118, v118
v_exp_f32 v119, v119
v_exp_f32 v120, v120
v_exp_f32 v121, v121
v_smfmac_f32_32x32x32_bf16 v[34:49], v[156:159], v[106:113], v5
s_and_b32 s35, s28, 63
s_lshl_b32 s35, s35, 8
v_mov_b32 v6, s35
v_mov_b32 v7, 1
s_mov_b64 exec, 1
global_atomic_add v6, v7, s[20:21]
s_mov_b64 exec, -1
v_exp_f32 v122, v122
v_exp_f32 v123, v123
v_exp_f32 v124, v124
v_exp_f32 v125, v125
v_smfmac_f32_32x32x32_bf16 v[50:65], v[152:155], v[114:121], v5
global_load_dwordx4 v[194:197], v3, s[38:39]
v_exp_f32 v126, v126
)ASM"
R"ASM(v_exp_f32 v127, v127
v_exp_f32 v128, v128
v_exp_f32 v129, v129
v_mfma_f32_32x32x16_bf16 v[98:113], v[146:149], v[28:31], 0
global_load_dwordx4 v[198:201], v3, s[38:39] offset:768
s_add_u32 s38, s38, 0x1600
s_addc_u32 s39, s39, 0
v_exp_f32 v130, v130
v_exp_f32 v131, v131
v_exp_f32 v132, v132
v_exp_f32 v133, v133
v_mfma_f32_32x32x8_bf16 v[98:113], v[150:151], v[32:33], v[98:113]
v_exp_f32 v134, v134
v_exp_f32 v135, v135
v_exp_f32 v136, v136
v_exp_f32 v137, v137
v_smfmac_f32_32x32x32_bf16 v[50:65], v[156:159], v[122:129], v5
global_load_dwordx4 a[72:75], v4, s[60:61] offset:-2048
v_exp_f32 v138, v138
v_exp_f32 v139, v139
v_exp_f32 v140, v140
v_exp_f32 v141, v141
v_smfmac_f32_32x32x32_bf16 v[66:81], v[152:155], v[130:137], v5
s_waitcnt vmcnt(16)
v_exp_f32 v142, v142
v_exp_f32 v143, v143
v_exp_f32 v144, v144
v_exp_f32 v145, v145
v_mfma_f32_32x32x16_bf16 v[114:129], v[160:163], v[10:13], 0
v_exp_f32 v98, v98
v_exp_f32 v99, v99
v_exp_f32 v100, v100
v_exp_f32 v101, v101
v_mfma_f32_32x32x8_bf16 v[114:129], v[164:165], v[14:15], v[114:129]
v_exp_f32 v102, v102
v_exp_f32 v103, v103
v_exp_f32 v104, v104
v_exp_f32 v105, v105
v_smfmac_f32_32x32x32_bf16 v[66:81], v[156:159], v[138:145], v5
global_load_dwordx4 a[76:79], v4, s[60:61] offset:-1024
v_exp_f32 v106, v106
v_exp_f32 v107, v107
v_exp_f32 v108, v108
v_exp_f32 v109, v109
v_smfmac_f32_32x32x32_bf16 v[82:97], v[152:155], v[98:105], v5
v_exp_f32 v110, v110
v_exp_f32 v111, v111
v_exp_f32 v112, v112
v_exp_f32 v113, v113
v_mfma_f32_32x32x16_bf16 v[130:145], v[160:163], v[16:19], 0
global_load_dwordx4 v[146:149], v1, s[38:39]
v_exp_f32 v114, v114
v_exp_f32 v115, v115
v_exp_f32 v116, v116
v_exp_f32 v117, v117
v_mfma_f32_32x32x8_bf16 v[130:145], v[164:165], v[20:21], v[130:145]
v_exp_f32 v118, v118
v_exp_f32 v119, v119
v_exp_f32 v120, v120
v_exp_f32 v121, v121
v_smfmac_f32_32x32x32_bf16 v[82:97], v[156:159], v[106:113], v5
v_exp_f32 v122, v122
v_exp_f32 v123, v123
v_exp_f32 v124, v124
v_exp_f32 v125, v125
v_smfmac_f32_32x32x32_bf16 v[34:49], v[166:169], v[114:121], v5
global_load_dwordx2 v[150:151], v2, s[38:39]
v_exp_f32 v126, v126
v_exp_f32 v127, v127
v_exp_f32 v128, v128
v_exp_f32 v129, v129
v_mfma_f32_32x32x16_bf16 v[98:113], v[160:163], v[22:25], 0
global_load_dwordx4 a[80:83], v4, s[60:61] offset:0
v_exp_f32 v130, v130
v_exp_f32 v131, v131
v_exp_f32 v132, v132
v_exp_f32 v133, v133
v_mfma_f32_32x32x8_bf16 v[98:113], v[164:165], v[26:27], v[98:113]
v_exp_f32 v134, v134
v_exp_f32 v135, v135
v_exp_f32 v136, v136
v_exp_f32 v137, v137
v_smfmac_f32_32x32x32_bf16 v[34:49], v[170:173], v[122:129], v5
global_load_dwordx4 a[84:87], v4, s[60:61] offset:1024
v_exp_f32 v138, v138
v_exp_f32 v139, v139
v_exp_f32 v140, v140
v_exp_f32 v141, v141
v_smfmac_f32_32x32x32_bf16 v[50:65], v[166:169], v[130:137], v5
global_load_dwordx4 v[152:155], v3, s[38:39]
v_exp_f32 v142, v142
v_exp_f32 v143, v143
v_exp_f32 v144, v144
v_exp_f32 v145, v145
v_mfma_f32_32x32x16_bf16 v[114:129], v[160:163], v[28:31], 0
global_load_dwordx4 v[156:159], v3, s[38:39] offset:768
s_add_u32 s38, s38, 0x1600
s_addc_u32 s39, s39, 0
v_exp_f32 v98, v98
v_exp_f32 v99, v99
v_exp_f32 v100, v100
v_exp_f32 v101, v101
v_mfma_f32_32x32x8_bf16 v[114:129], v[164:165], v[32:33], v[114:129]
v_exp_f32 v102, v102
v_exp_f32 v103, v103
v_exp_f32 v104, v104
v_exp_f32 v105, v105
v_smfmac_f32_32x32x32_bf16 v[50:65], v[170:173], v[138:145], v5
v_exp_f32 v106, v106
v_exp_f32 v107, v107
v_exp_f32 v108, v108
v_exp_f32 v109, v109
v_smfmac_f32_32x32x32_bf16 v[66:81], v[166:169], v[98:105], v5
s_waitcnt vmcnt(16)
v_exp_f32 v110, v110
v_exp_f32 v111, v111
v_exp_f32 v112, v112
v_exp_f32 v113, v113
v_mfma_f32_32x32x16_bf16 v[130:145], v[174:177], v[10:13], 0
global_load_dwordx4 a[88:91], v4, s[60:61] offset:2048
v_exp_f32 v114, v114
v_exp_f32 v115, v115
v_exp_f32 v116, v116
v_exp_f32 v117, v117
v_mfma_f32_32x32x8_bf16 v[130:145], v[178:179], v[14:15], v[130:145]
v_exp_f32 v118, v118
v_exp_f32 v119, v119
v_exp_f32 v120, v120
v_exp_f32 v121, v121
v_smfmac_f32_32x32x32_bf16 v[66:81], v[170:173], v[106:113], v5
v_exp_f32 v122, v122
v_exp_f32 v123, v123
v_exp_f32 v124, v124
v_exp_f32 v125, v125
v_smfmac_f32_32x32x32_bf16 v[82:97], v[166:169], v[114:121], v5
global_load_dwordx4 a[92:95], v4, s[60:61] offset:3072
v_exp_f32 v126, v126
v_exp_f32 v127, v127
v_exp_f32 v128, v128
v_exp_f32 v129, v129
v_mfma_f32_32x32x16_bf16 v[98:113], v[174:177], v[16:19], 0
global_load_dwordx4 v[160:163], v1, s[38:39]
v_exp_f32 v130, v130
v_exp_f32 v131, v131
v_exp_f32 v132, v132
v_exp_f32 v133, v133
v_mfma_f32_32x32x8_bf16 v[98:113], v[178:179], v[20:21], v[98:113]
v_exp_f32 v134, v134
v_exp_f32 v135, v135
v_exp_f32 v136, v136
v_exp_f32 v137, v137
v_smfmac_f32_32x32x32_bf16 v[82:97], v[170:173], v[122:129], v5
v_exp_f32 v138, v138
v_exp_f32 v139, v139
v_exp_f32 v140, v140
v_exp_f32 v141, v141
v_smfmac_f32_32x32x32_bf16 v[34:49], v[180:183], v[130:137], v5
global_load_dwordx2 v[164:165], v2, s[38:39]
s_add_u32 s60, s60, 0x2000
s_addc_u32 s61, s61, 0
v_exp_f32 v142, v142
v_exp_f32 v143, v143
v_exp_f32 v144, v144
v_exp_f32 v145, v145
v_mfma_f32_32x32x16_bf16 v[114:129], v[174:177], v[22:25], 0
v_exp_f32 v98, v98
v_exp_f32 v99, v99
v_exp_f32 v100, v100
v_exp_f32 v101, v101
v_mfma_f32_32x32x8_bf16 v[114:129], v[178:179], v[26:27], v[114:129]
v_exp_f32 v102, v102
v_exp_f32 v103, v103
v_exp_f32 v104, v104
v_exp_f32 v105, v105
v_smfmac_f32_32x32x32_bf16 v[34:49], v[184:187], v[138:145], v5
global_load_dwordx4 a[96:99], v4, s[60:61] offset:-4096
v_exp_f32 v106, v106
v_exp_f32 v107, v107
v_exp_f32 v108, v108
v_exp_f32 v109, v109
v_smfmac_f32_32x32x32_bf16 v[50:65], v[180:183], v[98:105], v5
global_load_dwordx4 v[166:169], v3, s[38:39]
v_exp_f32 v110, v110
v_exp_f32 v111, v111
v_exp_f32 v112, v112
v_exp_f32 v113, v113
v_mfma_f32_32x32x16_bf16 v[130:145], v[174:177], v[28:31], 0
global_load_dwordx4 v[170:173], v3, s[38:39] offset:768
s_add_u32 s38, s38, 0x1600
s_addc_u32 s39, s39, 0
v_exp_f32 v114, v114
v_exp_f32 v115, v115
v_exp_f32 v116, v116
v_exp_f32 v117, v117
v_mfma_f32_32x32x8_bf16 v[130:145], v[178:179], v[32:33], v[130:145]
v_exp_f32 v118, v118
v_exp_f32 v119, v119
v_exp_f32 v120, v120
v_exp_f32 v121, v121
v_smfmac_f32_32x32x32_bf16 v[50:65], v[184:187], v[106:113], v5
)ASM"
R"ASM(v_exp_f32 v122, v122
v_exp_f32 v123, v123
v_exp_f32 v124, v124
v_exp_f32 v125, v125
v_smfmac_f32_32x32x32_bf16 v[66:81], v[180:183], v[114:121], v5
s_waitcnt vmcnt(15)
v_exp_f32 v126, v126
v_exp_f32 v127, v127
v_exp_f32 v128, v128
v_exp_f32 v129, v129
v_mfma_f32_32x32x16_bf16 v[98:113], v[188:191], v[10:13], 0
global_load_dwordx4 a[100:103], v4, s[60:61] offset:-3072
v_exp_f32 v130, v130
v_exp_f32 v131, v131
v_exp_f32 v132, v132
v_exp_f32 v133, v133
v_mfma_f32_32x32x8_bf16 v[98:113], v[192:193], v[14:15], v[98:113]
v_exp_f32 v134, v134
v_exp_f32 v135, v135
v_exp_f32 v136, v136
v_exp_f32 v137, v137
v_smfmac_f32_32x32x32_bf16 v[66:81], v[184:187], v[122:129], v5
global_load_dwordx4 a[104:107], v4, s[60:61] offset:-2048
v_exp_f32 v138, v138
v_exp_f32 v139, v139
v_exp_f32 v140, v140
v_exp_f32 v141, v141
v_smfmac_f32_32x32x32_bf16 v[82:97], v[180:183], v[130:137], v5
v_exp_f32 v142, v142
v_exp_f32 v143, v143
v_exp_f32 v144, v144
v_exp_f32 v145, v145
v_mfma_f32_32x32x16_bf16 v[114:129], v[188:191], v[16:19], 0
global_load_dwordx4 v[174:177], v1, s[38:39]
v_exp_f32 v98, v98
v_exp_f32 v99, v99
v_exp_f32 v100, v100
v_exp_f32 v101, v101
v_mfma_f32_32x32x8_bf16 v[114:129], v[192:193], v[20:21], v[114:129]
v_exp_f32 v102, v102
v_exp_f32 v103, v103
v_exp_f32 v104, v104
v_exp_f32 v105, v105
v_smfmac_f32_32x32x32_bf16 v[82:97], v[184:187], v[138:145], v5
v_exp_f32 v106, v106
v_exp_f32 v107, v107
v_exp_f32 v108, v108
v_exp_f32 v109, v109
v_smfmac_f32_32x32x32_bf16 v[34:49], v[194:197], v[98:105], v5
global_load_dwordx2 v[178:179], v2, s[38:39]
v_exp_f32 v110, v110
v_exp_f32 v111, v111
v_exp_f32 v112, v112
v_exp_f32 v113, v113
v_mfma_f32_32x32x16_bf16 v[130:145], v[188:191], v[22:25], 0
global_load_dwordx4 a[108:111], v4, s[60:61] offset:-1024
v_exp_f32 v114, v114
v_exp_f32 v115, v115
v_exp_f32 v116, v116
v_exp_f32 v117, v117
v_mfma_f32_32x32x8_bf16 v[130:145], v[192:193], v[26:27], v[130:145]
v_exp_f32 v118, v118
v_exp_f32 v119, v119
v_exp_f32 v120, v120
v_exp_f32 v121, v121
v_smfmac_f32_32x32x32_bf16 v[34:49], v[198:201], v[106:113], v5
v_exp_f32 v122, v122
v_exp_f32 v123, v123
v_exp_f32 v124, v124
v_exp_f32 v125, v125
v_smfmac_f32_32x32x32_bf16 v[50:65], v[194:197], v[114:121], v5
global_load_dwordx4 v[180:183], v3, s[38:39]
v_exp_f32 v126, v126
v_exp_f32 v127, v127
v_exp_f32 v128, v128
v_exp_f32 v129, v129
v_mfma_f32_32x32x16_bf16 v[98:113], v[188:191], v[28:31], 0
global_load_dwordx4 v[184:187], v3, s[38:39] offset:768
s_add_u32 s38, s38, 0x1600
s_addc_u32 s39, s39, 0
v_exp_f32 v130, v130
v_exp_f32 v131, v131
v_exp_f32 v132, v132
v_exp_f32 v133, v133
v_mfma_f32_32x32x8_bf16 v[98:113], v[192:193], v[32:33], v[98:113]
v_exp_f32 v134, v134
v_exp_f32 v135, v135
v_exp_f32 v136, v136
v_exp_f32 v137, v137
v_smfmac_f32_32x32x32_bf16 v[50:65], v[198:201], v[122:129], v5
global_load_dwordx4 a[112:115], v4, s[60:61] offset:0
v_exp_f32 v138, v138
v_exp_f32 v139, v139
v_exp_f32 v140, v140
v_exp_f32 v141, v141
v_smfmac_f32_32x32x32_bf16 v[66:81], v[194:197], v[130:137], v5
s_waitcnt vmcnt(15)
v_exp_f32 v142, v142
v_exp_f32 v143, v143
v_exp_f32 v144, v144
v_exp_f32 v145, v145
v_mfma_f32_32x32x16_bf16 v[114:129], v[146:149], v[10:13], 0
v_exp_f32 v98, v98
v_exp_f32 v99, v99
v_exp_f32 v100, v100
v_exp_f32 v101, v101
v_mfma_f32_32x32x8_bf16 v[114:129], v[150:151], v[14:15], v[114:129]
v_exp_f32 v102, v102
v_exp_f32 v103, v103
v_exp_f32 v104, v104
v_exp_f32 v105, v105
v_smfmac_f32_32x32x32_bf16 v[66:81], v[198:201], v[138:145], v5
global_load_dwordx4 a[116:119], v4, s[60:61] offset:1024
v_exp_f32 v106, v106
v_exp_f32 v107, v107
v_exp_f32 v108, v108
v_exp_f32 v109, v109
v_smfmac_f32_32x32x32_bf16 v[82:97], v[194:197], v[98:105], v5
global_load_dwordx4 a[120:123], v4, s[60:61] offset:2048
v_exp_f32 v110, v110
v_exp_f32 v111, v111
v_exp_f32 v112, v112
v_exp_f32 v113, v113
v_mfma_f32_32x32x16_bf16 v[130:145], v[146:149], v[16:19], 0
global_load_dwordx4 v[188:191], v1, s[38:39]
v_exp_f32 v114, v114
v_exp_f32 v115, v115
v_exp_f32 v116, v116
v_exp_f32 v117, v117
v_mfma_f32_32x32x8_bf16 v[130:145], v[150:151], v[20:21], v[130:145]
v_exp_f32 v118, v118
v_exp_f32 v119, v119
v_exp_f32 v120, v120
v_exp_f32 v121, v121
v_smfmac_f32_32x32x32_bf16 v[82:97], v[198:201], v[106:113], v5
v_exp_f32 v122, v122
v_exp_f32 v123, v123
v_exp_f32 v124, v124
v_exp_f32 v125, v125
v_smfmac_f32_32x32x32_bf16 v[34:49], v[152:155], v[114:121], v5
global_load_dwordx2 v[192:193], v2, s[38:39]
v_exp_f32 v126, v126
v_exp_f32 v127, v127
v_exp_f32 v128, v128
v_exp_f32 v129, v129
v_mfma_f32_32x32x16_bf16 v[98:113], v[146:149], v[22:25], 0
v_exp_f32 v130, v130
v_exp_f32 v131, v131
v_exp_f32 v132, v132
v_exp_f32 v133, v133
v_mfma_f32_32x32x8_bf16 v[98:113], v[150:151], v[26:27], v[98:113]
v_exp_f32 v134, v134
v_exp_f32 v135, v135
v_exp_f32 v136, v136
v_exp_f32 v137, v137
v_smfmac_f32_32x32x32_bf16 v[34:49], v[156:159], v[122:129], v5
global_load_dwordx4 a[124:127], v4, s[60:61] offset:3072
v_exp_f32 v138, v138
v_exp_f32 v139, v139
v_exp_f32 v140, v140
v_exp_f32 v141, v141
v_smfmac_f32_32x32x32_bf16 v[50:65], v[152:155], v[130:137], v5
global_load_dwordx4 v[194:197], v3, s[38:39]
v_exp_f32 v142, v142
v_exp_f32 v143, v143
v_exp_f32 v144, v144
v_exp_f32 v145, v145
v_mfma_f32_32x32x16_bf16 v[114:129], v[146:149], v[28:31], 0
global_load_dwordx4 v[198:201], v3, s[38:39] offset:768
s_add_u32 s38, s38, 0x1600
s_addc_u32 s39, s39, 0
s_add_u32 s60, s60, 0x2000
s_addc_u32 s61, s61, 0
v_exp_f32 v98, v98
v_exp_f32 v99, v99
v_exp_f32 v100, v100
v_exp_f32 v101, v101
v_mfma_f32_32x32x8_bf16 v[114:129], v[150:151], v[32:33], v[114:129]
v_exp_f32 v102, v102
v_exp_f32 v103, v103
v_exp_f32 v104, v104
v_exp_f32 v105, v105
v_smfmac_f32_32x32x32_bf16 v[50:65], v[156:159], v[138:145], v5
v_exp_f32 v106, v106
v_exp_f32 v107, v107
v_exp_f32 v108, v108
v_exp_f32 v109, v109
v_smfmac_f32_32x32x32_bf16 v[66:81], v[152:155], v[98:105], v5
s_waitcnt vmcnt(15)
v_exp_f32 v110, v110
v_exp_f32 v111, v111
v_exp_f32 v112, v112
v_exp_f32 v113, v113
v_mfma_f32_32x32x16_bf16 v[130:145], v[160:163], v[10:13], 0
global_load_dwordx4 a[128:131], v4, s[60:61] offset:-4096
v_exp_f32 v114, v114
v_exp_f32 v115, v115
v_exp_f32 v116, v116
v_exp_f32 v117, v117
v_mfma_f32_32x32x8_bf16 v[130:145], v[164:165], v[14:15], v[130:145]
)ASM"
R"ASM(v_exp_f32 v118, v118
v_exp_f32 v119, v119
v_exp_f32 v120, v120
v_exp_f32 v121, v121
v_smfmac_f32_32x32x32_bf16 v[66:81], v[156:159], v[106:113], v5
v_exp_f32 v122, v122
v_exp_f32 v123, v123
v_exp_f32 v124, v124
v_exp_f32 v125, v125
v_smfmac_f32_32x32x32_bf16 v[82:97], v[152:155], v[114:121], v5
global_load_dwordx4 a[132:135], v4, s[60:61] offset:-3072
v_exp_f32 v126, v126
v_exp_f32 v127, v127
v_exp_f32 v128, v128
v_exp_f32 v129, v129
v_mfma_f32_32x32x16_bf16 v[98:113], v[160:163], v[16:19], 0
v_exp_f32 v130, v130
v_exp_f32 v131, v131
v_exp_f32 v132, v132
v_exp_f32 v133, v133
v_mfma_f32_32x32x8_bf16 v[98:113], v[164:165], v[20:21], v[98:113]
v_exp_f32 v134, v134
v_exp_f32 v135, v135
v_exp_f32 v136, v136
v_exp_f32 v137, v137
v_smfmac_f32_32x32x32_bf16 v[82:97], v[156:159], v[122:129], v5
v_exp_f32 v138, v138
v_exp_f32 v139, v139
v_exp_f32 v140, v140
v_exp_f32 v141, v141
v_smfmac_f32_32x32x32_bf16 v[34:49], v[166:169], v[130:137], v5
global_load_dwordx4 a[136:139], v4, s[60:61] offset:-2048
v_exp_f32 v142, v142
v_exp_f32 v143, v143
v_exp_f32 v144, v144
v_exp_f32 v145, v145
v_mfma_f32_32x32x16_bf16 v[114:129], v[160:163], v[22:25], 0
global_load_dwordx4 a[140:143], v4, s[60:61] offset:-1024
v_exp_f32 v98, v98
v_exp_f32 v99, v99
v_exp_f32 v100, v100
v_exp_f32 v101, v101
v_mfma_f32_32x32x8_bf16 v[114:129], v[164:165], v[26:27], v[114:129]
v_exp_f32 v102, v102
v_exp_f32 v103, v103
v_exp_f32 v104, v104
v_exp_f32 v105, v105
v_smfmac_f32_32x32x32_bf16 v[34:49], v[170:173], v[138:145], v5
v_exp_f32 v106, v106
v_exp_f32 v107, v107
v_exp_f32 v108, v108
v_exp_f32 v109, v109
v_smfmac_f32_32x32x32_bf16 v[50:65], v[166:169], v[98:105], v5
global_load_dwordx4 a[144:147], v4, s[60:61] offset:0
v_exp_f32 v110, v110
v_exp_f32 v111, v111
v_exp_f32 v112, v112
v_exp_f32 v113, v113
v_mfma_f32_32x32x16_bf16 v[130:145], v[160:163], v[28:31], 0
v_exp_f32 v114, v114
v_exp_f32 v115, v115
v_exp_f32 v116, v116
v_exp_f32 v117, v117
v_mfma_f32_32x32x8_bf16 v[130:145], v[164:165], v[32:33], v[130:145]
v_exp_f32 v118, v118
v_exp_f32 v119, v119
v_exp_f32 v120, v120
v_exp_f32 v121, v121
v_smfmac_f32_32x32x32_bf16 v[50:65], v[170:173], v[106:113], v5
global_load_dwordx4 a[148:151], v4, s[60:61] offset:1024
v_exp_f32 v122, v122
v_exp_f32 v123, v123
v_exp_f32 v124, v124
v_exp_f32 v125, v125
v_smfmac_f32_32x32x32_bf16 v[66:81], v[166:169], v[114:121], v5
s_waitcnt vmcnt(14)
v_exp_f32 v126, v126
v_exp_f32 v127, v127
v_exp_f32 v128, v128
v_exp_f32 v129, v129
v_mfma_f32_32x32x16_bf16 v[98:113], v[174:177], v[10:13], 0
v_exp_f32 v130, v130
v_exp_f32 v131, v131
v_exp_f32 v132, v132
v_exp_f32 v133, v133
v_mfma_f32_32x32x8_bf16 v[98:113], v[178:179], v[14:15], v[98:113]
v_exp_f32 v134, v134
v_exp_f32 v135, v135
v_exp_f32 v136, v136
v_exp_f32 v137, v137
v_smfmac_f32_32x32x32_bf16 v[66:81], v[170:173], v[122:129], v5
global_load_dwordx4 a[152:155], v4, s[60:61] offset:2048
v_exp_f32 v138, v138
v_exp_f32 v139, v139
v_exp_f32 v140, v140
v_exp_f32 v141, v141
v_smfmac_f32_32x32x32_bf16 v[82:97], v[166:169], v[130:137], v5
v_exp_f32 v142, v142
v_exp_f32 v143, v143
v_exp_f32 v144, v144
v_exp_f32 v145, v145
v_mfma_f32_32x32x16_bf16 v[114:129], v[174:177], v[16:19], 0
global_load_dwordx4 a[156:159], v4, s[60:61] offset:3072
s_add_u32 s60, s60, 0x2000
s_addc_u32 s61, s61, 0
v_exp_f32 v98, v98
v_exp_f32 v99, v99
v_exp_f32 v100, v100
v_exp_f32 v101, v101
v_mfma_f32_32x32x8_bf16 v[114:129], v[178:179], v[20:21], v[114:129]
v_exp_f32 v102, v102
v_exp_f32 v103, v103
v_exp_f32 v104, v104
v_exp_f32 v105, v105
v_smfmac_f32_32x32x32_bf16 v[82:97], v[170:173], v[138:145], v5
v_exp_f32 v106, v106
v_exp_f32 v107, v107
v_exp_f32 v108, v108
v_exp_f32 v109, v109
v_smfmac_f32_32x32x32_bf16 v[34:49], v[180:183], v[98:105], v5
global_load_dwordx4 a[160:163], v4, s[60:61] offset:-4096
v_exp_f32 v110, v110
v_exp_f32 v111, v111
v_exp_f32 v112, v112
v_exp_f32 v113, v113
v_mfma_f32_32x32x16_bf16 v[130:145], v[174:177], v[22:25], 0
v_exp_f32 v114, v114
v_exp_f32 v115, v115
v_exp_f32 v116, v116
v_exp_f32 v117, v117
v_mfma_f32_32x32x8_bf16 v[130:145], v[178:179], v[26:27], v[130:145]
v_exp_f32 v118, v118
v_exp_f32 v119, v119
v_exp_f32 v120, v120
v_exp_f32 v121, v121
v_smfmac_f32_32x32x32_bf16 v[34:49], v[184:187], v[106:113], v5
global_load_dword v213, v212, s[20:21] sc1
v_exp_f32 v122, v122
v_exp_f32 v123, v123
v_exp_f32 v124, v124
v_exp_f32 v125, v125
v_smfmac_f32_32x32x32_bf16 v[50:65], v[180:183], v[114:121], v5
global_load_dwordx4 a[164:167], v4, s[60:61] offset:-3072
v_exp_f32 v126, v126
v_exp_f32 v127, v127
v_exp_f32 v128, v128
v_exp_f32 v129, v129
v_mfma_f32_32x32x16_bf16 v[98:113], v[174:177], v[28:31], 0
v_exp_f32 v130, v130
v_exp_f32 v131, v131
v_exp_f32 v132, v132
v_exp_f32 v133, v133
v_mfma_f32_32x32x8_bf16 v[98:113], v[178:179], v[32:33], v[98:113]
v_exp_f32 v134, v134
v_exp_f32 v135, v135
v_exp_f32 v136, v136
v_exp_f32 v137, v137
v_smfmac_f32_32x32x32_bf16 v[50:65], v[184:187], v[122:129], v5
global_load_dwordx4 a[168:171], v4, s[60:61] offset:-2048
v_exp_f32 v138, v138
v_exp_f32 v139, v139
v_exp_f32 v140, v140
v_exp_f32 v141, v141
v_smfmac_f32_32x32x32_bf16 v[66:81], v[180:183], v[130:137], v5
s_waitcnt vmcnt(12)
v_exp_f32 v142, v142
v_exp_f32 v143, v143
v_exp_f32 v144, v144
v_exp_f32 v145, v145
v_mfma_f32_32x32x16_bf16 v[114:129], v[188:191], v[22:25], 0
v_exp_f32 v98, v98
v_exp_f32 v99, v99
v_exp_f32 v100, v100
v_exp_f32 v101, v101
v_mfma_f32_32x32x8_bf16 v[114:129], v[192:193], v[26:27], v[114:129]
v_exp_f32 v102, v102
v_exp_f32 v103, v103
v_exp_f32 v104, v104
v_exp_f32 v105, v105
v_smfmac_f32_32x32x32_bf16 v[66:81], v[184:187], v[138:145], v5
global_load_dwordx4 a[172:175], v4, s[60:61] offset:-1024
v_exp_f32 v106, v106
v_exp_f32 v107, v107
v_exp_f32 v108, v108
v_exp_f32 v109, v109
v_smfmac_f32_32x32x32_bf16 v[82:97], v[180:183], v[98:105], v5
v_exp_f32 v110, v110
v_exp_f32 v111, v111
v_exp_f32 v112, v112
v_exp_f32 v113, v113
v_mfma_f32_32x32x16_bf16 v[130:145], v[188:191], v[28:31], 0
v_exp_f32 v114, v114
v_exp_f32 v115, v115
v_exp_f32 v116, v116
v_exp_f32 v117, v117
v_mfma_f32_32x32x8_bf16 v[130:145], v[192:193], v[32:33], v[130:145]
v_exp_f32 v118, v118
v_exp_f32 v119, v119
v_exp_f32 v120, v120
v_exp_f32 v121, v121
)ASM"
R"ASM(v_smfmac_f32_32x32x32_bf16 v[82:97], v[184:187], v[106:113], v5
v_exp_f32 v122, v122
v_exp_f32 v123, v123
v_exp_f32 v124, v124
v_exp_f32 v125, v125
v_smfmac_f32_32x32x32_bf16 v[66:81], v[194:197], v[114:121], v5
v_exp_f32 v126, v126
v_exp_f32 v127, v127
v_exp_f32 v128, v128
v_exp_f32 v129, v129
v_mfma_f32_32x32x16_bf16 v[98:113], v[188:191], v[10:13], 0
v_exp_f32 v130, v130
v_exp_f32 v131, v131
v_exp_f32 v132, v132
v_exp_f32 v133, v133
v_mfma_f32_32x32x8_bf16 v[98:113], v[192:193], v[14:15], v[98:113]
v_exp_f32 v134, v134
v_exp_f32 v135, v135
v_exp_f32 v136, v136
v_exp_f32 v137, v137
v_smfmac_f32_32x32x32_bf16 v[66:81], v[198:201], v[122:129], v5
v_exp_f32 v138, v138
v_exp_f32 v139, v139
v_exp_f32 v140, v140
v_exp_f32 v141, v141
v_smfmac_f32_32x32x32_bf16 v[82:97], v[194:197], v[130:137], v5
v_exp_f32 v142, v142
v_exp_f32 v143, v143
v_exp_f32 v144, v144
v_exp_f32 v145, v145
v_mfma_f32_32x32x16_bf16 v[114:129], v[188:191], v[16:19], 0
s_nop 1
ds_write_b128 v205, v[66:69] offset:0
v_exp_f32 v98, v98
v_exp_f32 v99, v99
v_exp_f32 v100, v100
v_exp_f32 v101, v101
v_mfma_f32_32x32x8_bf16 v[114:129], v[192:193], v[20:21], v[114:129]
ds_write_b128 v205, v[70:73] offset:1024
v_exp_f32 v102, v102
v_exp_f32 v103, v103
v_exp_f32 v104, v104
v_exp_f32 v105, v105
v_smfmac_f32_32x32x32_bf16 v[82:97], v[198:201], v[138:145], v5
ds_write_b128 v205, v[74:77] offset:2048
v_exp_f32 v106, v106
v_exp_f32 v107, v107
v_exp_f32 v108, v108
v_exp_f32 v109, v109
v_smfmac_f32_32x32x32_bf16 v[34:49], v[194:197], v[98:105], v5
v_exp_f32 v110, v110
v_exp_f32 v111, v111
v_exp_f32 v112, v112
v_exp_f32 v113, v113
s_nop 1
ds_write_b128 v205, v[82:85] offset:3072
v_exp_f32 v114, v114
v_exp_f32 v115, v115
v_exp_f32 v116, v116
v_exp_f32 v117, v117
ds_write_b128 v205, v[86:89] offset:4096
v_exp_f32 v118, v118
v_exp_f32 v119, v119
v_exp_f32 v120, v120
v_exp_f32 v121, v121
v_smfmac_f32_32x32x32_bf16 v[34:49], v[198:201], v[106:113], v5
ds_write_b128 v205, v[90:93] offset:5120
s_and_b32 s35, s29, 1
s_mul_i32 s35, s35, 0xffffac00
s_add_u32 s35, s35, 0x2a00
v_add_u32 v6, s35, v202
ds_write_b32 v6, v202
v_exp_f32 v122, v122
v_exp_f32 v123, v123
v_exp_f32 v124, v124
v_exp_f32 v125, v125
v_smfmac_f32_32x32x32_bf16 v[50:65], v[194:197], v[114:121], v5
ds_read_b32 v7, v202
ds_read_b128 v[146:149], v206 offset:0
ds_read_b128 v[150:153], v206 offset:1024
ds_read_b128 v[154:157], v206 offset:2048
ds_read_b128 v[158:161], v206 offset:3072
ds_read_b128 v[162:165], v206 offset:4096
ds_read_b128 v[166:169], v206 offset:5120
v_exp_f32 v126, v126
v_exp_f32 v127, v127
v_exp_f32 v128, v128
v_exp_f32 v129, v129
s_waitcnt lgkmcnt(6)
v_cmp_eq_u32 vcc, 0, v7
s_mov_b32 s30, 0x7a12000
s_cbranch_vccnz Lcs_1_%=
Lcr_2_%=:
s_waitcnt lgkmcnt(5)
v_add_f32 v34, v34, v146
v_add_f32 v35, v35, v147
v_add_f32 v36, v36, v148
v_add_f32 v37, v37, v149
s_waitcnt lgkmcnt(4)
v_add_f32 v38, v38, v150
v_add_f32 v39, v39, v151
v_add_f32 v40, v40, v152
v_add_f32 v41, v41, v153
s_waitcnt lgkmcnt(3)
v_add_f32 v42, v42, v154
v_add_f32 v43, v43, v155
v_add_f32 v44, v44, v156
v_add_f32 v45, v45, v157
v_smfmac_f32_32x32x32_bf16 v[50:65], v[198:201], v[122:129], v5
; ==== add the partner wave's key half, check the row sums (run-time guard of the unshifted exponentials), normalise, publish O
v_mov_b32 v98, v42
v_mov_b32 v102, v42
s_waitcnt lgkmcnt(2)
s_nop 8
v_pk_add_f32 v[50:51], v[50:51], v[158:159]
v_pk_add_f32 v[52:53], v[52:53], v[160:161]
s_waitcnt lgkmcnt(1)
v_pk_add_f32 v[54:55], v[54:55], v[162:163]
v_pk_add_f32 v[56:57], v[56:57], v[164:165]
s_waitcnt lgkmcnt(0)
v_pk_add_f32 v[58:59], v[58:59], v[166:167]
v_pk_add_f32 v[60:61], v[60:61], v[168:169]
v_permlane32_swap_b32 v102, v98
v_mov_b32 v100, v58
v_mov_b32 v103, v58
s_nop 1
v_permlane32_swap_b32 v103, v100
v_mov_b32 v6, 0x21800000
v_mov_b32 v7, 0x5d800000
v_med3_f32 v8, v98, v6, v7
v_med3_f32 v9, v100, v6, v7
v_cmp_eq_f32 s[46:47], v8, v98
v_cmp_eq_f32 vcc, v9, v100
v_rcp_f32 v98, v98
v_rcp_f32 v100, v100
s_and_b64 s[46:47], s[46:47], vcc
s_cmp_eq_u64 s[46:47], -1
s_cbranch_scc0 Lfb_3_%=
Lback_4_%=:
s_waitcnt vmcnt(3)
v_cmp_ne_u32 vcc, 16, v213
s_cbranch_vccnz Lfs_5_%=
Lfr_6_%=:
v_pk_mul_f32 v[42:43], v[42:43], v[98:99] op_sel_hi:[1,0]
v_pk_mul_f32 v[44:45], v[44:45], v[98:99] op_sel_hi:[1,0]
v_cvt_pk_bf16_f32 v112, v42, v43
v_cvt_pk_bf16_f32 v113, v44, v45
v_pk_mul_f32 v[58:59], v[58:59], v[100:101] op_sel_hi:[1,0]
v_pk_mul_f32 v[60:61], v[60:61], v[100:101] op_sel_hi:[1,0]
v_cvt_pk_bf16_f32 v114, v58, v59
v_cvt_pk_bf16_f32 v115, v60, v61
v_pk_mul_f32 v[34:35], v[34:35], v[98:99] op_sel_hi:[1,0]
v_pk_mul_f32 v[36:37], v[36:37], v[98:99] op_sel_hi:[1,0]
v_pk_mul_f32 v[38:39], v[38:39], v[98:99] op_sel_hi:[1,0]
v_pk_mul_f32 v[40:41], v[40:41], v[98:99] op_sel_hi:[1,0]
v_permlane32_swap_b32 v112, v114
v_permlane32_swap_b32 v113, v115
ds_write_b64 v214, v[112:113]
s_and_b32 s35, s29, 2
s_mul_i32 s35, s35, 0xffffac00
s_add_u32 s35, s35, 0x5400
v_add_u32 v6, s35, v202
ds_write_b32 v6, v202 offset:4
v_cvt_pk_bf16_f32 v104, v34, v35
v_cvt_pk_bf16_f32 v105, v36, v37
v_cvt_pk_bf16_f32 v106, v38, v39
v_cvt_pk_bf16_f32 v107, v40, v41
global_store_dwordx4 v203, v[104:107], s[4:5] sc1
ds_read_b32 v7, v202 offset:4
ds_read_b128 v[116:119], v215
v_pk_mul_f32 v[50:51], v[50:51], v[100:101] op_sel_hi:[1,0]
v_pk_mul_f32 v[52:53], v[52:53], v[100:101] op_sel_hi:[1,0]
v_pk_mul_f32 v[54:55], v[54:55], v[100:101] op_sel_hi:[1,0]
v_pk_mul_f32 v[56:57], v[56:57], v[100:101] op_sel_hi:[1,0]
v_cvt_pk_bf16_f32 v108, v50, v51
v_cvt_pk_bf16_f32 v109, v52, v53
v_cvt_pk_bf16_f32 v110, v54, v55
v_cvt_pk_bf16_f32 v111, v56, v57
s_waitcnt lgkmcnt(1)
v_cmp_eq_u32 vcc, 0, v7
s_mov_b32 s30, 0x7a12000
s_cbranch_vccnz Lcs_7_%=
Lcr_8_%=:
s_mov_b32 exec_hi, 0
s_waitcnt lgkmcnt(0)
global_store_dwordx4 v204, v[116:119], s[8:9] sc1
s_mov_b64 exec, -1
global_store_dwordx4 v203, v[108:111], s[6:7] sc1
; ==== output projection: h = x + O Wo, K split over the four waves, one extra column tile yields the row mean
s_add_u32 s37, s57, s54
v_and_b32 v14, 15, v0
v_lshrrev_b32 v15, 4, v0
v_lshlrev_b32 v210, 2, v14
v_mul_u32_u24 v14, 0x500, v14
v_lshl_add_u32 v1, v15, 3, v14
s_mul_i32 s35, s37, 0x5000
s_mul_i32 s36, s34, 0xa0
s_add_u32 s35, s35, s36
s_add_u32 s40, s22, s35
s_addc_u32 s41, s23, 0
)ASM"
R"ASM(s_add_u32 s24, s26, s35
s_addc_u32 s25, s27, 0
global_load_dwordx2 v[186:187], v1, s[40:41] offset:0
global_load_dwordx2 v[188:189], v1, s[40:41] offset:32
global_load_dwordx2 v[190:191], v1, s[40:41] offset:64
global_load_dwordx2 v[192:193], v1, s[40:41] offset:96
global_load_dwordx2 v[194:195], v1, s[40:41] offset:128
s_lshl_b32 s35, s37, 6
s_add_u32 s35, s35, 0xe000
s_add_u32 s44, s20, s35
s_addc_u32 s45, s21, 0
global_load_dword v210, v210, s[44:45]
s_add_u32 s35, s29, 1
s_and_b32 s35, s35, 3
s_mul_i32 s36, s35, 0x2a00
s_add_u32 s36, s36, 0x1b000
v_mov_b32 v26, s36
s_add_u32 s36, s36, 0xffffd800
v_add_u32 v10, s36, v4
s_add_u32 s35, s29, 2
s_and_b32 s35, s35, 3
s_mul_i32 s36, s35, 0x2a00
s_add_u32 s36, s36, 0x1b000
v_mov_b32 v27, s36
s_add_u32 s36, s36, 0xffffec00
v_add_u32 v11, s36, v4
s_add_u32 s35, s29, 3
s_and_b32 s35, s35, 3
s_mul_i32 s36, s35, 0x2a00
s_add_u32 s36, s36, 0x1b000
v_mov_b32 v28, s36
s_mul_i32 s36, s35, 0x1400
s_add_u32 s36, s36, 0x23000
v_add_u32 v12, s36, v4
s_mul_i32 s48, s29, 0x2a00
s_add_u32 s48, s48, 0x18800
v_add_u32 v13, s48, v4
s_add_u32 s35, s48, 0x2800
v_mov_b32 v29, s35
s_mul_i32 s36, s29, 0x1400
s_add_u32 s36, s36, 0x23000
v_add_u32 v33, s36, v4
v_mov_b32 v30, 0
ds_write_b32 v29, v30 offset:192
ds_write_b32 v29, v30 offset:196
ds_write_b32 v29, v30 offset:200
s_mov_b32 s31, 0x7a12000
v_and_b32 v25, 15, v0
v_lshlrev_b32 v25, 2, v25
v_add_u32 v24, v25, v29
s_add_u32 s38, s52, s48
s_addc_u32 s39, s53, 0
s_mul_i32 s35, s37, 0x5000
s_mul_i32 s36, s34, 0xa00
s_add_u32 s35, s35, s36
s_add_u32 s35, s35, 0xfa4000
s_add_u32 s4, s20, s35
s_addc_u32 s5, s21, 0
v_and_b32 v14, 0xf0, v4
v_lshrrev_b32 v15, 5, v0
v_lshl_add_u32 v18, v15, 8, v14
v_bfe_u32 v15, v0, 4, 1
v_lshl_add_u32 v17, v15, 9, v18
s_lshl_b32 s35, s37, 3
s_add_u32 s35, s35, s34
s_lshl_b32 s35, s35, 6
s_add_u32 s35, s35, 0x1224000
s_add_u32 s8, s20, s35
s_addc_u32 s9, s21, 0
v_and_b32 v14, 7, v0
v_mul_u32_u24 v14, 0xa00, v14
v_bfe_u32 v15, v0, 3, 1
v_mul_u32_u24 v15, 0x5000, v15
v_add_u32 v14, v14, v15
s_lshl_b32 s35, s55, 3
s_lshl_b32 s36, s29, 1
s_add_u32 s35, s35, s36
s_mul_i32 s35, s35, 0x5000
s_add_u32 s35, s35, 0xfa4400
v_add_u32 v16, s35, v14
s_waitcnt lgkmcnt(0)
s_barrier
global_load_dword v7, v207, s[20:21] sc1
global_load_dwordx4 v[34:37], v4, s[10:11] offset:-2048 sc1
global_load_dwordx4 v[38:41], v4, s[10:11] offset:-1024 sc1
global_load_dwordx4 v[42:45], v4, s[10:11] offset:0 sc1
s_waitcnt vmcnt(0)
v_max3_u32 v7, v7, v34, v35
v_max3_u32 v7, v7, v36, v37
v_max3_u32 v7, v7, v38, v39
v_max3_u32 v7, v7, v40, v41
v_max3_u32 v7, v7, v42, v43
v_max3_u32 v7, v7, v44, v45
v_cmp_eq_u32 vcc, -1, v7
v_mov_b32 v6, 0
s_cbranch_vccnz Lg_9_%=
Lgd_10_%=:
global_load_dwordx4 v[46:49], v4, s[10:11] offset:1024 sc1
global_load_dwordx4 v[50:53], v4, s[10:11] offset:2048 sc1
global_load_dwordx4 v[54:57], v4, s[12:13] offset:-2048 sc1
v_mfma_f32_16x16x32_bf16 v[114:117], a[176:179], v[34:37], 0
global_load_dwordx4 v[58:61], v4, s[12:13] offset:-1024 sc1
v_mfma_f32_16x16x32_bf16 v[118:121], a[180:183], v[34:37], 0
v_max3_u32 v6, v6, v34, v35
v_max3_u32 v6, v6, v36, v37
v_mfma_f32_16x16x32_bf16 v[122:125], a[184:187], v[34:37], 0
v_lshlrev_b32 v162, 16, v186
v_and_b32 v163, 0xffff0000, v186
v_mfma_f32_16x16x32_bf16 v[126:129], a[188:191], v[34:37], 0
v_lshlrev_b32 v164, 16, v187
v_and_b32 v165, 0xffff0000, v187
v_mfma_f32_16x16x32_bf16 v[130:133], a[192:195], v[34:37], 0
v_lshlrev_b32 v166, 16, v188
v_and_b32 v167, 0xffff0000, v188
v_mfma_f32_16x16x32_bf16 v[134:137], a[196:199], v[34:37], 0
v_lshlrev_b32 v168, 16, v189
v_and_b32 v169, 0xffff0000, v189
v_mfma_f32_16x16x32_bf16 v[114:117], a[200:203], v[38:41], v[114:117]
global_load_dwordx4 v[62:65], v4, s[12:13] offset:0 sc1
v_mfma_f32_16x16x32_bf16 v[118:121], a[204:207], v[38:41], v[118:121]
v_max3_u32 v6, v6, v38, v39
v_max3_u32 v6, v6, v40, v41
v_mfma_f32_16x16x32_bf16 v[122:125], a[208:211], v[38:41], v[122:125]
v_lshlrev_b32 v170, 16, v190
v_and_b32 v171, 0xffff0000, v190
v_mfma_f32_16x16x32_bf16 v[126:129], a[212:215], v[38:41], v[126:129]
v_lshlrev_b32 v172, 16, v191
v_and_b32 v173, 0xffff0000, v191
v_mfma_f32_16x16x32_bf16 v[130:133], a[216:219], v[38:41], v[130:133]
v_lshlrev_b32 v174, 16, v192
v_and_b32 v175, 0xffff0000, v192
v_mfma_f32_16x16x32_bf16 v[134:137], a[220:223], v[38:41], v[134:137]
v_lshlrev_b32 v176, 16, v193
v_and_b32 v177, 0xffff0000, v193
v_mfma_f32_16x16x32_bf16 v[114:117], a[224:227], v[42:45], v[114:117]
global_load_dwordx4 v[66:69], v4, s[12:13] offset:1024 sc1
v_mfma_f32_16x16x32_bf16 v[118:121], a[228:231], v[42:45], v[118:121]
v_max3_u32 v6, v6, v42, v43
v_max3_u32 v6, v6, v44, v45
v_mfma_f32_16x16x32_bf16 v[122:125], a[232:235], v[42:45], v[122:125]
v_lshlrev_b32 v178, 16, v194
v_and_b32 v179, 0xffff0000, v194
v_mfma_f32_16x16x32_bf16 v[126:129], a[236:239], v[42:45], v[126:129]
v_lshlrev_b32 v180, 16, v195
v_and_b32 v181, 0xffff0000, v195
v_mfma_f32_16x16x32_bf16 v[130:133], a[240:243], v[42:45], v[130:133]
v_mfma_f32_16x16x32_bf16 v[134:137], a[244:247], v[42:45], v[134:137]
s_waitcnt vmcnt(5)
v_mfma_f32_16x16x32_bf16 v[114:117], a[248:251], v[46:49], v[114:117]
global_load_dwordx4 v[70:73], v4, s[12:13] offset:2048 sc1
v_mfma_f32_16x16x32_bf16 v[118:121], a[252:255], v[46:49], v[118:121]
v_max3_u32 v6, v6, v46, v47
v_max3_u32 v6, v6, v48, v49
v_mfma_f32_16x16x32_bf16 v[122:125], v[216:219], v[46:49], v[122:125]
v_mfma_f32_16x16x32_bf16 v[126:129], v[220:223], v[46:49], v[126:129]
v_mfma_f32_16x16x32_bf16 v[130:133], v[224:227], v[46:49], v[130:133]
v_mfma_f32_16x16x32_bf16 v[134:137], v[228:231], v[46:49], v[134:137]
s_waitcnt vmcnt(5)
v_mfma_f32_16x16x32_bf16 v[114:117], v[232:235], v[50:53], v[114:117]
global_load_dwordx4 v[74:77], v4, s[14:15] offset:-2048 sc1
v_mfma_f32_16x16x32_bf16 v[118:121], v[236:239], v[50:53], v[118:121]
v_max3_u32 v6, v6, v50, v51
v_max3_u32 v6, v6, v52, v53
v_mfma_f32_16x16x32_bf16 v[122:125], v[240:243], v[50:53], v[122:125]
v_mfma_f32_16x16x32_bf16 v[126:129], v[244:247], v[50:53], v[126:129]
v_mfma_f32_16x16x32_bf16 v[130:133], v[248:251], v[50:53], v[130:133]
v_cmp_eq_u32 vcc, -1, v6
v_mov_b32 v6, 0
v_mfma_f32_16x16x32_bf16 v[134:137], v[252:255], v[50:53], v[134:137]
s_cbranch_vccnz Lst_11_%=
Lsr_12_%=:
s_waitcnt vmcnt(5)
v_mfma_f32_16x16x32_bf16 v[138:141], a[176:179], v[54:57], 0
global_load_dwordx4 v[78:81], v4, s[14:15] offset:-1024 sc1
v_mfma_f32_16x16x32_bf16 v[142:145], a[180:183], v[54:57], 0
v_max3_u32 v6, v6, v54, v55
v_max3_u32 v6, v6, v56, v57
v_mfma_f32_16x16x32_bf16 v[146:149], a[184:187], v[54:57], 0
v_mfma_f32_16x16x32_bf16 v[150:153], a[188:191], v[54:57], 0
v_mfma_f32_16x16x32_bf16 v[154:157], a[192:195], v[54:57], 0
v_add_f32 v114, v114, v115
v_add_u32 v20, v25, v26
v_mfma_f32_16x16x32_bf16 v[158:161], a[196:199], v[54:57], 0
ds_write_b128 v10, v[118:121] offset:0
s_waitcnt vmcnt(5)
v_mfma_f32_16x16x32_bf16 v[138:141], a[200:203], v[58:61], v[138:141]
global_load_dwordx4 v[82:85], v4, s[14:15] offset:0 sc1
v_mfma_f32_16x16x32_bf16 v[142:145], a[204:207], v[58:61], v[142:145]
v_max3_u32 v6, v6, v58, v59
v_max3_u32 v6, v6, v60, v61
v_mfma_f32_16x16x32_bf16 v[146:149], a[208:211], v[58:61], v[146:149]
ds_write_b128 v10, v[122:125] offset:1024
v_mfma_f32_16x16x32_bf16 v[150:153], a[212:215], v[58:61], v[150:153]
v_add_f32 v114, v114, v116
ds_write_b32 v20, v114 offset:0
v_mfma_f32_16x16x32_bf16 v[154:157], a[216:219], v[58:61], v[154:157]
ds_write_b128 v10, v[126:129] offset:2048
v_mfma_f32_16x16x32_bf16 v[158:161], a[220:223], v[58:61], v[158:161]
ds_write_b128 v10, v[130:133] offset:3072
s_waitcnt vmcnt(5)
)ASM"
R"ASM(v_mfma_f32_16x16x32_bf16 v[138:141], a[224:227], v[62:65], v[138:141]
global_load_dwordx4 v[86:89], v4, s[14:15] offset:1024 sc1
v_mfma_f32_16x16x32_bf16 v[142:145], a[228:231], v[62:65], v[142:145]
v_max3_u32 v6, v6, v62, v63
v_max3_u32 v6, v6, v64, v65
v_mfma_f32_16x16x32_bf16 v[146:149], a[232:235], v[62:65], v[146:149]
ds_write_b128 v10, v[134:137] offset:4096
v_mfma_f32_16x16x32_bf16 v[150:153], a[236:239], v[62:65], v[150:153]
ds_write_b32 v26, v29 offset:192
v_mfma_f32_16x16x32_bf16 v[154:157], a[240:243], v[62:65], v[154:157]
v_mfma_f32_16x16x32_bf16 v[158:161], a[244:247], v[62:65], v[158:161]
s_waitcnt vmcnt(5)
v_mfma_f32_16x16x32_bf16 v[138:141], a[248:251], v[66:69], v[138:141]
global_load_dwordx4 v[90:93], v4, s[14:15] offset:2048 sc1
v_mfma_f32_16x16x32_bf16 v[142:145], a[252:255], v[66:69], v[142:145]
v_max3_u32 v6, v6, v66, v67
v_max3_u32 v6, v6, v68, v69
v_mfma_f32_16x16x32_bf16 v[146:149], v[216:219], v[66:69], v[146:149]
v_mfma_f32_16x16x32_bf16 v[150:153], v[220:223], v[66:69], v[150:153]
v_mfma_f32_16x16x32_bf16 v[154:157], v[224:227], v[66:69], v[154:157]
v_mfma_f32_16x16x32_bf16 v[158:161], v[228:231], v[66:69], v[158:161]
s_waitcnt vmcnt(5)
v_mfma_f32_16x16x32_bf16 v[138:141], v[232:235], v[70:73], v[138:141]
global_load_dwordx4 v[94:97], v4, s[16:17] offset:-2048 sc1
v_mfma_f32_16x16x32_bf16 v[142:145], v[236:239], v[70:73], v[142:145]
v_max3_u32 v6, v6, v70, v71
v_max3_u32 v6, v6, v72, v73
v_mfma_f32_16x16x32_bf16 v[146:149], v[240:243], v[70:73], v[146:149]
v_mfma_f32_16x16x32_bf16 v[150:153], v[244:247], v[70:73], v[150:153]
v_mfma_f32_16x16x32_bf16 v[154:157], v[248:251], v[70:73], v[154:157]
v_cmp_eq_u32 vcc, -1, v6
v_mov_b32 v6, 0
v_mfma_f32_16x16x32_bf16 v[158:161], v[252:255], v[70:73], v[158:161]
s_cbranch_vccnz Lst_13_%=
Lsr_14_%=:
s_waitcnt vmcnt(5)
v_mfma_f32_16x16x32_bf16 v[114:117], a[176:179], v[74:77], 0
global_load_dwordx4 v[98:101], v4, s[16:17] offset:-1024 sc1
v_mfma_f32_16x16x32_bf16 v[118:121], a[180:183], v[74:77], 0
v_max3_u32 v6, v6, v74, v75
v_max3_u32 v6, v6, v76, v77
v_mfma_f32_16x16x32_bf16 v[122:125], a[184:187], v[74:77], 0
v_mfma_f32_16x16x32_bf16 v[126:129], a[188:191], v[74:77], 0
v_mfma_f32_16x16x32_bf16 v[130:133], a[192:195], v[74:77], 0
v_add_f32 v138, v138, v139
v_add_u32 v20, v25, v27
v_mfma_f32_16x16x32_bf16 v[134:137], a[196:199], v[74:77], 0
ds_write_b128 v11, v[142:145] offset:0
s_waitcnt vmcnt(5)
v_mfma_f32_16x16x32_bf16 v[114:117], a[200:203], v[78:81], v[114:117]
global_load_dwordx4 v[102:105], v4, s[16:17] offset:0 sc1
v_mfma_f32_16x16x32_bf16 v[118:121], a[204:207], v[78:81], v[118:121]
v_max3_u32 v6, v6, v78, v79
v_max3_u32 v6, v6, v80, v81
v_mfma_f32_16x16x32_bf16 v[122:125], a[208:211], v[78:81], v[122:125]
ds_write_b128 v11, v[146:149] offset:1024
v_mfma_f32_16x16x32_bf16 v[126:129], a[212:215], v[78:81], v[126:129]
v_add_f32 v138, v138, v140
ds_write_b32 v20, v138 offset:64
v_mfma_f32_16x16x32_bf16 v[130:133], a[216:219], v[78:81], v[130:133]
ds_write_b128 v11, v[150:153] offset:2048
v_mfma_f32_16x16x32_bf16 v[134:137], a[220:223], v[78:81], v[134:137]
ds_write_b128 v11, v[154:157] offset:3072
s_waitcnt vmcnt(5)
v_mfma_f32_16x16x32_bf16 v[114:117], a[224:227], v[82:85], v[114:117]
global_load_dwordx4 v[106:109], v4, s[16:17] offset:1024 sc1
v_mfma_f32_16x16x32_bf16 v[118:121], a[228:231], v[82:85], v[118:121]
v_max3_u32 v6, v6, v82, v83
v_max3_u32 v6, v6, v84, v85
v_mfma_f32_16x16x32_bf16 v[122:125], a[232:235], v[82:85], v[122:125]
ds_write_b128 v11, v[158:161] offset:4096
v_mfma_f32_16x16x32_bf16 v[126:129], a[236:239], v[82:85], v[126:129]
ds_write_b32 v27, v29 offset:196
v_mfma_f32_16x16x32_bf16 v[130:133], a[240:243], v[82:85], v[130:133]
ds_read_b32 v30, v29 offset:192
v_mfma_f32_16x16x32_bf16 v[134:137], a[244:247], v[82:85], v[134:137]
ds_read_b128 v[186:189], v13 offset:0
s_waitcnt vmcnt(5)
v_mfma_f32_16x16x32_bf16 v[114:117], a[248:251], v[86:89], v[114:117]
global_load_dwordx4 v[110:113], v4, s[16:17] offset:2048 sc1
v_mfma_f32_16x16x32_bf16 v[118:121], a[252:255], v[86:89], v[118:121]
v_max3_u32 v6, v6, v86, v87
v_max3_u32 v6, v6, v88, v89
v_mfma_f32_16x16x32_bf16 v[122:125], v[216:219], v[86:89], v[122:125]
ds_read_b128 v[190:193], v13 offset:1024
v_mfma_f32_16x16x32_bf16 v[126:129], v[220:223], v[86:89], v[126:129]
ds_read_b128 v[194:197], v13 offset:2048
v_mfma_f32_16x16x32_bf16 v[130:133], v[224:227], v[86:89], v[130:133]
ds_read_b128 v[198:201], v13 offset:3072
v_mfma_f32_16x16x32_bf16 v[134:137], v[228:231], v[86:89], v[134:137]
ds_read_b128 v[202:205], v13 offset:4096
s_waitcnt vmcnt(5)
v_mfma_f32_16x16x32_bf16 v[114:117], v[232:235], v[90:93], v[114:117]
ds_read_b32 v21, v24 offset:0
v_mfma_f32_16x16x32_bf16 v[118:121], v[236:239], v[90:93], v[118:121]
v_max3_u32 v6, v6, v90, v91
v_max3_u32 v6, v6, v92, v93
v_mfma_f32_16x16x32_bf16 v[122:125], v[240:243], v[90:93], v[122:125]
v_mfma_f32_16x16x32_bf16 v[126:129], v[244:247], v[90:93], v[126:129]
v_mfma_f32_16x16x32_bf16 v[130:133], v[248:251], v[90:93], v[130:133]
v_cmp_eq_u32 vcc, -1, v6
v_mov_b32 v6, 0
v_mfma_f32_16x16x32_bf16 v[134:137], v[252:255], v[90:93], v[134:137]
s_waitcnt lgkmcnt(6)
v_cmp_eq_u32 s[46:47], 0, v30
s_nop 0
s_cmp_lg_u64 s[46:47], 0
s_cbranch_scc1 Lfs_15_%=
Lfr_16_%=:
s_cbranch_vccnz Lst_17_%=
Lsr_18_%=:
s_waitcnt vmcnt(4)
v_mfma_f32_16x16x32_bf16 v[138:141], a[176:179], v[94:97], 0
ds_read_b32 v31, v29 offset:196
s_waitcnt lgkmcnt(6)
v_mfma_f32_16x16x32_bf16 v[142:145], a[180:183], v[94:97], v[186:189]
v_max3_u32 v6, v6, v94, v95
v_max3_u32 v6, v6, v96, v97
s_waitcnt lgkmcnt(5)
v_mfma_f32_16x16x32_bf16 v[146:149], a[184:187], v[94:97], v[190:193]
ds_read_b128 v[34:37], v13 offset:5120
s_waitcnt lgkmcnt(5)
v_mfma_f32_16x16x32_bf16 v[150:153], a[188:191], v[94:97], v[194:197]
ds_read_b128 v[38:41], v13 offset:6144
s_waitcnt lgkmcnt(5)
v_mfma_f32_16x16x32_bf16 v[154:157], a[192:195], v[94:97], v[198:201]
s_waitcnt lgkmcnt(4)
v_add_f32 v114, v114, v115
v_add_u32 v20, v25, v28
v_mfma_f32_16x16x32_bf16 v[158:161], a[196:199], v[94:97], v[202:205]
ds_write_b128 v12, v[118:121] offset:0
s_waitcnt vmcnt(3)
v_mfma_f32_16x16x32_bf16 v[138:141], a[200:203], v[98:101], v[138:141]
s_add_u32 m0, s48, 0
s_add_u32 s60, s38, 0
s_addc_u32 s61, s39, 0
global_load_lds_dwordx4 v4, s[60:61]
v_mfma_f32_16x16x32_bf16 v[142:145], a[204:207], v[98:101], v[142:145]
v_max3_u32 v6, v6, v98, v99
v_max3_u32 v6, v6, v100, v101
v_mfma_f32_16x16x32_bf16 v[146:149], a[208:211], v[98:101], v[146:149]
ds_write_b128 v12, v[122:125] offset:1024
v_mfma_f32_16x16x32_bf16 v[150:153], a[212:215], v[98:101], v[150:153]
v_add_f32 v114, v114, v116
ds_write_b32 v20, v114 offset:128
v_mfma_f32_16x16x32_bf16 v[154:157], a[216:219], v[98:101], v[154:157]
s_add_u32 m0, s48, 0x400
s_add_u32 s60, s38, 0x400
s_addc_u32 s61, s39, 0
global_load_lds_dwordx4 v4, s[60:61]
v_mfma_f32_16x16x32_bf16 v[158:161], a[220:223], v[98:101], v[158:161]
ds_write_b128 v12, v[126:129] offset:2048
s_waitcnt vmcnt(4)
v_mfma_f32_16x16x32_bf16 v[138:141], a[224:227], v[102:105], v[138:141]
ds_write_b128 v12, v[130:133] offset:3072
v_mfma_f32_16x16x32_bf16 v[142:145], a[228:231], v[102:105], v[142:145]
v_max3_u32 v6, v6, v102, v103
v_max3_u32 v6, v6, v104, v105
v_mfma_f32_16x16x32_bf16 v[146:149], a[232:235], v[102:105], v[146:149]
s_add_u32 m0, s48, 0x800
s_add_u32 s60, s38, 0x800
s_addc_u32 s61, s39, 0
global_load_lds_dwordx4 v4, s[60:61]
v_mfma_f32_16x16x32_bf16 v[150:153], a[236:239], v[102:105], v[150:153]
ds_write_b128 v12, v[134:137] offset:4096
v_mfma_f32_16x16x32_bf16 v[154:157], a[240:243], v[102:105], v[154:157]
ds_write_b32 v28, v29 offset:200
v_mfma_f32_16x16x32_bf16 v[158:161], a[244:247], v[102:105], v[158:161]
s_add_u32 m0, s48, 0xc00
s_add_u32 s60, s38, 0xc00
s_addc_u32 s61, s39, 0
global_load_lds_dwordx4 v4, s[60:61]
s_waitcnt vmcnt(5)
v_mfma_f32_16x16x32_bf16 v[138:141], a[248:251], v[106:109], v[138:141]
ds_read_b128 v[42:45], v13 offset:7168
v_mfma_f32_16x16x32_bf16 v[142:145], a[252:255], v[106:109], v[142:145]
v_max3_u32 v6, v6, v106, v107
v_max3_u32 v6, v6, v108, v109
v_mfma_f32_16x16x32_bf16 v[146:149], v[216:219], v[106:109], v[146:149]
ds_read_b128 v[46:49], v13 offset:8192
v_mfma_f32_16x16x32_bf16 v[150:153], v[220:223], v[106:109], v[150:153]
ds_read_b128 v[50:53], v13 offset:9216
v_mfma_f32_16x16x32_bf16 v[154:157], v[224:227], v[106:109], v[154:157]
ds_read_b32 v22, v24 offset:64
v_mfma_f32_16x16x32_bf16 v[158:161], v[228:231], v[106:109], v[158:161]
s_waitcnt lgkmcnt(13)
v_cmp_eq_u32 s[46:47], 0, v31
s_nop 0
s_cmp_lg_u64 s[46:47], 0
s_cbranch_scc1 Lfs_19_%=
Lfr_20_%=:
s_waitcnt vmcnt(4)
v_mfma_f32_16x16x32_bf16 v[138:141], v[232:235], v[110:113], v[138:141]
s_add_u32 m0, s48, 0x1000
s_add_u32 s60, s38, 0x1000
s_addc_u32 s61, s39, 0
global_load_lds_dwordx4 v4, s[60:61]
v_mfma_f32_16x16x32_bf16 v[142:145], v[236:239], v[110:113], v[142:145]
v_max3_u32 v6, v6, v110, v111
v_max3_u32 v6, v6, v112, v113
)ASM"
R"ASM(v_mfma_f32_16x16x32_bf16 v[146:149], v[240:243], v[110:113], v[146:149]
ds_read_b32 v32, v29 offset:200
v_mfma_f32_16x16x32_bf16 v[150:153], v[244:247], v[110:113], v[150:153]
ds_read_b128 v[54:57], v33 offset:0
v_mfma_f32_16x16x32_bf16 v[154:157], v[248:251], v[110:113], v[154:157]
v_cmp_eq_u32 vcc, -1, v6
v_mov_b32 v6, 0
v_mfma_f32_16x16x32_bf16 v[158:161], v[252:255], v[110:113], v[158:161]
ds_read_b128 v[58:61], v33 offset:1024
s_cbranch_vccnz Lst_23_%=
Lsr_24_%=:
ds_read_b128 v[62:65], v33 offset:2048
ds_read_b128 v[66:69], v33 offset:3072
ds_read_b128 v[70:73], v33 offset:4096
ds_read_b32 v23, v24 offset:128
s_waitcnt lgkmcnt(8)
v_pk_add_f32 v[162:163], v[162:163], v[34:35]
v_pk_add_f32 v[164:165], v[164:165], v[36:37]
s_add_u32 m0, s48, 0x1400
s_add_u32 s60, s38, 0x1400
s_addc_u32 s61, s39, 0
global_load_lds_dwordx4 v4, s[60:61]
v_pk_add_f32 v[166:167], v[166:167], v[38:39]
v_pk_add_f32 v[168:169], v[168:169], v[40:41]
s_add_u32 m0, s48, 0x1800
s_add_u32 s60, s38, 0x1800
s_addc_u32 s61, s39, 0
global_load_lds_dwordx4 v4, s[60:61]
v_pk_add_f32 v[170:171], v[170:171], v[42:43]
v_pk_add_f32 v[172:173], v[172:173], v[44:45]
s_add_u32 m0, s48, 0x1c00
s_add_u32 s60, s38, 0x1c00
s_addc_u32 s61, s39, 0
global_load_lds_dwordx4 v4, s[60:61]
v_pk_add_f32 v[174:175], v[174:175], v[46:47]
v_pk_add_f32 v[176:177], v[176:177], v[48:49]
s_add_u32 m0, s48, 0x2000
s_add_u32 s60, s38, 0x2000
s_addc_u32 s61, s39, 0
global_load_lds_dwordx4 v4, s[60:61]
v_pk_add_f32 v[178:179], v[178:179], v[50:51]
v_pk_add_f32 v[180:181], v[180:181], v[52:53]
s_add_u32 m0, s48, 0x2400
s_add_u32 s60, s38, 0x2400
s_addc_u32 s61, s39, 0
global_load_lds_dwordx4 v4, s[60:61]
s_waitcnt lgkmcnt(6)
v_cmp_eq_u32 s[46:47], 0, v32
s_nop 0
s_cmp_lg_u64 s[46:47], 0
s_cbranch_scc1 Lfs_21_%=
Lfr_22_%=:
s_waitcnt lgkmcnt(5)
v_pk_add_f32 v[162:163], v[162:163], v[54:55]
v_pk_add_f32 v[164:165], v[164:165], v[56:57]
s_waitcnt lgkmcnt(4)
v_pk_add_f32 v[166:167], v[166:167], v[58:59]
v_pk_add_f32 v[168:169], v[168:169], v[60:61]
s_waitcnt lgkmcnt(3)
v_pk_add_f32 v[170:171], v[170:171], v[62:63]
v_pk_add_f32 v[172:173], v[172:173], v[64:65]
s_waitcnt lgkmcnt(2)
v_pk_add_f32 v[174:175], v[174:175], v[66:67]
v_pk_add_f32 v[176:177], v[176:177], v[68:69]
s_waitcnt lgkmcnt(1)
v_pk_add_f32 v[178:179], v[178:179], v[70:71]
v_pk_add_f32 v[180:181], v[180:181], v[72:73]
s_waitcnt lgkmcnt(0)
s_add_u32 m0, s48, 0x2600
s_add_u32 s60, s38, 0x2600
s_addc_u32 s61, s39, 0
global_load_lds_dwordx4 v4, s[60:61]
; ==== publish h - mean(h) and the partial sums of squares
v_add_f32 v210, v210, v21
v_add_f32 v22, v22, v23
v_add_f32 v210, v210, v22
v_add_f32 v138, v138, v139
v_add_f32 v210, v210, v140
v_add_f32 v210, v210, v138
v_mul_f32 v14, 0xbacccccd, v210
v_pk_add_f32 v[228:229], v[158:159], v[178:179]
v_pk_add_f32 v[50:51], v[228:229], v[14:15] op_sel_hi:[1,0]
v_cvt_pk_bf16_f32 v66, v50, v51
v_mov_b32 v68, v66
v_pk_add_f32 v[230:231], v[160:161], v[180:181]
v_pk_add_f32 v[52:53], v[230:231], v[14:15] op_sel_hi:[1,0]
v_cvt_pk_bf16_f32 v67, v52, v53
v_mov_b32 v69, v67
v_pk_add_f32 v[212:213], v[142:143], v[162:163]
v_pk_add_f32 v[34:35], v[212:213], v[14:15] op_sel_hi:[1,0]
v_cvt_pk_bf16_f32 v58, v34, v35
v_pk_add_f32 v[214:215], v[144:145], v[164:165]
v_pk_add_f32 v[36:37], v[214:215], v[14:15] op_sel_hi:[1,0]
v_cvt_pk_bf16_f32 v59, v36, v37
v_permlane16_swap_b32 v66, v68
v_permlane16_swap_b32 v67, v69
v_pk_add_f32 v[216:217], v[146:147], v[166:167]
v_pk_add_f32 v[38:39], v[216:217], v[14:15] op_sel_hi:[1,0]
v_cvt_pk_bf16_f32 v60, v38, v39
v_pk_add_f32 v[218:219], v[148:149], v[168:169]
v_pk_add_f32 v[40:41], v[218:219], v[14:15] op_sel_hi:[1,0]
v_cvt_pk_bf16_f32 v61, v40, v41
s_mov_b32 exec_lo, 0xffff
s_mov_b32 exec_hi, 0xffff
global_store_dwordx4 v18, v[66:69], s[4:5] offset:2048 sc1
s_mov_b64 exec, -1
v_pk_add_f32 v[220:221], v[150:151], v[170:171]
v_pk_add_f32 v[42:43], v[220:221], v[14:15] op_sel_hi:[1,0]
v_cvt_pk_bf16_f32 v62, v42, v43
v_permlane16_swap_b32 v58, v60
v_permlane16_swap_b32 v59, v61
v_pk_add_f32 v[222:223], v[152:153], v[172:173]
v_pk_add_f32 v[44:45], v[222:223], v[14:15] op_sel_hi:[1,0]
v_cvt_pk_bf16_f32 v63, v44, v45
global_store_dwordx4 v17, v[58:61], s[4:5] sc1
v_pk_add_f32 v[224:225], v[154:155], v[174:175]
v_pk_add_f32 v[46:47], v[224:225], v[14:15] op_sel_hi:[1,0]
v_cvt_pk_bf16_f32 v64, v46, v47
v_pk_add_f32 v[226:227], v[156:157], v[176:177]
v_pk_add_f32 v[48:49], v[226:227], v[14:15] op_sel_hi:[1,0]
v_cvt_pk_bf16_f32 v65, v48, v49
s_nop 0
v_permlane16_swap_b32 v62, v64
v_permlane16_swap_b32 v63, v65
global_store_dwordx4 v17, v[62:65], s[4:5] offset:1024 sc1
v_pk_mul_f32 v[70:71], v[34:35], v[34:35]
v_pk_fma_f32 v[70:71], v[36:37], v[36:37], v[70:71]
v_pk_fma_f32 v[70:71], v[38:39], v[38:39], v[70:71]
v_pk_fma_f32 v[70:71], v[40:41], v[40:41], v[70:71]
v_pk_fma_f32 v[70:71], v[42:43], v[42:43], v[70:71]
v_pk_fma_f32 v[70:71], v[44:45], v[44:45], v[70:71]
v_pk_fma_f32 v[70:71], v[46:47], v[46:47], v[70:71]
v_pk_fma_f32 v[70:71], v[48:49], v[48:49], v[70:71]
v_pk_fma_f32 v[70:71], v[50:51], v[50:51], v[70:71]
v_pk_fma_f32 v[70:71], v[52:53], v[52:53], v[70:71]
v_add_f32 v72, v70, v71
v_mov_b32 v73, v72
s_nop 1
v_permlane16_swap_b32 v72, v73
v_add_f32 v72, v72, v73
v_mov_b32 v73, v72
s_nop 1
v_permlane32_swap_b32 v72, v73
v_add_f32 v72, v72, v73
v_lshlrev_b32 v73, 2, v0
s_mov_b32 exec_lo, 0xffff
s_mov_b32 exec_hi, 0
global_store_dword v73, v72, s[8:9] sc1
s_mov_b64 exec, -1
v_add_u32 v11, 0x10000, v4
v_add_u32 v12, 0x20000, v4
s_lshl_b32 s35, s55, 3
s_lshl_b32 s36, s29, 1
s_add_u32 s37, s35, s36
s_mul_i32 s35, s34, 54
s_add_u32 s35, s35, s62
s_mul_i32 s35, s35, 0x1400
s_add_u32 s35, s35, 0x8e8000
s_add_u32 s60, s20, s35
s_addc_u32 s61, s21, 0
global_load_dwordx4 v[232:235], v4, s[60:61]
s_add_u32 s60, s60, 0x800
s_addc_u32 s61, s61, 0
s_add_u32 s46, s20, 0x4400
s_addc_u32 s47, s21, 0
s_cmp_ge_u32 s29, 2
s_cselect_b32 s60, s46, s60
s_cselect_b32 s61, s47, s61
s_mul_i32 s51, s37, 0xd800
s_lshl_b32 s58, s56, 9
s_add_u32 s51, s51, s58
s_add_u32 s51, s51, 0x1234000
s_add_u32 s4, s20, s51
s_addc_u32 s5, s21, 0
s_add_u32 s6, s4, 0xd800
s_addc_u32 s7, s5, 0
v_bfe_u32 v26, v0, 4, 1
v_mul_u32_u24 v26, 0xd800, v26
v_add_u32 v19, v26, v18
v_and_b32 v26, 7, v0
v_min_u32 v26, 4, v26
s_lshl_b32 s51, s29, 2
v_add_u32 v26, s51, v26
v_min_u32 v26, 15, v26
v_mul_u32_u24 v26, 0x1af, v26
v_lshrrev_b32 v26, 6, v26
global_load_dwordx4 v[236:239], v4, s[60:61] offset:-1024
v_bfe_u32 v27, v0, 3, 1
v_mul_u32_u24 v27, 0xd8, v27
v_add_u32 v26, v26, v27
v_lshlrev_b32 v26, 9, v26
s_mul_i32 s51, s57, 0xd800
s_add_u32 s51, s51, 0x1234a00
v_add_u32 v13, s51, v26
s_add_u32 s51, s54, 1
s_and_b32 s51, s51, 3
s_add_u32 s51, s51, s57
s_mul_i32 s51, s51, 54
s_add_u32 s51, s51, s62
s_lshl_b32 s51, s51, 10
)ASM"
R"ASM(s_add_u32 s51, s51, 0x1235000
s_add_u32 s10, s20, s51
s_addc_u32 s11, s21, 0
s_add_u32 s51, s54, 2
global_load_dwordx4 v[240:243], v4, s[60:61] offset:0
s_and_b32 s51, s51, 3
s_add_u32 s51, s51, s57
s_mul_i32 s51, s51, 54
s_add_u32 s51, s51, s62
s_lshl_b32 s51, s51, 10
s_add_u32 s51, s51, 0x1235000
s_add_u32 s12, s20, s51
s_addc_u32 s13, s21, 0
s_add_u32 s51, s54, 3
s_and_b32 s51, s51, 3
s_add_u32 s51, s51, s57
s_mul_i32 s51, s51, 54
s_add_u32 s51, s51, s62
s_lshl_b32 s51, s51, 10
s_add_u32 s51, s51, 0x1235000
s_add_u32 s14, s20, s51
s_addc_u32 s15, s21, 0
s_add_u32 s51, s54, 4
global_load_dwordx4 v[244:247], v4, s[60:61] offset:1024
s_and_b32 s51, s51, 3
s_add_u32 s51, s51, s57
s_mul_i32 s51, s51, 54
s_add_u32 s51, s51, s62
s_lshl_b32 s51, s51, 10
s_add_u32 s51, s51, 0x1235000
s_add_u32 s16, s20, s51
s_addc_u32 s17, s21, 0
s_add_u32 s51, s29, 1
s_and_b32 s51, s51, 3
s_mul_i32 s51, s51, 0x3c00
s_add_u32 s51, s51, 0
v_add_u32 v2, s51, v4
s_add_u32 s51, s29, 2
s_and_b32 s51, s51, 3
s_mul_i32 s51, s51, 0x3c00
s_add_u32 s51, s51, 0x1400
global_load_dwordx4 v[248:251], v4, s[60:61] offset:2048
v_add_u32 v3, s51, v4
s_add_u32 s51, s29, 3
s_and_b32 s51, s51, 3
s_mul_i32 s51, s51, 0x3c00
s_add_u32 s51, s51, 0x2800
v_add_u32 v5, s51, v4
s_mul_i32 s51, s29, 0x3c00
v_add_u32 v9, s51, v4
s_cmp_eq_u32 s29, 3
s_cselect_b32 s51, 0, 0x400
v_add_u32 v14, s51, v4
v_and_b32 v26, 15, v0
v_lshrrev_b32 v27, 4, v0
v_lshlrev_b32 v26, 2, v26
v_lshl_add_u32 v15, v27, 7, v26
s_add_u32 s99, s57, s54
s_lshl_b32 s51, s99, 9
global_load_dwordx4 v[252:255], v4, s[60:61] offset:3072
s_add_u32 s51, s51, 0x1224000
s_add_u32 s38, s20, s51
s_addc_u32 s39, s21, 0
v_and_b32 v26, 15, v0
v_mul_u32_u24 v26, 0x500, v26
v_lshrrev_b32 v27, 5, v0
v_lshl_add_u32 v8, v27, 4, v26
v_bfe_u32 v27, v0, 4, 1
v_lshl_add_u32 v1, v27, 5, v8
; ==== FFN1: H = relu((h - mean) W1); the row scale 1/sqrt(var + eps) commutes with relu and is applied after FFN2
s_mov_b32 s50, 0
s_nop 3
Lf1_25_%=:
s_mul_i32 s35, s37, 0x5000
s_add_u32 s35, s35, 0xfa5000
s_add_u32 s35, s35, s63
s_add_u32 s40, s20, s35
s_addc_u32 s41, s21, 0
s_add_u32 s42, s40, 0x5000
s_addc_u32 s43, s41, 0
s_lshl_b32 s36, s63, 1
s_sub_u32 s44, 0x800, s36
s_subb_u32 s45, 0, 0
s_mul_i32 s35, s34, 54
s_add_u32 s35, s35, s62
s_mul_i32 s35, s35, 0x1400
s_add_u32 s35, s35, 0x8e3000
s_add_u32 s60, s20, s35
s_addc_u32 s61, s21, 0
s_add_u32 s60, s60, 0x1000
s_addc_u32 s61, s61, 0
ds_read_b128 v[170:173], v4 offset:6144
ds_read_b128 v[174:177], v4 offset:0
ds_read_b128 v[178:181], v4 offset:1024
ds_read_b128 v[182:185], v4 offset:2048
ds_read_b128 v[186:189], v4 offset:3072
ds_read_b128 v[190:193], v4 offset:13312
ds_read_b128 v[194:197], v4 offset:7168
global_load_dword v7, v16, s[20:21] sc1
global_load_dwordx4 a[176:179], v4, s[40:41] offset:-4096 sc1
global_load_dwordx4 a[180:183], v4, s[42:43] offset:-4096 sc1
global_load_dwordx4 a[184:187], v4, s[40:41] offset:-3072 sc1
global_load_dwordx4 a[188:191], v4, s[42:43] offset:-3072 sc1
s_waitcnt vmcnt(15)
s_cmp_lg_u32 s50, 0
s_cbranch_scc1 Lnb_28_%=
s_barrier
Lnb_28_%=:
s_waitcnt vmcnt(0)
v_accvgpr_read_b32 v20, a176
v_accvgpr_read_b32 v21, a177
v_accvgpr_read_b32 v22, a178
v_accvgpr_read_b32 v23, a179
v_max3_u32 v7, v7, v20, v21
v_max3_u32 v7, v7, v22, v23
v_accvgpr_read_b32 v20, a180
v_accvgpr_read_b32 v21, a181
v_accvgpr_read_b32 v22, a182
v_accvgpr_read_b32 v23, a183
v_max3_u32 v7, v7, v20, v21
v_max3_u32 v7, v7, v22, v23
v_accvgpr_read_b32 v20, a184
v_accvgpr_read_b32 v21, a185
v_accvgpr_read_b32 v22, a186
v_accvgpr_read_b32 v23, a187
v_max3_u32 v7, v7, v20, v21
v_max3_u32 v7, v7, v22, v23
v_accvgpr_read_b32 v20, a188
v_accvgpr_read_b32 v21, a189
v_accvgpr_read_b32 v22, a190
v_accvgpr_read_b32 v23, a191
v_max3_u32 v7, v7, v20, v21
v_max3_u32 v7, v7, v22, v23
v_cmp_eq_u32 vcc, -1, v7
v_mov_b32 v6, 0
s_cbranch_vccnz Lg_26_%=
Lgd_27_%=:
global_load_dwordx4 a[192:195], v4, s[40:41] offset:-2048 sc1
global_load_dwordx4 a[196:199], v4, s[42:43] offset:-2048 sc1
global_load_dwordx4 a[200:203], v4, s[40:41] offset:-1024 sc1
global_load_dwordx4 a[204:207], v4, s[42:43] offset:-1024 sc1
global_load_dwordx4 a[208:211], v4, s[40:41] offset:0 sc1
global_load_dwordx4 a[212:215], v4, s[42:43] offset:0 sc1
s_waitcnt lgkmcnt(5)
v_mfma_f32_16x16x32_bf16 v[162:165], v[170:173], a[176:179], 0
ds_read_b128 v[198:201], v4 offset:8192
v_accvgpr_read_b32 v20, a176
v_accvgpr_read_b32 v21, a177
v_mfma_f32_16x16x32_bf16 v[166:169], v[170:173], a[180:183], 0
v_accvgpr_read_b32 v22, a178
v_accvgpr_read_b32 v23, a179
v_mfma_f32_16x16x32_bf16 v[114:117], v[174:177], a[176:179], 0
ds_read_b128 v[202:205], v4 offset:9216
v_max3_u32 v6, v6, v20, v21
v_max3_u32 v6, v6, v22, v23
v_mfma_f32_16x16x32_bf16 v[118:121], v[174:177], a[180:183], 0
global_load_dwordx4 a[216:219], v4, s[40:41] offset:1024 sc1
s_waitcnt lgkmcnt(5)
v_mfma_f32_16x16x32_bf16 v[122:125], v[178:181], a[176:179], 0
ds_read_b128 v[206:209], v4 offset:10240
v_accvgpr_read_b32 v20, a180
v_accvgpr_read_b32 v21, a181
v_mfma_f32_16x16x32_bf16 v[126:129], v[178:181], a[180:183], 0
v_accvgpr_read_b32 v22, a182
v_accvgpr_read_b32 v23, a183
v_mfma_f32_16x16x32_bf16 v[130:133], v[182:185], a[176:179], 0
ds_read_b128 v[170:173], v4 offset:20480
v_mfma_f32_16x16x32_bf16 v[134:137], v[182:185], a[180:183], 0
global_load_dwordx4 a[220:223], v4, s[42:43] offset:1024 sc1
s_waitcnt lgkmcnt(5)
v_mfma_f32_16x16x32_bf16 v[138:141], v[186:189], a[176:179], 0
ds_read_b128 v[174:177], v4 offset:14336
v_max3_u32 v6, v6, v20, v21
v_max3_u32 v6, v6, v22, v23
v_mfma_f32_16x16x32_bf16 v[142:145], v[186:189], a[180:183], 0
v_mfma_f32_16x16x32_bf16 v[162:165], v[190:193], a[184:187], v[162:165]
ds_read_b128 v[178:181], v4 offset:15360
v_accvgpr_read_b32 v20, a184
v_accvgpr_read_b32 v21, a185
v_mfma_f32_16x16x32_bf16 v[166:169], v[190:193], a[188:191], v[166:169]
v_accvgpr_read_b32 v22, a186
v_accvgpr_read_b32 v23, a187
s_waitcnt lgkmcnt(5)
v_mfma_f32_16x16x32_bf16 v[114:117], v[194:197], a[184:187], v[114:117]
ds_read_b128 v[182:185], v4 offset:16384
v_max3_u32 v6, v6, v20, v21
v_max3_u32 v6, v6, v22, v23
v_mfma_f32_16x16x32_bf16 v[118:121], v[194:197], a[188:191], v[118:121]
global_load_dwordx4 a[224:227], v4, s[40:41] offset:2048 sc1
v_mfma_f32_16x16x32_bf16 v[122:125], v[198:201], a[184:187], v[122:125]
ds_read_b128 v[186:189], v4 offset:17408
v_accvgpr_read_b32 v20, a188
v_accvgpr_read_b32 v21, a189
v_mfma_f32_16x16x32_bf16 v[126:129], v[198:201], a[188:191], v[126:129]
v_accvgpr_read_b32 v22, a190
v_accvgpr_read_b32 v23, a191
s_waitcnt lgkmcnt(5)
v_mfma_f32_16x16x32_bf16 v[130:133], v[202:205], a[184:187], v[130:133]
ds_read_b128 v[190:193], v4 offset:27648
v_mfma_f32_16x16x32_bf16 v[134:137], v[202:205], a[188:191], v[134:137]
)ASM"
R"ASM(global_load_dwordx4 a[228:231], v4, s[42:43] offset:2048 sc1
v_mfma_f32_16x16x32_bf16 v[138:141], v[206:209], a[184:187], v[138:141]
ds_read_b128 v[194:197], v4 offset:21504
v_max3_u32 v6, v6, v20, v21
v_max3_u32 v6, v6, v22, v23
v_mfma_f32_16x16x32_bf16 v[142:145], v[206:209], a[188:191], v[142:145]
s_waitcnt vmcnt(8) lgkmcnt(5)
v_mfma_f32_16x16x32_bf16 v[162:165], v[170:173], a[192:195], v[162:165]
ds_read_b128 v[198:201], v4 offset:22528
v_accvgpr_read_b32 v20, a192
v_accvgpr_read_b32 v21, a193
v_mfma_f32_16x16x32_bf16 v[166:169], v[170:173], a[196:199], v[166:169]
v_accvgpr_read_b32 v22, a194
v_accvgpr_read_b32 v23, a195
v_mfma_f32_16x16x32_bf16 v[114:117], v[174:177], a[192:195], v[114:117]
ds_read_b128 v[202:205], v4 offset:23552
v_max3_u32 v6, v6, v20, v21
v_max3_u32 v6, v6, v22, v23
v_mfma_f32_16x16x32_bf16 v[118:121], v[174:177], a[196:199], v[118:121]
global_load_dwordx4 a[232:235], v4, s[40:41] offset:3072 sc1
s_waitcnt lgkmcnt(5)
v_mfma_f32_16x16x32_bf16 v[122:125], v[178:181], a[192:195], v[122:125]
ds_read_b128 v[206:209], v4 offset:24576
v_accvgpr_read_b32 v20, a196
v_accvgpr_read_b32 v21, a197
v_mfma_f32_16x16x32_bf16 v[126:129], v[178:181], a[196:199], v[126:129]
v_accvgpr_read_b32 v22, a198
v_accvgpr_read_b32 v23, a199
v_mfma_f32_16x16x32_bf16 v[130:133], v[182:185], a[192:195], v[130:133]
ds_read_b128 v[170:173], v4 offset:34816
v_mfma_f32_16x16x32_bf16 v[134:137], v[182:185], a[196:199], v[134:137]
global_load_dwordx4 a[236:239], v4, s[42:43] offset:3072 sc1
s_waitcnt lgkmcnt(5)
v_mfma_f32_16x16x32_bf16 v[138:141], v[186:189], a[192:195], v[138:141]
ds_read_b128 v[174:177], v4 offset:28672
v_max3_u32 v6, v6, v20, v21
v_max3_u32 v6, v6, v22, v23
v_mfma_f32_16x16x32_bf16 v[142:145], v[186:189], a[196:199], v[142:145]
s_waitcnt vmcnt(8)
v_mfma_f32_16x16x32_bf16 v[162:165], v[190:193], a[200:203], v[162:165]
ds_read_b128 v[178:181], v4 offset:29696
v_accvgpr_read_b32 v20, a200
v_accvgpr_read_b32 v21, a201
v_mfma_f32_16x16x32_bf16 v[166:169], v[190:193], a[204:207], v[166:169]
v_accvgpr_read_b32 v22, a202
v_accvgpr_read_b32 v23, a203
s_waitcnt lgkmcnt(5)
v_mfma_f32_16x16x32_bf16 v[114:117], v[194:197], a[200:203], v[114:117]
ds_read_b128 v[182:185], v4 offset:30720
v_max3_u32 v6, v6, v20, v21
v_max3_u32 v6, v6, v22, v23
v_mfma_f32_16x16x32_bf16 v[118:121], v[194:197], a[204:207], v[118:121]
s_add_u32 s40, s40, 0x2000
s_addc_u32 s41, s41, 0
global_load_dwordx4 a[240:243], v4, s[40:41] offset:-4096 sc1
v_mfma_f32_16x16x32_bf16 v[122:125], v[198:201], a[200:203], v[122:125]
ds_read_b128 v[186:189], v4 offset:31744
v_accvgpr_read_b32 v20, a204
v_accvgpr_read_b32 v21, a205
v_mfma_f32_16x16x32_bf16 v[126:129], v[198:201], a[204:207], v[126:129]
v_accvgpr_read_b32 v22, a206
v_accvgpr_read_b32 v23, a207
s_waitcnt lgkmcnt(5)
v_mfma_f32_16x16x32_bf16 v[130:133], v[202:205], a[200:203], v[130:133]
ds_read_b128 v[190:193], v4 offset:41984
v_mfma_f32_16x16x32_bf16 v[134:137], v[202:205], a[204:207], v[134:137]
s_add_u32 s42, s42, 0x2000
s_addc_u32 s43, s43, 0
global_load_dwordx4 a[244:247], v4, s[42:43] offset:-4096 sc1
v_mfma_f32_16x16x32_bf16 v[138:141], v[206:209], a[200:203], v[138:141]
ds_read_b128 v[194:197], v4 offset:35840
v_max3_u32 v6, v6, v20, v21
v_max3_u32 v6, v6, v22, v23
v_mfma_f32_16x16x32_bf16 v[142:145], v[206:209], a[204:207], v[142:145]
s_waitcnt vmcnt(8) lgkmcnt(5)
v_mfma_f32_16x16x32_bf16 v[162:165], v[170:173], a[208:211], v[162:165]
ds_read_b128 v[198:201], v4 offset:36864
v_accvgpr_read_b32 v20, a208
v_accvgpr_read_b32 v21, a209
v_mfma_f32_16x16x32_bf16 v[166:169], v[170:173], a[212:215], v[166:169]
v_accvgpr_read_b32 v22, a210
v_accvgpr_read_b32 v23, a211
v_mfma_f32_16x16x32_bf16 v[114:117], v[174:177], a[208:211], v[114:117]
ds_read_b128 v[202:205], v4 offset:37888
v_max3_u32 v6, v6, v20, v21
v_max3_u32 v6, v6, v22, v23
v_mfma_f32_16x16x32_bf16 v[118:121], v[174:177], a[212:215], v[118:121]
global_load_dwordx4 a[248:251], v4, s[40:41] offset:-3072 sc1
s_waitcnt lgkmcnt(5)
v_mfma_f32_16x16x32_bf16 v[122:125], v[178:181], a[208:211], v[122:125]
ds_read_b128 v[206:209], v4 offset:38912
v_accvgpr_read_b32 v20, a212
v_accvgpr_read_b32 v21, a213
v_mfma_f32_16x16x32_bf16 v[126:129], v[178:181], a[212:215], v[126:129]
v_accvgpr_read_b32 v22, a214
v_accvgpr_read_b32 v23, a215
v_mfma_f32_16x16x32_bf16 v[130:133], v[182:185], a[208:211], v[130:133]
ds_read_b128 v[170:173], v4 offset:49152
v_mfma_f32_16x16x32_bf16 v[134:137], v[182:185], a[212:215], v[134:137]
global_load_dwordx4 a[252:255], v4, s[42:43] offset:-3072 sc1
s_waitcnt lgkmcnt(5)
v_mfma_f32_16x16x32_bf16 v[138:141], v[186:189], a[208:211], v[138:141]
ds_read_b128 v[174:177], v4 offset:43008
v_max3_u32 v6, v6, v20, v21
v_max3_u32 v6, v6, v22, v23
v_mfma_f32_16x16x32_bf16 v[142:145], v[186:189], a[212:215], v[142:145]
s_waitcnt vmcnt(8)
v_mfma_f32_16x16x32_bf16 v[162:165], v[190:193], a[216:219], v[162:165]
ds_read_b128 v[178:181], v4 offset:44032
v_accvgpr_read_b32 v20, a216
v_accvgpr_read_b32 v21, a217
v_mfma_f32_16x16x32_bf16 v[166:169], v[190:193], a[220:223], v[166:169]
v_accvgpr_read_b32 v22, a218
v_accvgpr_read_b32 v23, a219
s_waitcnt lgkmcnt(5)
v_mfma_f32_16x16x32_bf16 v[114:117], v[194:197], a[216:219], v[114:117]
ds_read_b128 v[182:185], v4 offset:45056
v_max3_u32 v6, v6, v20, v21
v_max3_u32 v6, v6, v22, v23
v_mfma_f32_16x16x32_bf16 v[118:121], v[194:197], a[220:223], v[118:121]
s_add_u32 s40, s40, s44
s_addc_u32 s41, s41, s45
global_load_dwordx4 v[34:37], v4, s[40:41] offset:-4096 sc1
v_mfma_f32_16x16x32_bf16 v[122:125], v[198:201], a[216:219], v[122:125]
ds_read_b128 v[186:189], v4 offset:46080
v_accvgpr_read_b32 v20, a220
v_accvgpr_read_b32 v21, a221
v_mfma_f32_16x16x32_bf16 v[126:129], v[198:201], a[220:223], v[126:129]
v_accvgpr_read_b32 v22, a222
v_accvgpr_read_b32 v23, a223
s_waitcnt lgkmcnt(5)
v_mfma_f32_16x16x32_bf16 v[130:133], v[202:205], a[216:219], v[130:133]
ds_read_b128 v[190:193], v4 offset:56320
v_mfma_f32_16x16x32_bf16 v[134:137], v[202:205], a[220:223], v[134:137]
s_add_u32 s42, s42, s44
s_addc_u32 s43, s43, s45
global_load_dwordx4 v[38:41], v4, s[42:43] offset:-4096 sc1
v_mfma_f32_16x16x32_bf16 v[138:141], v[206:209], a[216:219], v[138:141]
ds_read_b128 v[194:197], v4 offset:50176
v_max3_u32 v6, v6, v20, v21
v_max3_u32 v6, v6, v22, v23
v_mfma_f32_16x16x32_bf16 v[142:145], v[206:209], a[220:223], v[142:145]
s_waitcnt vmcnt(8) lgkmcnt(5)
v_mfma_f32_16x16x32_bf16 v[162:165], v[170:173], a[224:227], v[162:165]
ds_read_b128 v[198:201], v4 offset:51200
v_accvgpr_read_b32 v20, a224
v_accvgpr_read_b32 v21, a225
v_mfma_f32_16x16x32_bf16 v[166:169], v[170:173], a[228:231], v[166:169]
v_accvgpr_read_b32 v22, a226
v_accvgpr_read_b32 v23, a227
v_mfma_f32_16x16x32_bf16 v[114:117], v[174:177], a[224:227], v[114:117]
ds_read_b128 v[202:205], v4 offset:52224
v_max3_u32 v6, v6, v20, v21
v_max3_u32 v6, v6, v22, v23
v_mfma_f32_16x16x32_bf16 v[118:121], v[174:177], a[228:231], v[118:121]
global_load_dwordx4 v[42:45], v4, s[40:41] offset:-3072 sc1
s_waitcnt lgkmcnt(5)
v_mfma_f32_16x16x32_bf16 v[122:125], v[178:181], a[224:227], v[122:125]
ds_read_b128 v[206:209], v4 offset:53248
v_accvgpr_read_b32 v20, a228
v_accvgpr_read_b32 v21, a229
v_mfma_f32_16x16x32_bf16 v[126:129], v[178:181], a[228:231], v[126:129]
v_accvgpr_read_b32 v22, a230
v_accvgpr_read_b32 v23, a231
v_mfma_f32_16x16x32_bf16 v[130:133], v[182:185], a[224:227], v[130:133]
ds_read_b128 v[170:173], v4 offset:63488
v_mfma_f32_16x16x32_bf16 v[134:137], v[182:185], a[228:231], v[134:137]
global_load_dwordx4 v[46:49], v4, s[42:43] offset:-3072 sc1
s_waitcnt lgkmcnt(5)
v_mfma_f32_16x16x32_bf16 v[138:141], v[186:189], a[224:227], v[138:141]
ds_read_b128 v[174:177], v4 offset:57344
v_max3_u32 v6, v6, v20, v21
v_max3_u32 v6, v6, v22, v23
v_mfma_f32_16x16x32_bf16 v[142:145], v[186:189], a[228:231], v[142:145]
s_waitcnt vmcnt(8)
v_mfma_f32_16x16x32_bf16 v[162:165], v[190:193], a[232:235], v[162:165]
ds_read_b128 v[178:181], v4 offset:58368
v_accvgpr_read_b32 v20, a232
v_accvgpr_read_b32 v21, a233
v_mfma_f32_16x16x32_bf16 v[166:169], v[190:193], a[236:239], v[166:169]
v_accvgpr_read_b32 v22, a234
v_accvgpr_read_b32 v23, a235
s_waitcnt lgkmcnt(5)
v_mfma_f32_16x16x32_bf16 v[114:117], v[194:197], a[232:235], v[114:117]
ds_read_b128 v[182:185], v4 offset:59392
v_max3_u32 v6, v6, v20, v21
v_max3_u32 v6, v6, v22, v23
v_mfma_f32_16x16x32_bf16 v[118:121], v[194:197], a[236:239], v[118:121]
global_load_dwordx4 v[50:53], v4, s[40:41] offset:-2048 sc1
v_mfma_f32_16x16x32_bf16 v[122:125], v[198:201], a[232:235], v[122:125]
ds_read_b128 v[186:189], v4 offset:60416
v_accvgpr_read_b32 v20, a236
v_accvgpr_read_b32 v21, a237
v_mfma_f32_16x16x32_bf16 v[126:129], v[198:201], a[236:239], v[126:129]
v_accvgpr_read_b32 v22, a238
v_accvgpr_read_b32 v23, a239
s_waitcnt lgkmcnt(5)
v_mfma_f32_16x16x32_bf16 v[130:133], v[202:205], a[232:235], v[130:133]
ds_read_b128 v[190:193], v11 offset:5120
v_mfma_f32_16x16x32_bf16 v[134:137], v[202:205], a[236:239], v[134:137]
)ASM"
R"ASM(global_load_dwordx4 v[54:57], v4, s[42:43] offset:-2048 sc1
v_mfma_f32_16x16x32_bf16 v[138:141], v[206:209], a[232:235], v[138:141]
ds_read_b128 v[194:197], v4 offset:64512
v_max3_u32 v6, v6, v20, v21
v_max3_u32 v6, v6, v22, v23
v_mfma_f32_16x16x32_bf16 v[142:145], v[206:209], a[236:239], v[142:145]
s_waitcnt vmcnt(8) lgkmcnt(5)
v_mfma_f32_16x16x32_bf16 v[162:165], v[170:173], a[240:243], v[162:165]
ds_read_b128 v[198:201], v11 offset:0
v_accvgpr_read_b32 v20, a240
v_accvgpr_read_b32 v21, a241
v_mfma_f32_16x16x32_bf16 v[166:169], v[170:173], a[244:247], v[166:169]
v_accvgpr_read_b32 v22, a242
v_accvgpr_read_b32 v23, a243
v_mfma_f32_16x16x32_bf16 v[114:117], v[174:177], a[240:243], v[114:117]
ds_read_b128 v[202:205], v11 offset:1024
v_max3_u32 v6, v6, v20, v21
v_max3_u32 v6, v6, v22, v23
v_mfma_f32_16x16x32_bf16 v[118:121], v[174:177], a[244:247], v[118:121]
global_load_dwordx4 v[58:61], v4, s[40:41] offset:-1024 sc1
s_waitcnt lgkmcnt(5)
v_mfma_f32_16x16x32_bf16 v[122:125], v[178:181], a[240:243], v[122:125]
ds_read_b128 v[206:209], v11 offset:2048
v_accvgpr_read_b32 v20, a244
v_accvgpr_read_b32 v21, a245
v_mfma_f32_16x16x32_bf16 v[126:129], v[178:181], a[244:247], v[126:129]
v_accvgpr_read_b32 v22, a246
v_accvgpr_read_b32 v23, a247
v_mfma_f32_16x16x32_bf16 v[130:133], v[182:185], a[240:243], v[130:133]
ds_read_b128 v[170:173], v11 offset:12288
v_mfma_f32_16x16x32_bf16 v[134:137], v[182:185], a[244:247], v[134:137]
global_load_dwordx4 v[62:65], v4, s[42:43] offset:-1024 sc1
s_waitcnt lgkmcnt(5)
v_mfma_f32_16x16x32_bf16 v[138:141], v[186:189], a[240:243], v[138:141]
ds_read_b128 v[174:177], v11 offset:6144
v_max3_u32 v6, v6, v20, v21
v_max3_u32 v6, v6, v22, v23
v_mfma_f32_16x16x32_bf16 v[142:145], v[186:189], a[244:247], v[142:145]
s_waitcnt vmcnt(8)
v_mfma_f32_16x16x32_bf16 v[162:165], v[190:193], a[248:251], v[162:165]
ds_read_b128 v[178:181], v11 offset:7168
v_accvgpr_read_b32 v20, a248
v_accvgpr_read_b32 v21, a249
v_mfma_f32_16x16x32_bf16 v[166:169], v[190:193], a[252:255], v[166:169]
v_accvgpr_read_b32 v22, a250
v_accvgpr_read_b32 v23, a251
s_waitcnt lgkmcnt(5)
v_mfma_f32_16x16x32_bf16 v[114:117], v[194:197], a[248:251], v[114:117]
ds_read_b128 v[182:185], v11 offset:8192
v_max3_u32 v6, v6, v20, v21
v_max3_u32 v6, v6, v22, v23
v_mfma_f32_16x16x32_bf16 v[118:121], v[194:197], a[252:255], v[118:121]
global_load_dwordx4 v[66:69], v4, s[40:41] offset:0 sc1
v_mfma_f32_16x16x32_bf16 v[122:125], v[198:201], a[248:251], v[122:125]
ds_read_b128 v[186:189], v11 offset:9216
v_accvgpr_read_b32 v20, a252
v_accvgpr_read_b32 v21, a253
v_mfma_f32_16x16x32_bf16 v[126:129], v[198:201], a[252:255], v[126:129]
v_accvgpr_read_b32 v22, a254
v_accvgpr_read_b32 v23, a255
s_waitcnt lgkmcnt(5)
v_mfma_f32_16x16x32_bf16 v[130:133], v[202:205], a[248:251], v[130:133]
ds_read_b128 v[190:193], v11 offset:19456
v_mfma_f32_16x16x32_bf16 v[134:137], v[202:205], a[252:255], v[134:137]
global_load_dwordx4 v[70:73], v4, s[42:43] offset:0 sc1
v_mfma_f32_16x16x32_bf16 v[138:141], v[206:209], a[248:251], v[138:141]
ds_read_b128 v[194:197], v11 offset:13312
v_max3_u32 v6, v6, v20, v21
v_max3_u32 v6, v6, v22, v23
v_mfma_f32_16x16x32_bf16 v[142:145], v[206:209], a[252:255], v[142:145]
s_waitcnt vmcnt(8) lgkmcnt(5)
v_mfma_f32_16x16x32_bf16 v[162:165], v[170:173], v[34:37], v[162:165]
ds_read_b128 v[198:201], v11 offset:14336
v_mfma_f32_16x16x32_bf16 v[166:169], v[170:173], v[38:41], v[166:169]
v_max3_u32 v6, v6, v34, v35
v_max3_u32 v6, v6, v36, v37
v_mfma_f32_16x16x32_bf16 v[114:117], v[174:177], v[34:37], v[114:117]
ds_read_b128 v[202:205], v11 offset:15360
v_mfma_f32_16x16x32_bf16 v[118:121], v[174:177], v[38:41], v[118:121]
global_load_dwordx4 v[74:77], v4, s[40:41] offset:1024 sc1
s_waitcnt lgkmcnt(5)
v_mfma_f32_16x16x32_bf16 v[122:125], v[178:181], v[34:37], v[122:125]
ds_read_b128 v[206:209], v11 offset:16384
v_mfma_f32_16x16x32_bf16 v[126:129], v[178:181], v[38:41], v[126:129]
v_max3_u32 v6, v6, v38, v39
v_max3_u32 v6, v6, v40, v41
v_mfma_f32_16x16x32_bf16 v[130:133], v[182:185], v[34:37], v[130:133]
ds_read_b128 v[170:173], v11 offset:26624
v_mfma_f32_16x16x32_bf16 v[134:137], v[182:185], v[38:41], v[134:137]
global_load_dwordx4 v[78:81], v4, s[42:43] offset:1024 sc1
s_waitcnt lgkmcnt(5)
v_mfma_f32_16x16x32_bf16 v[138:141], v[186:189], v[34:37], v[138:141]
ds_read_b128 v[174:177], v11 offset:20480
v_mfma_f32_16x16x32_bf16 v[142:145], v[186:189], v[38:41], v[142:145]
s_waitcnt vmcnt(8)
v_mfma_f32_16x16x32_bf16 v[162:165], v[190:193], v[42:45], v[162:165]
ds_read_b128 v[178:181], v11 offset:21504
v_mfma_f32_16x16x32_bf16 v[166:169], v[190:193], v[46:49], v[166:169]
v_max3_u32 v6, v6, v42, v43
v_max3_u32 v6, v6, v44, v45
s_waitcnt lgkmcnt(5)
v_mfma_f32_16x16x32_bf16 v[114:117], v[194:197], v[42:45], v[114:117]
ds_read_b128 v[182:185], v11 offset:22528
v_mfma_f32_16x16x32_bf16 v[118:121], v[194:197], v[46:49], v[118:121]
global_load_dwordx4 v[82:85], v4, s[40:41] offset:2048 sc1
v_mfma_f32_16x16x32_bf16 v[122:125], v[198:201], v[42:45], v[122:125]
ds_read_b128 v[186:189], v11 offset:23552
v_mfma_f32_16x16x32_bf16 v[126:129], v[198:201], v[46:49], v[126:129]
v_max3_u32 v6, v6, v46, v47
v_max3_u32 v6, v6, v48, v49
s_waitcnt lgkmcnt(5)
v_mfma_f32_16x16x32_bf16 v[130:133], v[202:205], v[42:45], v[130:133]
ds_read_b128 v[190:193], v11 offset:33792
v_mfma_f32_16x16x32_bf16 v[134:137], v[202:205], v[46:49], v[134:137]
global_load_dwordx4 v[86:89], v4, s[42:43] offset:2048 sc1
v_mfma_f32_16x16x32_bf16 v[138:141], v[206:209], v[42:45], v[138:141]
ds_read_b128 v[194:197], v11 offset:27648
v_mfma_f32_16x16x32_bf16 v[142:145], v[206:209], v[46:49], v[142:145]
s_waitcnt vmcnt(8) lgkmcnt(5)
v_mfma_f32_16x16x32_bf16 v[162:165], v[170:173], v[50:53], v[162:165]
ds_read_b128 v[198:201], v11 offset:28672
v_mfma_f32_16x16x32_bf16 v[166:169], v[170:173], v[54:57], v[166:169]
v_max3_u32 v6, v6, v50, v51
v_max3_u32 v6, v6, v52, v53
v_mfma_f32_16x16x32_bf16 v[114:117], v[174:177], v[50:53], v[114:117]
ds_read_b128 v[202:205], v11 offset:29696
v_mfma_f32_16x16x32_bf16 v[118:121], v[174:177], v[54:57], v[118:121]
global_load_dwordx4 v[90:93], v4, s[40:41] offset:3072 sc1
s_waitcnt lgkmcnt(5)
v_mfma_f32_16x16x32_bf16 v[122:125], v[178:181], v[50:53], v[122:125]
ds_read_b128 v[206:209], v11 offset:30720
v_mfma_f32_16x16x32_bf16 v[126:129], v[178:181], v[54:57], v[126:129]
v_max3_u32 v6, v6, v54, v55
v_max3_u32 v6, v6, v56, v57
v_mfma_f32_16x16x32_bf16 v[130:133], v[182:185], v[50:53], v[130:133]
ds_read_b128 v[170:173], v11 offset:40960
v_mfma_f32_16x16x32_bf16 v[134:137], v[182:185], v[54:57], v[134:137]
global_load_dwordx4 v[94:97], v4, s[42:43] offset:3072 sc1
s_waitcnt lgkmcnt(5)
v_mfma_f32_16x16x32_bf16 v[138:141], v[186:189], v[50:53], v[138:141]
ds_read_b128 v[174:177], v11 offset:34816
v_mfma_f32_16x16x32_bf16 v[142:145], v[186:189], v[54:57], v[142:145]
s_waitcnt vmcnt(8)
v_mfma_f32_16x16x32_bf16 v[162:165], v[190:193], v[58:61], v[162:165]
ds_read_b128 v[178:181], v11 offset:35840
v_mfma_f32_16x16x32_bf16 v[166:169], v[190:193], v[62:65], v[166:169]
v_max3_u32 v6, v6, v58, v59
v_max3_u32 v6, v6, v60, v61
s_waitcnt lgkmcnt(5)
v_mfma_f32_16x16x32_bf16 v[114:117], v[194:197], v[58:61], v[114:117]
ds_read_b128 v[182:185], v11 offset:36864
v_mfma_f32_16x16x32_bf16 v[118:121], v[194:197], v[62:65], v[118:121]
s_add_u32 s40, s40, 0x2000
s_addc_u32 s41, s41, 0
global_load_dwordx4 v[98:101], v4, s[40:41] offset:-4096 sc1
v_mfma_f32_16x16x32_bf16 v[122:125], v[198:201], v[58:61], v[122:125]
ds_read_b128 v[186:189], v11 offset:37888
v_mfma_f32_16x16x32_bf16 v[126:129], v[198:201], v[62:65], v[126:129]
v_max3_u32 v6, v6, v62, v63
v_max3_u32 v6, v6, v64, v65
s_waitcnt lgkmcnt(5)
v_mfma_f32_16x16x32_bf16 v[130:133], v[202:205], v[58:61], v[130:133]
ds_read_b128 v[190:193], v11 offset:48128
v_mfma_f32_16x16x32_bf16 v[134:137], v[202:205], v[62:65], v[134:137]
s_add_u32 s42, s42, 0x2000
s_addc_u32 s43, s43, 0
global_load_dwordx4 v[102:105], v4, s[42:43] offset:-4096 sc1
v_mfma_f32_16x16x32_bf16 v[138:141], v[206:209], v[58:61], v[138:141]
ds_read_b128 v[194:197], v11 offset:41984
v_mfma_f32_16x16x32_bf16 v[142:145], v[206:209], v[62:65], v[142:145]
s_waitcnt vmcnt(8) lgkmcnt(5)
v_mfma_f32_16x16x32_bf16 v[162:165], v[170:173], v[66:69], v[162:165]
ds_read_b128 v[198:201], v11 offset:43008
v_mfma_f32_16x16x32_bf16 v[166:169], v[170:173], v[70:73], v[166:169]
v_max3_u32 v6, v6, v66, v67
v_max3_u32 v6, v6, v68, v69
v_mfma_f32_16x16x32_bf16 v[114:117], v[174:177], v[66:69], v[114:117]
ds_read_b128 v[202:205], v11 offset:44032
v_mfma_f32_16x16x32_bf16 v[118:121], v[174:177], v[70:73], v[118:121]
global_load_dwordx4 v[106:109], v4, s[40:41] offset:-3072 sc1
s_waitcnt lgkmcnt(5)
v_mfma_f32_16x16x32_bf16 v[122:125], v[178:181], v[66:69], v[122:125]
ds_read_b128 v[206:209], v11 offset:45056
v_mfma_f32_16x16x32_bf16 v[126:129], v[178:181], v[70:73], v[126:129]
v_max3_u32 v6, v6, v70, v71
v_max3_u32 v6, v6, v72, v73
v_mfma_f32_16x16x32_bf16 v[130:133], v[182:185], v[66:69], v[130:133]
ds_read_b128 v[170:173], v11 offset:55296
v_mfma_f32_16x16x32_bf16 v[134:137], v[182:185], v[70:73], v[134:137]
global_load_dwordx4 v[110:113], v4, s[42:43] offset:-3072 sc1
s_waitcnt lgkmcnt(5)
v_mfma_f32_16x16x32_bf16 v[138:141], v[186:189], v[66:69], v[138:141]
ds_read_b128 v[174:177], v11 offset:49152
v_mfma_f32_16x16x32_bf16 v[142:145], v[186:189], v[70:73], v[142:145]
s_waitcnt vmcnt(8)
v_mfma_f32_16x16x32_bf16 v[162:165], v[190:193], v[74:77], v[162:165]
ds_read_b128 v[178:181], v11 offset:50176
v_mfma_f32_16x16x32_bf16 v[166:169], v[190:193], v[78:81], v[166:169]
v_max3_u32 v6, v6, v74, v75
v_max3_u32 v6, v6, v76, v77
)ASM"
R"ASM(s_waitcnt lgkmcnt(5)
v_mfma_f32_16x16x32_bf16 v[114:117], v[194:197], v[74:77], v[114:117]
ds_read_b128 v[182:185], v11 offset:51200
v_mfma_f32_16x16x32_bf16 v[118:121], v[194:197], v[78:81], v[118:121]
v_max3_u32 v6, v6, v78, v79
v_max3_u32 v6, v6, v80, v81
v_mfma_f32_16x16x32_bf16 v[122:125], v[198:201], v[74:77], v[122:125]
ds_read_b128 v[186:189], v11 offset:52224
v_mfma_f32_16x16x32_bf16 v[126:129], v[198:201], v[78:81], v[126:129]
s_waitcnt lgkmcnt(5)
v_mfma_f32_16x16x32_bf16 v[130:133], v[202:205], v[74:77], v[130:133]
ds_read_b128 v[190:193], v11 offset:62464
v_mfma_f32_16x16x32_bf16 v[134:137], v[202:205], v[78:81], v[134:137]
v_mfma_f32_16x16x32_bf16 v[138:141], v[206:209], v[74:77], v[138:141]
ds_read_b128 v[194:197], v11 offset:56320
v_mfma_f32_16x16x32_bf16 v[142:145], v[206:209], v[78:81], v[142:145]
s_waitcnt vmcnt(6) lgkmcnt(5)
v_mfma_f32_16x16x32_bf16 v[162:165], v[170:173], v[82:85], v[162:165]
ds_read_b128 v[198:201], v11 offset:57344
v_mfma_f32_16x16x32_bf16 v[166:169], v[170:173], v[86:89], v[166:169]
v_max3_u32 v6, v6, v82, v83
v_max3_u32 v6, v6, v84, v85
v_mfma_f32_16x16x32_bf16 v[114:117], v[174:177], v[82:85], v[114:117]
ds_read_b128 v[202:205], v11 offset:58368
v_mfma_f32_16x16x32_bf16 v[118:121], v[174:177], v[86:89], v[118:121]
v_max3_u32 v6, v6, v86, v87
v_max3_u32 v6, v6, v88, v89
s_waitcnt lgkmcnt(5)
v_mfma_f32_16x16x32_bf16 v[122:125], v[178:181], v[82:85], v[122:125]
ds_read_b128 v[206:209], v11 offset:59392
v_mfma_f32_16x16x32_bf16 v[126:129], v[178:181], v[86:89], v[126:129]
v_mfma_f32_16x16x32_bf16 v[130:133], v[182:185], v[82:85], v[130:133]
ds_read_b128 v[170:173], v12 offset:4096
v_mfma_f32_16x16x32_bf16 v[134:137], v[182:185], v[86:89], v[134:137]
s_waitcnt lgkmcnt(5)
v_mfma_f32_16x16x32_bf16 v[138:141], v[186:189], v[82:85], v[138:141]
ds_read_b128 v[174:177], v11 offset:63488
v_mfma_f32_16x16x32_bf16 v[142:145], v[186:189], v[86:89], v[142:145]
s_waitcnt vmcnt(4)
v_mfma_f32_16x16x32_bf16 v[162:165], v[190:193], v[90:93], v[162:165]
ds_read_b128 v[178:181], v11 offset:64512
v_mfma_f32_16x16x32_bf16 v[166:169], v[190:193], v[94:97], v[166:169]
v_max3_u32 v6, v6, v90, v91
v_max3_u32 v6, v6, v92, v93
s_waitcnt lgkmcnt(5)
v_mfma_f32_16x16x32_bf16 v[114:117], v[194:197], v[90:93], v[114:117]
ds_read_b128 v[182:185], v12 offset:0
v_mfma_f32_16x16x32_bf16 v[118:121], v[194:197], v[94:97], v[118:121]
v_max3_u32 v6, v6, v94, v95
v_max3_u32 v6, v6, v96, v97
v_mfma_f32_16x16x32_bf16 v[122:125], v[198:201], v[90:93], v[122:125]
ds_read_b128 v[186:189], v12 offset:1024
v_mfma_f32_16x16x32_bf16 v[126:129], v[198:201], v[94:97], v[126:129]
s_waitcnt lgkmcnt(5)
v_mfma_f32_16x16x32_bf16 v[130:133], v[202:205], v[90:93], v[130:133]
ds_read_b128 v[190:193], v12 offset:11264
v_mfma_f32_16x16x32_bf16 v[134:137], v[202:205], v[94:97], v[134:137]
v_mfma_f32_16x16x32_bf16 v[138:141], v[206:209], v[90:93], v[138:141]
ds_read_b128 v[194:197], v12 offset:5120
v_mfma_f32_16x16x32_bf16 v[142:145], v[206:209], v[94:97], v[142:145]
s_waitcnt vmcnt(2) lgkmcnt(5)
v_mfma_f32_16x16x32_bf16 v[162:165], v[170:173], v[98:101], v[162:165]
ds_read_b128 v[198:201], v12 offset:6144
v_mfma_f32_16x16x32_bf16 v[166:169], v[170:173], v[102:105], v[166:169]
v_max3_u32 v6, v6, v98, v99
v_max3_u32 v6, v6, v100, v101
v_mfma_f32_16x16x32_bf16 v[114:117], v[174:177], v[98:101], v[114:117]
ds_read_b128 v[202:205], v12 offset:7168
v_mfma_f32_16x16x32_bf16 v[118:121], v[174:177], v[102:105], v[118:121]
v_max3_u32 v6, v6, v102, v103
v_max3_u32 v6, v6, v104, v105
s_waitcnt lgkmcnt(5)
v_mfma_f32_16x16x32_bf16 v[122:125], v[178:181], v[98:101], v[122:125]
ds_read_b128 v[206:209], v12 offset:8192
v_mfma_f32_16x16x32_bf16 v[126:129], v[178:181], v[102:105], v[126:129]
v_mfma_f32_16x16x32_bf16 v[130:133], v[182:185], v[98:101], v[130:133]
ds_read_b128 v[170:173], v4 offset:4096
v_mfma_f32_16x16x32_bf16 v[134:137], v[182:185], v[102:105], v[134:137]
s_waitcnt lgkmcnt(5)
v_mfma_f32_16x16x32_bf16 v[138:141], v[186:189], v[98:101], v[138:141]
ds_read_b128 v[174:177], v4 offset:5120
v_mfma_f32_16x16x32_bf16 v[142:145], v[186:189], v[102:105], v[142:145]
s_waitcnt vmcnt(0)
v_mfma_f32_16x16x32_bf16 v[162:165], v[190:193], v[106:109], v[162:165]
ds_read_b128 v[178:181], v4 offset:11264
v_mfma_f32_16x16x32_bf16 v[166:169], v[190:193], v[110:113], v[166:169]
v_max3_u32 v6, v6, v106, v107
v_max3_u32 v6, v6, v108, v109
s_waitcnt lgkmcnt(5)
v_mfma_f32_16x16x32_bf16 v[114:117], v[194:197], v[106:109], v[114:117]
ds_read_b128 v[182:185], v4 offset:12288
v_mfma_f32_16x16x32_bf16 v[118:121], v[194:197], v[110:113], v[118:121]
v_max3_u32 v6, v6, v110, v111
v_max3_u32 v6, v6, v112, v113
v_mfma_f32_16x16x32_bf16 v[122:125], v[198:201], v[106:109], v[122:125]
ds_read_b128 v[186:189], v4 offset:18432
v_mfma_f32_16x16x32_bf16 v[126:129], v[198:201], v[110:113], v[126:129]
s_waitcnt lgkmcnt(5)
v_mfma_f32_16x16x32_bf16 v[130:133], v[202:205], v[106:109], v[130:133]
ds_read_b128 v[190:193], v4 offset:19456
v_mfma_f32_16x16x32_bf16 v[134:137], v[202:205], v[110:113], v[134:137]
v_cmp_eq_u32 vcc, -1, v6
s_sub_u32 s49, s49, 1
s_cmp_gt_i32 s49, 0
s_cselect_b64 vcc, vcc, 0
v_mfma_f32_16x16x32_bf16 v[138:141], v[206:209], v[106:109], v[138:141]
ds_read_b128 v[194:197], v4 offset:25600
v_mfma_f32_16x16x32_bf16 v[142:145], v[206:209], v[110:113], v[142:145]
s_mov_b32 s50, 1
s_cbranch_vccnz Lr1_29_%=
s_waitcnt lgkmcnt(5)
v_mfma_f32_16x16x32_bf16 v[146:149], v[170:173], a[176:179], 0
ds_read_b128 v[198:201], v4 offset:26624
v_max_f32 v162, 0, v162
v_max_f32 v163, 0, v163
v_mfma_f32_16x16x32_bf16 v[150:153], v[170:173], a[180:183], 0
v_max_f32 v164, 0, v164
v_max_f32 v165, 0, v165
v_mfma_f32_16x16x32_bf16 v[154:157], v[174:177], a[176:179], 0
ds_read_b128 v[202:205], v4 offset:32768
v_max_f32 v166, 0, v166
v_max_f32 v167, 0, v167
v_mfma_f32_16x16x32_bf16 v[158:161], v[174:177], a[180:183], 0
v_max_f32 v168, 0, v168
v_max_f32 v169, 0, v169
s_waitcnt lgkmcnt(5)
v_mfma_f32_16x16x32_bf16 v[146:149], v[178:181], a[184:187], v[146:149]
ds_read_b128 v[206:209], v4 offset:33792
v_cvt_pk_bf16_f32 v162, v162, v163
v_cvt_pk_bf16_f32 v163, v164, v165
v_mfma_f32_16x16x32_bf16 v[150:153], v[178:181], a[188:191], v[150:153]
v_cvt_pk_bf16_f32 v164, v166, v167
v_cvt_pk_bf16_f32 v165, v168, v169
v_mfma_f32_16x16x32_bf16 v[154:157], v[182:185], a[184:187], v[154:157]
ds_read_b128 v[170:173], v4 offset:39936
v_max_f32 v114, 0, v114
v_max_f32 v115, 0, v115
v_mfma_f32_16x16x32_bf16 v[158:161], v[182:185], a[188:191], v[158:161]
v_permlane16_swap_b32 v162, v164
v_permlane16_swap_b32 v163, v165
s_waitcnt lgkmcnt(5)
v_mfma_f32_16x16x32_bf16 v[146:149], v[186:189], a[192:195], v[146:149]
ds_read_b128 v[174:177], v4 offset:40960
global_store_dwordx4 v19, v[162:165], s[4:5] offset:3072 sc1
v_mfma_f32_16x16x32_bf16 v[150:153], v[186:189], a[196:199], v[150:153]
v_max_f32 v116, 0, v116
v_max_f32 v117, 0, v117
v_mfma_f32_16x16x32_bf16 v[154:157], v[190:193], a[192:195], v[154:157]
ds_read_b128 v[178:181], v4 offset:47104
v_max_f32 v118, 0, v118
v_max_f32 v119, 0, v119
v_mfma_f32_16x16x32_bf16 v[158:161], v[190:193], a[196:199], v[158:161]
v_max_f32 v120, 0, v120
v_max_f32 v121, 0, v121
s_waitcnt lgkmcnt(5)
v_mfma_f32_16x16x32_bf16 v[146:149], v[194:197], a[200:203], v[146:149]
ds_read_b128 v[182:185], v4 offset:48128
v_cvt_pk_bf16_f32 v114, v114, v115
v_cvt_pk_bf16_f32 v115, v116, v117
v_mfma_f32_16x16x32_bf16 v[150:153], v[194:197], a[204:207], v[150:153]
v_cvt_pk_bf16_f32 v116, v118, v119
v_cvt_pk_bf16_f32 v117, v120, v121
v_mfma_f32_16x16x32_bf16 v[154:157], v[198:201], a[200:203], v[154:157]
ds_read_b128 v[186:189], v4 offset:54272
v_max_f32 v122, 0, v122
v_max_f32 v123, 0, v123
v_mfma_f32_16x16x32_bf16 v[158:161], v[198:201], a[204:207], v[158:161]
v_permlane16_swap_b32 v114, v116
v_permlane16_swap_b32 v115, v117
s_waitcnt lgkmcnt(5)
v_mfma_f32_16x16x32_bf16 v[146:149], v[202:205], a[208:211], v[146:149]
ds_read_b128 v[190:193], v4 offset:55296
global_store_dwordx4 v19, v[114:117], s[4:5] offset:0 sc1
v_mfma_f32_16x16x32_bf16 v[150:153], v[202:205], a[212:215], v[150:153]
v_max_f32 v124, 0, v124
v_max_f32 v125, 0, v125
v_mfma_f32_16x16x32_bf16 v[154:157], v[206:209], a[208:211], v[154:157]
ds_read_b128 v[194:197], v4 offset:61440
v_max_f32 v126, 0, v126
v_max_f32 v127, 0, v127
v_mfma_f32_16x16x32_bf16 v[158:161], v[206:209], a[212:215], v[158:161]
v_max_f32 v128, 0, v128
v_max_f32 v129, 0, v129
s_waitcnt lgkmcnt(5)
v_mfma_f32_16x16x32_bf16 v[146:149], v[170:173], a[216:219], v[146:149]
ds_read_b128 v[198:201], v4 offset:62464
v_cvt_pk_bf16_f32 v122, v122, v123
v_cvt_pk_bf16_f32 v123, v124, v125
v_mfma_f32_16x16x32_bf16 v[150:153], v[170:173], a[220:223], v[150:153]
v_cvt_pk_bf16_f32 v124, v126, v127
v_cvt_pk_bf16_f32 v125, v128, v129
v_mfma_f32_16x16x32_bf16 v[154:157], v[174:177], a[216:219], v[154:157]
ds_read_b128 v[202:205], v11 offset:3072
v_max_f32 v130, 0, v130
v_max_f32 v131, 0, v131
v_mfma_f32_16x16x32_bf16 v[158:161], v[174:177], a[220:223], v[158:161]
v_permlane16_swap_b32 v122, v124
v_permlane16_swap_b32 v123, v125
s_waitcnt lgkmcnt(5)
v_mfma_f32_16x16x32_bf16 v[146:149], v[178:181], a[224:227], v[146:149]
)ASM"
R"ASM(ds_read_b128 v[206:209], v11 offset:4096
global_store_dwordx4 v19, v[122:125], s[4:5] offset:512 sc1
v_mfma_f32_16x16x32_bf16 v[150:153], v[178:181], a[228:231], v[150:153]
v_max_f32 v132, 0, v132
v_max_f32 v133, 0, v133
v_mfma_f32_16x16x32_bf16 v[154:157], v[182:185], a[224:227], v[154:157]
ds_read_b128 v[170:173], v11 offset:10240
v_max_f32 v134, 0, v134
v_max_f32 v135, 0, v135
v_mfma_f32_16x16x32_bf16 v[158:161], v[182:185], a[228:231], v[158:161]
v_max_f32 v136, 0, v136
v_max_f32 v137, 0, v137
s_waitcnt lgkmcnt(5)
v_mfma_f32_16x16x32_bf16 v[146:149], v[186:189], a[232:235], v[146:149]
ds_read_b128 v[174:177], v11 offset:11264
v_cvt_pk_bf16_f32 v130, v130, v131
v_cvt_pk_bf16_f32 v131, v132, v133
v_mfma_f32_16x16x32_bf16 v[150:153], v[186:189], a[236:239], v[150:153]
v_cvt_pk_bf16_f32 v132, v134, v135
v_cvt_pk_bf16_f32 v133, v136, v137
v_mfma_f32_16x16x32_bf16 v[154:157], v[190:193], a[232:235], v[154:157]
ds_read_b128 v[178:181], v11 offset:17408
v_max_f32 v138, 0, v138
v_max_f32 v139, 0, v139
v_mfma_f32_16x16x32_bf16 v[158:161], v[190:193], a[236:239], v[158:161]
v_permlane16_swap_b32 v130, v132
v_permlane16_swap_b32 v131, v133
s_waitcnt lgkmcnt(5)
v_mfma_f32_16x16x32_bf16 v[146:149], v[194:197], a[240:243], v[146:149]
ds_read_b128 v[182:185], v11 offset:18432
global_store_dwordx4 v19, v[130:133], s[4:5] offset:1024 sc1
v_mfma_f32_16x16x32_bf16 v[150:153], v[194:197], a[244:247], v[150:153]
v_max_f32 v140, 0, v140
v_max_f32 v141, 0, v141
v_mfma_f32_16x16x32_bf16 v[154:157], v[198:201], a[240:243], v[154:157]
ds_read_b128 v[186:189], v11 offset:24576
v_max_f32 v142, 0, v142
v_max_f32 v143, 0, v143
v_mfma_f32_16x16x32_bf16 v[158:161], v[198:201], a[244:247], v[158:161]
v_max_f32 v144, 0, v144
v_max_f32 v145, 0, v145
s_waitcnt lgkmcnt(5)
v_mfma_f32_16x16x32_bf16 v[146:149], v[202:205], a[248:251], v[146:149]
ds_read_b128 v[190:193], v11 offset:25600
v_cvt_pk_bf16_f32 v138, v138, v139
v_cvt_pk_bf16_f32 v139, v140, v141
v_mfma_f32_16x16x32_bf16 v[150:153], v[202:205], a[252:255], v[150:153]
v_cvt_pk_bf16_f32 v140, v142, v143
v_cvt_pk_bf16_f32 v141, v144, v145
v_mfma_f32_16x16x32_bf16 v[154:157], v[206:209], a[248:251], v[154:157]
ds_read_b128 v[194:197], v11 offset:31744
v_mfma_f32_16x16x32_bf16 v[158:161], v[206:209], a[252:255], v[158:161]
v_permlane16_swap_b32 v138, v140
v_permlane16_swap_b32 v139, v141
s_waitcnt lgkmcnt(5)
v_mfma_f32_16x16x32_bf16 v[146:149], v[170:173], v[34:37], v[146:149]
ds_read_b128 v[198:201], v11 offset:32768
global_store_dwordx4 v19, v[138:141], s[4:5] offset:1536 sc1
v_mfma_f32_16x16x32_bf16 v[150:153], v[170:173], v[38:41], v[150:153]
global_load_dwordx4 a[176:179], v4, s[60:61] offset:-4096
v_mfma_f32_16x16x32_bf16 v[154:157], v[174:177], v[34:37], v[154:157]
ds_read_b128 v[202:205], v11 offset:38912
v_mfma_f32_16x16x32_bf16 v[158:161], v[174:177], v[38:41], v[158:161]
global_load_dwordx4 a[180:183], v4, s[60:61] offset:-3072
s_waitcnt lgkmcnt(5)
v_mfma_f32_16x16x32_bf16 v[146:149], v[178:181], v[42:45], v[146:149]
ds_read_b128 v[206:209], v11 offset:39936
v_mfma_f32_16x16x32_bf16 v[150:153], v[178:181], v[46:49], v[150:153]
global_load_dwordx4 a[184:187], v4, s[60:61] offset:-2048
v_mfma_f32_16x16x32_bf16 v[154:157], v[182:185], v[42:45], v[154:157]
ds_read_b128 v[170:173], v11 offset:46080
v_mfma_f32_16x16x32_bf16 v[158:161], v[182:185], v[46:49], v[158:161]
global_load_dwordx4 a[188:191], v4, s[60:61] offset:-1024
s_waitcnt lgkmcnt(5)
v_mfma_f32_16x16x32_bf16 v[146:149], v[186:189], v[50:53], v[146:149]
ds_read_b128 v[174:177], v11 offset:47104
v_mfma_f32_16x16x32_bf16 v[150:153], v[186:189], v[54:57], v[150:153]
global_load_dwordx4 a[192:195], v4, s[60:61] offset:0
v_mfma_f32_16x16x32_bf16 v[154:157], v[190:193], v[50:53], v[154:157]
ds_read_b128 v[178:181], v11 offset:53248
v_mfma_f32_16x16x32_bf16 v[158:161], v[190:193], v[54:57], v[158:161]
global_load_dwordx4 a[196:199], v4, s[60:61] offset:1024
s_waitcnt lgkmcnt(5)
v_mfma_f32_16x16x32_bf16 v[146:149], v[194:197], v[58:61], v[146:149]
ds_read_b128 v[182:185], v11 offset:54272
v_mfma_f32_16x16x32_bf16 v[150:153], v[194:197], v[62:65], v[150:153]
global_load_dwordx4 a[200:203], v4, s[60:61] offset:2048
v_mfma_f32_16x16x32_bf16 v[154:157], v[198:201], v[58:61], v[154:157]
ds_read_b128 v[186:189], v11 offset:60416
v_mfma_f32_16x16x32_bf16 v[158:161], v[198:201], v[62:65], v[158:161]
global_load_dwordx4 a[204:207], v4, s[60:61] offset:3072
s_waitcnt lgkmcnt(5)
v_mfma_f32_16x16x32_bf16 v[146:149], v[202:205], v[66:69], v[146:149]
ds_read_b128 v[190:193], v11 offset:61440
v_mfma_f32_16x16x32_bf16 v[150:153], v[202:205], v[70:73], v[150:153]
s_add_u32 s60, s60, 0x2000
s_addc_u32 s61, s61, 0
global_load_dwordx4 a[208:211], v4, s[60:61] offset:-4096
v_mfma_f32_16x16x32_bf16 v[154:157], v[206:209], v[66:69], v[154:157]
ds_read_b128 v[194:197], v12 offset:2048
v_mfma_f32_16x16x32_bf16 v[158:161], v[206:209], v[70:73], v[158:161]
global_load_dwordx4 a[212:215], v4, s[60:61] offset:-3072
s_waitcnt lgkmcnt(5)
v_mfma_f32_16x16x32_bf16 v[146:149], v[170:173], v[74:77], v[146:149]
ds_read_b128 v[198:201], v12 offset:3072
v_mfma_f32_16x16x32_bf16 v[150:153], v[170:173], v[78:81], v[150:153]
v_mfma_f32_16x16x32_bf16 v[154:157], v[174:177], v[74:77], v[154:157]
ds_read_b128 v[202:205], v12 offset:9216
v_mfma_f32_16x16x32_bf16 v[158:161], v[174:177], v[78:81], v[158:161]
s_waitcnt lgkmcnt(5)
v_mfma_f32_16x16x32_bf16 v[146:149], v[178:181], v[82:85], v[146:149]
ds_read_b128 v[206:209], v12 offset:10240
v_mfma_f32_16x16x32_bf16 v[150:153], v[178:181], v[86:89], v[150:153]
v_mfma_f32_16x16x32_bf16 v[154:157], v[182:185], v[82:85], v[154:157]
v_mfma_f32_16x16x32_bf16 v[158:161], v[182:185], v[86:89], v[158:161]
s_waitcnt lgkmcnt(4)
v_mfma_f32_16x16x32_bf16 v[146:149], v[186:189], v[90:93], v[146:149]
v_mfma_f32_16x16x32_bf16 v[150:153], v[186:189], v[94:97], v[150:153]
v_mfma_f32_16x16x32_bf16 v[154:157], v[190:193], v[90:93], v[154:157]
v_mfma_f32_16x16x32_bf16 v[158:161], v[190:193], v[94:97], v[158:161]
s_waitcnt lgkmcnt(2)
v_mfma_f32_16x16x32_bf16 v[146:149], v[194:197], v[98:101], v[146:149]
v_mfma_f32_16x16x32_bf16 v[150:153], v[194:197], v[102:105], v[150:153]
v_mfma_f32_16x16x32_bf16 v[154:157], v[198:201], v[98:101], v[154:157]
v_mfma_f32_16x16x32_bf16 v[158:161], v[198:201], v[102:105], v[158:161]
s_waitcnt lgkmcnt(0)
v_mfma_f32_16x16x32_bf16 v[146:149], v[202:205], v[106:109], v[146:149]
v_mfma_f32_16x16x32_bf16 v[150:153], v[202:205], v[110:113], v[150:153]
v_mfma_f32_16x16x32_bf16 v[154:157], v[206:209], v[106:109], v[154:157]
v_mfma_f32_16x16x32_bf16 v[158:161], v[206:209], v[110:113], v[158:161]
s_nop 4
v_cvt_pk_bf16_f32 v146, v146, v147
v_cvt_pk_bf16_f32 v147, v148, v149
v_cvt_pk_bf16_f32 v148, v150, v151
v_cvt_pk_bf16_f32 v149, v152, v153
v_pk_max_i16 v146, v146, 0
v_pk_max_i16 v147, v147, 0
v_pk_max_i16 v148, v148, 0
v_pk_max_i16 v149, v149, 0
v_cvt_pk_bf16_f32 v154, v154, v155
v_cvt_pk_bf16_f32 v155, v156, v157
v_cvt_pk_bf16_f32 v156, v158, v159
v_cvt_pk_bf16_f32 v157, v160, v161
v_permlane16_swap_b32 v146, v148
v_permlane16_swap_b32 v147, v149
global_store_dwordx4 v19, v[146:149], s[4:5] offset:2048 sc1
v_pk_max_i16 v154, v154, 0
v_pk_max_i16 v155, v155, 0
v_pk_max_i16 v156, v156, 0
v_pk_max_i16 v157, v157, 0
s_nop 0
v_permlane16_swap_b32 v154, v156
v_permlane16_swap_b32 v155, v157
global_store_dwordx4 v19, v[154:157], s[4:5] offset:2560 sc1
s_add_u32 s35, s29, 1
s_and_b32 s35, s35, 3
s_lshl_b32 s35, s35, 8
s_add_u32 s35, s35, 0x23000
v_mov_b32 v206, s35
s_add_u32 s35, s29, 2
s_and_b32 s35, s35, 3
s_lshl_b32 s35, s35, 8
s_add_u32 s35, s35, 0x23000
v_mov_b32 v207, s35
s_add_u32 s35, s29, 3
s_and_b32 s35, s35, 3
s_lshl_b32 s35, s35, 8
s_add_u32 s35, s35, 0x23000
v_mov_b32 v210, s35
s_add_u32 s35, s29, 0
s_and_b32 s35, s35, 3
s_lshl_b32 s35, s35, 8
s_add_u32 s35, s35, 0x23000
v_mov_b32 v211, s35
v_mov_b32 v7, 0
ds_write_b32 v211, v7 offset:192
ds_write_b32 v211, v7 offset:196
ds_write_b32 v211, v7 offset:200
s_waitcnt lgkmcnt(0)
s_barrier
global_load_dwordx4 a[216:219], v4, s[60:61] offset:-2048
global_load_dwordx4 a[220:223], v4, s[60:61] offset:-1024
global_load_dwordx4 a[224:227], v4, s[60:61] offset:0
global_load_dwordx4 a[228:231], v4, s[60:61] offset:1024
global_load_dwordx4 a[232:235], v4, s[60:61] offset:2048
global_load_dwordx4 a[236:239], v4, s[60:61] offset:3072
s_add_u32 s60, s60, 0x2000
s_addc_u32 s61, s61, 0
global_load_dwordx4 a[240:243], v4, s[60:61] offset:-4096
global_load_dwordx4 a[244:247], v4, s[60:61] offset:-3072
global_load_dwordx4 a[248:251], v4, s[60:61] offset:-2048
global_load_dwordx4 a[252:255], v4, s[60:61] offset:-1024
; ==== FFN2: out = h + rstd * (H W2), K split over the four waves
global_load_dword v7, v13, s[20:21] sc1
global_load_dwordx4 v[36:39], v4, s[10:11] offset:-4096 sc1
global_load_dwordx4 v[40:43], v4, s[10:11] offset:-3072 sc1
global_load_dwordx4 v[44:47], v4, s[10:11] offset:-2048 sc1
global_load_dwordx4 v[48:51], v4, s[10:11] offset:-1024 sc1
s_waitcnt vmcnt(0)
v_max3_u32 v7, v7, v36, v37
)ASM"
R"ASM(v_max3_u32 v7, v7, v38, v39
v_max3_u32 v7, v7, v40, v41
v_max3_u32 v7, v7, v42, v43
v_max3_u32 v7, v7, v44, v45
v_max3_u32 v7, v7, v46, v47
v_max3_u32 v7, v7, v48, v49
v_max3_u32 v7, v7, v50, v51
v_cmp_eq_u32 vcc, -1, v7
v_mov_b32 v6, 0
s_cbranch_vccnz Lg_30_%=
Lgd_31_%=:
global_load_dwordx4 v[52:55], v4, s[10:11] offset:0 sc1
global_load_dwordx4 v[56:59], v4, s[10:11] offset:1024 sc1
global_load_dwordx4 v[60:63], v4, s[10:11] offset:2048 sc1
global_load_dwordx4 v[64:67], v4, s[10:11] offset:3072 sc1
global_load_dword v208, v15, s[38:39] sc1
global_load_dword v209, v15, s[38:39] offset:64 sc1
v_mfma_f32_16x16x32_bf16 v[146:149], a[0:3], v[36:39], 0
s_add_u32 s10, s10, 0x2000
s_addc_u32 s11, s11, 0
global_load_dwordx4 v[68:71], v4, s[10:11] offset:-4096 sc1
v_mfma_f32_16x16x32_bf16 v[150:153], a[4:7], v[36:39], 0
v_max3_u32 v6, v6, v36, v37
v_max3_u32 v6, v6, v38, v39
v_mfma_f32_16x16x32_bf16 v[154:157], a[8:11], v[36:39], 0
v_mfma_f32_16x16x32_bf16 v[158:161], a[12:15], v[36:39], 0
v_mfma_f32_16x16x32_bf16 v[162:165], a[16:19], v[36:39], 0
v_mfma_f32_16x16x32_bf16 v[146:149], a[20:23], v[40:43], v[146:149]
global_load_dwordx4 v[72:75], v4, s[10:11] offset:-3072 sc1
v_mfma_f32_16x16x32_bf16 v[150:153], a[24:27], v[40:43], v[150:153]
v_max3_u32 v6, v6, v40, v41
v_max3_u32 v6, v6, v42, v43
v_mfma_f32_16x16x32_bf16 v[154:157], a[28:31], v[40:43], v[154:157]
v_mfma_f32_16x16x32_bf16 v[158:161], a[32:35], v[40:43], v[158:161]
v_mfma_f32_16x16x32_bf16 v[162:165], a[36:39], v[40:43], v[162:165]
v_mfma_f32_16x16x32_bf16 v[146:149], a[40:43], v[44:47], v[146:149]
global_load_dwordx4 v[76:79], v4, s[10:11] offset:-2048 sc1
v_mfma_f32_16x16x32_bf16 v[150:153], a[44:47], v[44:47], v[150:153]
v_max3_u32 v6, v6, v44, v45
v_max3_u32 v6, v6, v46, v47
v_mfma_f32_16x16x32_bf16 v[154:157], a[48:51], v[44:47], v[154:157]
v_mfma_f32_16x16x32_bf16 v[158:161], a[52:55], v[44:47], v[158:161]
v_mfma_f32_16x16x32_bf16 v[162:165], a[56:59], v[44:47], v[162:165]
v_mfma_f32_16x16x32_bf16 v[146:149], a[60:63], v[48:51], v[146:149]
global_load_dwordx4 v[80:83], v4, s[10:11] offset:-1024 sc1
v_mfma_f32_16x16x32_bf16 v[150:153], a[64:67], v[48:51], v[150:153]
v_max3_u32 v6, v6, v48, v49
v_max3_u32 v6, v6, v50, v51
v_mfma_f32_16x16x32_bf16 v[154:157], a[68:71], v[48:51], v[154:157]
v_mfma_f32_16x16x32_bf16 v[158:161], a[72:75], v[48:51], v[158:161]
v_mfma_f32_16x16x32_bf16 v[162:165], a[76:79], v[48:51], v[162:165]
s_waitcnt vmcnt(9)
v_mfma_f32_16x16x32_bf16 v[146:149], a[80:83], v[52:55], v[146:149]
global_load_dwordx4 v[84:87], v4, s[10:11] offset:0 sc1
v_mfma_f32_16x16x32_bf16 v[150:153], a[84:87], v[52:55], v[150:153]
v_max3_u32 v6, v6, v52, v53
v_max3_u32 v6, v6, v54, v55
v_mfma_f32_16x16x32_bf16 v[154:157], a[88:91], v[52:55], v[154:157]
v_mfma_f32_16x16x32_bf16 v[158:161], a[92:95], v[52:55], v[158:161]
v_mfma_f32_16x16x32_bf16 v[162:165], a[96:99], v[52:55], v[162:165]
s_waitcnt vmcnt(9)
v_mfma_f32_16x16x32_bf16 v[146:149], a[100:103], v[56:59], v[146:149]
global_load_dwordx4 v[88:91], v14, s[10:11] offset:0 sc1
v_mfma_f32_16x16x32_bf16 v[150:153], a[104:107], v[56:59], v[150:153]
v_max3_u32 v6, v6, v56, v57
v_max3_u32 v6, v6, v58, v59
v_mfma_f32_16x16x32_bf16 v[154:157], a[108:111], v[56:59], v[154:157]
v_mfma_f32_16x16x32_bf16 v[158:161], a[112:115], v[56:59], v[158:161]
v_mfma_f32_16x16x32_bf16 v[162:165], a[116:119], v[56:59], v[162:165]
s_waitcnt vmcnt(9)
v_mfma_f32_16x16x32_bf16 v[146:149], a[120:123], v[60:63], v[146:149]
global_load_dwordx4 v[92:95], v4, s[12:13] offset:-4096 sc1
v_mfma_f32_16x16x32_bf16 v[150:153], a[124:127], v[60:63], v[150:153]
v_max3_u32 v6, v6, v60, v61
v_max3_u32 v6, v6, v62, v63
v_mfma_f32_16x16x32_bf16 v[154:157], a[128:131], v[60:63], v[154:157]
v_mfma_f32_16x16x32_bf16 v[158:161], a[132:135], v[60:63], v[158:161]
v_mfma_f32_16x16x32_bf16 v[162:165], a[136:139], v[60:63], v[162:165]
s_waitcnt vmcnt(9)
v_mfma_f32_16x16x32_bf16 v[146:149], a[140:143], v[64:67], v[146:149]
global_load_dwordx4 v[96:99], v4, s[12:13] offset:-3072 sc1
v_mfma_f32_16x16x32_bf16 v[150:153], a[144:147], v[64:67], v[150:153]
v_max3_u32 v6, v6, v64, v65
v_max3_u32 v6, v6, v66, v67
v_mfma_f32_16x16x32_bf16 v[154:157], a[148:151], v[64:67], v[154:157]
v_mfma_f32_16x16x32_bf16 v[158:161], a[152:155], v[64:67], v[158:161]
v_mfma_f32_16x16x32_bf16 v[162:165], a[156:159], v[64:67], v[162:165]
s_waitcnt vmcnt(7)
v_mfma_f32_16x16x32_bf16 v[146:149], a[160:163], v[68:71], v[146:149]
global_load_dwordx4 v[100:103], v4, s[12:13] offset:-2048 sc1
v_mfma_f32_16x16x32_bf16 v[150:153], a[164:167], v[68:71], v[150:153]
v_max3_u32 v6, v6, v68, v69
v_max3_u32 v6, v6, v70, v71
v_mfma_f32_16x16x32_bf16 v[154:157], a[168:171], v[68:71], v[154:157]
v_mfma_f32_16x16x32_bf16 v[158:161], a[172:175], v[68:71], v[158:161]
v_mfma_f32_16x16x32_bf16 v[162:165], a[176:179], v[68:71], v[162:165]
s_waitcnt vmcnt(7)
v_mfma_f32_16x16x32_bf16 v[146:149], a[180:183], v[72:75], v[146:149]
global_load_dwordx4 v[104:107], v4, s[12:13] offset:-1024 sc1
v_mfma_f32_16x16x32_bf16 v[150:153], a[184:187], v[72:75], v[150:153]
v_max3_u32 v6, v6, v72, v73
v_max3_u32 v6, v6, v74, v75
v_mfma_f32_16x16x32_bf16 v[154:157], a[188:191], v[72:75], v[154:157]
v_mfma_f32_16x16x32_bf16 v[158:161], a[192:195], v[72:75], v[158:161]
v_mfma_f32_16x16x32_bf16 v[162:165], a[196:199], v[72:75], v[162:165]
s_waitcnt vmcnt(7)
v_mfma_f32_16x16x32_bf16 v[146:149], a[200:203], v[76:79], v[146:149]
global_load_dwordx4 v[108:111], v4, s[12:13] offset:0 sc1
v_mfma_f32_16x16x32_bf16 v[150:153], a[204:207], v[76:79], v[150:153]
v_max3_u32 v6, v6, v76, v77
v_max3_u32 v6, v6, v78, v79
v_mfma_f32_16x16x32_bf16 v[154:157], a[208:211], v[76:79], v[154:157]
v_mfma_f32_16x16x32_bf16 v[158:161], a[212:215], v[76:79], v[158:161]
v_mfma_f32_16x16x32_bf16 v[162:165], a[216:219], v[76:79], v[162:165]
s_waitcnt vmcnt(7)
v_mfma_f32_16x16x32_bf16 v[146:149], a[220:223], v[80:83], v[146:149]
global_load_dwordx4 v[112:115], v4, s[12:13] offset:1024 sc1
v_mfma_f32_16x16x32_bf16 v[150:153], a[224:227], v[80:83], v[150:153]
v_max3_u32 v6, v6, v80, v81
v_max3_u32 v6, v6, v82, v83
v_mfma_f32_16x16x32_bf16 v[154:157], a[228:231], v[80:83], v[154:157]
v_mfma_f32_16x16x32_bf16 v[158:161], a[232:235], v[80:83], v[158:161]
v_mfma_f32_16x16x32_bf16 v[162:165], a[236:239], v[80:83], v[162:165]
s_waitcnt vmcnt(7)
v_mfma_f32_16x16x32_bf16 v[146:149], a[240:243], v[84:87], v[146:149]
global_load_dwordx4 v[116:119], v4, s[12:13] offset:2048 sc1
v_mfma_f32_16x16x32_bf16 v[150:153], a[244:247], v[84:87], v[150:153]
v_max3_u32 v6, v6, v84, v85
v_max3_u32 v6, v6, v86, v87
v_mfma_f32_16x16x32_bf16 v[154:157], a[248:251], v[84:87], v[154:157]
v_mfma_f32_16x16x32_bf16 v[158:161], a[252:255], v[84:87], v[158:161]
v_mfma_f32_16x16x32_bf16 v[162:165], v[232:235], v[84:87], v[162:165]
s_waitcnt vmcnt(7)
v_mfma_f32_16x16x32_bf16 v[146:149], v[236:239], v[88:91], v[146:149]
global_load_dwordx4 v[120:123], v4, s[12:13] offset:3072 sc1
v_mfma_f32_16x16x32_bf16 v[150:153], v[240:243], v[88:91], v[150:153]
v_max3_u32 v6, v6, v88, v89
v_max3_u32 v6, v6, v90, v91
v_mfma_f32_16x16x32_bf16 v[154:157], v[244:247], v[88:91], v[154:157]
v_mfma_f32_16x16x32_bf16 v[158:161], v[248:251], v[88:91], v[158:161]
v_cmp_eq_u32 vcc, -1, v6
v_mov_b32 v6, 0
v_mfma_f32_16x16x32_bf16 v[162:165], v[252:255], v[88:91], v[162:165]
s_cbranch_vccnz Lst_34_%=
Lsr_35_%=:
s_waitcnt vmcnt(7)
v_mfma_f32_16x16x32_bf16 v[166:169], a[0:3], v[92:95], 0
s_add_u32 s12, s12, 0x2000
s_addc_u32 s13, s13, 0
global_load_dwordx4 v[124:127], v4, s[12:13] offset:-4096 sc1
v_mfma_f32_16x16x32_bf16 v[170:173], a[4:7], v[92:95], 0
v_max3_u32 v6, v6, v92, v93
v_max3_u32 v6, v6, v94, v95
v_mfma_f32_16x16x32_bf16 v[174:177], a[8:11], v[92:95], 0
v_mfma_f32_16x16x32_bf16 v[178:181], a[12:15], v[92:95], 0
v_mfma_f32_16x16x32_bf16 v[182:185], a[16:19], v[92:95], 0
ds_write_b128 v2, v[146:149] offset:0
s_waitcnt vmcnt(7)
v_mfma_f32_16x16x32_bf16 v[166:169], a[20:23], v[96:99], v[166:169]
global_load_dwordx4 v[128:131], v4, s[12:13] offset:-3072 sc1
v_mfma_f32_16x16x32_bf16 v[170:173], a[24:27], v[96:99], v[170:173]
v_max3_u32 v6, v6, v96, v97
v_max3_u32 v6, v6, v98, v99
v_mfma_f32_16x16x32_bf16 v[174:177], a[28:31], v[96:99], v[174:177]
ds_write_b128 v2, v[150:153] offset:1024
v_mfma_f32_16x16x32_bf16 v[178:181], a[32:35], v[96:99], v[178:181]
ds_write_b128 v2, v[154:157] offset:2048
v_mfma_f32_16x16x32_bf16 v[182:185], a[36:39], v[96:99], v[182:185]
ds_write_b128 v2, v[158:161] offset:3072
s_waitcnt vmcnt(7)
v_mfma_f32_16x16x32_bf16 v[166:169], a[40:43], v[100:103], v[166:169]
global_load_dwordx4 v[132:135], v4, s[12:13] offset:-2048 sc1
v_mfma_f32_16x16x32_bf16 v[170:173], a[44:47], v[100:103], v[170:173]
v_max3_u32 v6, v6, v100, v101
v_max3_u32 v6, v6, v102, v103
v_mfma_f32_16x16x32_bf16 v[174:177], a[48:51], v[100:103], v[174:177]
ds_write_b128 v2, v[162:165] offset:4096
v_mfma_f32_16x16x32_bf16 v[178:181], a[52:55], v[100:103], v[178:181]
ds_write_b32 v206, v211 offset:192
v_mfma_f32_16x16x32_bf16 v[182:185], a[56:59], v[100:103], v[182:185]
s_waitcnt vmcnt(7)
v_mfma_f32_16x16x32_bf16 v[166:169], a[60:63], v[104:107], v[166:169]
global_load_dwordx4 v[136:139], v4, s[12:13] offset:-1024 sc1
v_mfma_f32_16x16x32_bf16 v[170:173], a[64:67], v[104:107], v[170:173]
v_max3_u32 v6, v6, v104, v105
v_max3_u32 v6, v6, v106, v107
v_mfma_f32_16x16x32_bf16 v[174:177], a[68:71], v[104:107], v[174:177]
v_mfma_f32_16x16x32_bf16 v[178:181], a[72:75], v[104:107], v[178:181]
v_mfma_f32_16x16x32_bf16 v[182:185], a[76:79], v[104:107], v[182:185]
s_waitcnt vmcnt(7)
v_mfma_f32_16x16x32_bf16 v[166:169], a[80:83], v[108:111], v[166:169]
global_load_dwordx4 v[36:39], v4, s[12:13] offset:0 sc1
v_mfma_f32_16x16x32_bf16 v[170:173], a[84:87], v[108:111], v[170:173]
v_max3_u32 v6, v6, v108, v109
v_max3_u32 v6, v6, v110, v111
v_mfma_f32_16x16x32_bf16 v[174:177], a[88:91], v[108:111], v[174:177]
v_mfma_f32_16x16x32_bf16 v[178:181], a[92:95], v[108:111], v[178:181]
v_mfma_f32_16x16x32_bf16 v[182:185], a[96:99], v[108:111], v[182:185]
s_waitcnt vmcnt(7)
v_mfma_f32_16x16x32_bf16 v[166:169], a[100:103], v[112:115], v[166:169]
)ASM"
R"ASM(global_load_dwordx4 v[40:43], v14, s[12:13] offset:0 sc1
v_mfma_f32_16x16x32_bf16 v[170:173], a[104:107], v[112:115], v[170:173]
v_max3_u32 v6, v6, v112, v113
v_max3_u32 v6, v6, v114, v115
v_mfma_f32_16x16x32_bf16 v[174:177], a[108:111], v[112:115], v[174:177]
v_mfma_f32_16x16x32_bf16 v[178:181], a[112:115], v[112:115], v[178:181]
v_mfma_f32_16x16x32_bf16 v[182:185], a[116:119], v[112:115], v[182:185]
s_waitcnt vmcnt(7)
v_mfma_f32_16x16x32_bf16 v[166:169], a[120:123], v[116:119], v[166:169]
global_load_dwordx4 v[44:47], v4, s[14:15] offset:-4096 sc1
v_mfma_f32_16x16x32_bf16 v[170:173], a[124:127], v[116:119], v[170:173]
v_max3_u32 v6, v6, v116, v117
v_max3_u32 v6, v6, v118, v119
v_mfma_f32_16x16x32_bf16 v[174:177], a[128:131], v[116:119], v[174:177]
v_mfma_f32_16x16x32_bf16 v[178:181], a[132:135], v[116:119], v[178:181]
v_mfma_f32_16x16x32_bf16 v[182:185], a[136:139], v[116:119], v[182:185]
s_waitcnt vmcnt(7)
v_mfma_f32_16x16x32_bf16 v[166:169], a[140:143], v[120:123], v[166:169]
global_load_dwordx4 v[48:51], v4, s[14:15] offset:-3072 sc1
v_mfma_f32_16x16x32_bf16 v[170:173], a[144:147], v[120:123], v[170:173]
v_max3_u32 v6, v6, v120, v121
v_max3_u32 v6, v6, v122, v123
v_mfma_f32_16x16x32_bf16 v[174:177], a[148:151], v[120:123], v[174:177]
v_mfma_f32_16x16x32_bf16 v[178:181], a[152:155], v[120:123], v[178:181]
v_mfma_f32_16x16x32_bf16 v[182:185], a[156:159], v[120:123], v[182:185]
s_waitcnt vmcnt(7)
v_mfma_f32_16x16x32_bf16 v[166:169], a[160:163], v[124:127], v[166:169]
global_load_dwordx4 v[52:55], v4, s[14:15] offset:-2048 sc1
v_mfma_f32_16x16x32_bf16 v[170:173], a[164:167], v[124:127], v[170:173]
v_max3_u32 v6, v6, v124, v125
v_max3_u32 v6, v6, v126, v127
v_mfma_f32_16x16x32_bf16 v[174:177], a[168:171], v[124:127], v[174:177]
v_mfma_f32_16x16x32_bf16 v[178:181], a[172:175], v[124:127], v[178:181]
v_mfma_f32_16x16x32_bf16 v[182:185], a[176:179], v[124:127], v[182:185]
s_waitcnt vmcnt(7)
v_mfma_f32_16x16x32_bf16 v[166:169], a[180:183], v[128:131], v[166:169]
global_load_dwordx4 v[56:59], v4, s[14:15] offset:-1024 sc1
v_mfma_f32_16x16x32_bf16 v[170:173], a[184:187], v[128:131], v[170:173]
v_max3_u32 v6, v6, v128, v129
v_max3_u32 v6, v6, v130, v131
v_mfma_f32_16x16x32_bf16 v[174:177], a[188:191], v[128:131], v[174:177]
v_mfma_f32_16x16x32_bf16 v[178:181], a[192:195], v[128:131], v[178:181]
v_mfma_f32_16x16x32_bf16 v[182:185], a[196:199], v[128:131], v[182:185]
s_waitcnt vmcnt(7)
v_mfma_f32_16x16x32_bf16 v[166:169], a[200:203], v[132:135], v[166:169]
global_load_dwordx4 v[60:63], v4, s[14:15] offset:0 sc1
v_mfma_f32_16x16x32_bf16 v[170:173], a[204:207], v[132:135], v[170:173]
v_max3_u32 v6, v6, v132, v133
v_max3_u32 v6, v6, v134, v135
v_mfma_f32_16x16x32_bf16 v[174:177], a[208:211], v[132:135], v[174:177]
v_mfma_f32_16x16x32_bf16 v[178:181], a[212:215], v[132:135], v[178:181]
v_mfma_f32_16x16x32_bf16 v[182:185], a[216:219], v[132:135], v[182:185]
s_waitcnt vmcnt(7)
v_mfma_f32_16x16x32_bf16 v[166:169], a[220:223], v[136:139], v[166:169]
global_load_dwordx4 v[64:67], v4, s[14:15] offset:1024 sc1
v_mfma_f32_16x16x32_bf16 v[170:173], a[224:227], v[136:139], v[170:173]
v_max3_u32 v6, v6, v136, v137
v_max3_u32 v6, v6, v138, v139
v_mfma_f32_16x16x32_bf16 v[174:177], a[228:231], v[136:139], v[174:177]
v_mfma_f32_16x16x32_bf16 v[178:181], a[232:235], v[136:139], v[178:181]
v_mfma_f32_16x16x32_bf16 v[182:185], a[236:239], v[136:139], v[182:185]
s_waitcnt vmcnt(7)
v_mfma_f32_16x16x32_bf16 v[166:169], a[240:243], v[36:39], v[166:169]
global_load_dwordx4 v[68:71], v4, s[14:15] offset:2048 sc1
v_mfma_f32_16x16x32_bf16 v[170:173], a[244:247], v[36:39], v[170:173]
v_max3_u32 v6, v6, v36, v37
v_max3_u32 v6, v6, v38, v39
v_mfma_f32_16x16x32_bf16 v[174:177], a[248:251], v[36:39], v[174:177]
v_mfma_f32_16x16x32_bf16 v[178:181], a[252:255], v[36:39], v[178:181]
v_mfma_f32_16x16x32_bf16 v[182:185], v[232:235], v[36:39], v[182:185]
s_waitcnt vmcnt(7)
v_mfma_f32_16x16x32_bf16 v[166:169], v[236:239], v[40:43], v[166:169]
global_load_dwordx4 v[72:75], v4, s[14:15] offset:3072 sc1
v_mfma_f32_16x16x32_bf16 v[170:173], v[240:243], v[40:43], v[170:173]
v_max3_u32 v6, v6, v40, v41
v_max3_u32 v6, v6, v42, v43
v_mfma_f32_16x16x32_bf16 v[174:177], v[244:247], v[40:43], v[174:177]
v_mfma_f32_16x16x32_bf16 v[178:181], v[248:251], v[40:43], v[178:181]
v_cmp_eq_u32 vcc, -1, v6
v_mov_b32 v6, 0
v_mfma_f32_16x16x32_bf16 v[182:185], v[252:255], v[40:43], v[182:185]
s_cbranch_vccnz Lst_36_%=
Lsr_37_%=:
s_waitcnt vmcnt(7)
v_mfma_f32_16x16x32_bf16 v[146:149], a[0:3], v[44:47], 0
s_add_u32 s14, s14, 0x2000
s_addc_u32 s15, s15, 0
global_load_dwordx4 v[76:79], v4, s[14:15] offset:-4096 sc1
v_mfma_f32_16x16x32_bf16 v[150:153], a[4:7], v[44:47], 0
v_max3_u32 v6, v6, v44, v45
v_max3_u32 v6, v6, v46, v47
v_mfma_f32_16x16x32_bf16 v[154:157], a[8:11], v[44:47], 0
v_mfma_f32_16x16x32_bf16 v[158:161], a[12:15], v[44:47], 0
v_mfma_f32_16x16x32_bf16 v[162:165], a[16:19], v[44:47], 0
ds_write_b128 v3, v[166:169] offset:0
s_waitcnt vmcnt(7)
v_mfma_f32_16x16x32_bf16 v[146:149], a[20:23], v[48:51], v[146:149]
global_load_dwordx4 v[80:83], v4, s[14:15] offset:-3072 sc1
v_mfma_f32_16x16x32_bf16 v[150:153], a[24:27], v[48:51], v[150:153]
v_max3_u32 v6, v6, v48, v49
v_max3_u32 v6, v6, v50, v51
v_mfma_f32_16x16x32_bf16 v[154:157], a[28:31], v[48:51], v[154:157]
ds_write_b128 v3, v[170:173] offset:1024
v_mfma_f32_16x16x32_bf16 v[158:161], a[32:35], v[48:51], v[158:161]
ds_write_b128 v3, v[174:177] offset:2048
v_mfma_f32_16x16x32_bf16 v[162:165], a[36:39], v[48:51], v[162:165]
ds_write_b128 v3, v[178:181] offset:3072
s_waitcnt vmcnt(7)
v_mfma_f32_16x16x32_bf16 v[146:149], a[40:43], v[52:55], v[146:149]
global_load_dwordx4 v[84:87], v4, s[14:15] offset:-2048 sc1
v_mfma_f32_16x16x32_bf16 v[150:153], a[44:47], v[52:55], v[150:153]
v_max3_u32 v6, v6, v52, v53
v_max3_u32 v6, v6, v54, v55
v_mfma_f32_16x16x32_bf16 v[154:157], a[48:51], v[52:55], v[154:157]
ds_write_b128 v3, v[182:185] offset:4096
v_mfma_f32_16x16x32_bf16 v[158:161], a[52:55], v[52:55], v[158:161]
ds_write_b32 v207, v211 offset:196
v_mfma_f32_16x16x32_bf16 v[162:165], a[56:59], v[52:55], v[162:165]
ds_read_b32 v142, v211 offset:192
s_waitcnt vmcnt(7)
v_mfma_f32_16x16x32_bf16 v[146:149], a[60:63], v[56:59], v[146:149]
global_load_dwordx4 v[88:91], v4, s[14:15] offset:-1024 sc1
v_mfma_f32_16x16x32_bf16 v[150:153], a[64:67], v[56:59], v[150:153]
v_max3_u32 v6, v6, v56, v57
v_max3_u32 v6, v6, v58, v59
v_mfma_f32_16x16x32_bf16 v[154:157], a[68:71], v[56:59], v[154:157]
ds_read_b128 v[16:19], v9 offset:0
v_mfma_f32_16x16x32_bf16 v[158:161], a[72:75], v[56:59], v[158:161]
ds_read_b128 v[20:23], v9 offset:1024
v_mfma_f32_16x16x32_bf16 v[162:165], a[76:79], v[56:59], v[162:165]
ds_read_b128 v[24:27], v9 offset:2048
s_waitcnt vmcnt(7)
v_mfma_f32_16x16x32_bf16 v[146:149], a[80:83], v[60:63], v[146:149]
global_load_dwordx4 v[92:95], v4, s[14:15] offset:0 sc1
v_mfma_f32_16x16x32_bf16 v[150:153], a[84:87], v[60:63], v[150:153]
v_max3_u32 v6, v6, v60, v61
v_max3_u32 v6, v6, v62, v63
v_mfma_f32_16x16x32_bf16 v[154:157], a[88:91], v[60:63], v[154:157]
ds_read_b128 v[28:31], v9 offset:3072
v_mfma_f32_16x16x32_bf16 v[158:161], a[92:95], v[60:63], v[158:161]
ds_read_b128 v[32:35], v9 offset:4096
v_mfma_f32_16x16x32_bf16 v[162:165], a[96:99], v[60:63], v[162:165]
s_waitcnt vmcnt(7)
v_mfma_f32_16x16x32_bf16 v[146:149], a[100:103], v[64:67], v[146:149]
global_load_dwordx4 v[96:99], v14, s[14:15] offset:0 sc1
v_mfma_f32_16x16x32_bf16 v[150:153], a[104:107], v[64:67], v[150:153]
v_max3_u32 v6, v6, v64, v65
v_max3_u32 v6, v6, v66, v67
v_mfma_f32_16x16x32_bf16 v[154:157], a[108:111], v[64:67], v[154:157]
v_mfma_f32_16x16x32_bf16 v[158:161], a[112:115], v[64:67], v[158:161]
s_waitcnt lgkmcnt(5)
v_cmp_eq_u32 s[46:47], 0, v142
s_nop 0
s_cmp_lg_u64 s[46:47], 0
s_cbranch_scc1 Lfs_38_%=
Lfr_39_%=:
v_mfma_f32_16x16x32_bf16 v[162:165], a[116:119], v[64:67], v[162:165]
s_waitcnt vmcnt(7)
v_mfma_f32_16x16x32_bf16 v[146:149], a[120:123], v[68:71], v[146:149]
global_load_dwordx4 v[100:103], v4, s[16:17] offset:-4096 sc1
v_mfma_f32_16x16x32_bf16 v[150:153], a[124:127], v[68:71], v[150:153]
v_max3_u32 v6, v6, v68, v69
v_max3_u32 v6, v6, v70, v71
v_mfma_f32_16x16x32_bf16 v[154:157], a[128:131], v[68:71], v[154:157]
v_mfma_f32_16x16x32_bf16 v[158:161], a[132:135], v[68:71], v[158:161]
v_mfma_f32_16x16x32_bf16 v[162:165], a[136:139], v[68:71], v[162:165]
s_waitcnt vmcnt(7)
v_mfma_f32_16x16x32_bf16 v[146:149], a[140:143], v[72:75], v[146:149]
global_load_dwordx4 v[104:107], v4, s[16:17] offset:-3072 sc1
v_mfma_f32_16x16x32_bf16 v[150:153], a[144:147], v[72:75], v[150:153]
v_max3_u32 v6, v6, v72, v73
v_max3_u32 v6, v6, v74, v75
v_mfma_f32_16x16x32_bf16 v[154:157], a[148:151], v[72:75], v[154:157]
v_mfma_f32_16x16x32_bf16 v[158:161], a[152:155], v[72:75], v[158:161]
v_mfma_f32_16x16x32_bf16 v[162:165], a[156:159], v[72:75], v[162:165]
s_waitcnt vmcnt(7)
v_mfma_f32_16x16x32_bf16 v[146:149], a[160:163], v[76:79], v[146:149]
global_load_dwordx4 v[108:111], v4, s[16:17] offset:-2048 sc1
v_mfma_f32_16x16x32_bf16 v[150:153], a[164:167], v[76:79], v[150:153]
v_max3_u32 v6, v6, v76, v77
v_max3_u32 v6, v6, v78, v79
v_mfma_f32_16x16x32_bf16 v[154:157], a[168:171], v[76:79], v[154:157]
v_mfma_f32_16x16x32_bf16 v[158:161], a[172:175], v[76:79], v[158:161]
v_mfma_f32_16x16x32_bf16 v[162:165], a[176:179], v[76:79], v[162:165]
s_waitcnt vmcnt(7)
v_mfma_f32_16x16x32_bf16 v[146:149], a[180:183], v[80:83], v[146:149]
global_load_dwordx4 v[112:115], v4, s[16:17] offset:-1024 sc1
v_mfma_f32_16x16x32_bf16 v[150:153], a[184:187], v[80:83], v[150:153]
v_max3_u32 v6, v6, v80, v81
v_max3_u32 v6, v6, v82, v83
v_mfma_f32_16x16x32_bf16 v[154:157], a[188:191], v[80:83], v[154:157]
v_mfma_f32_16x16x32_bf16 v[158:161], a[192:195], v[80:83], v[158:161]
v_mfma_f32_16x16x32_bf16 v[162:165], a[196:199], v[80:83], v[162:165]
s_waitcnt vmcnt(7)
v_mfma_f32_16x16x32_bf16 v[146:149], a[200:203], v[84:87], v[146:149]
global_load_dwordx4 v[116:119], v4, s[16:17] offset:0 sc1
v_mfma_f32_16x16x32_bf16 v[150:153], a[204:207], v[84:87], v[150:153]
v_max3_u32 v6, v6, v84, v85
v_max3_u32 v6, v6, v86, v87
v_mfma_f32_16x16x32_bf16 v[154:157], a[208:211], v[84:87], v[154:157]
)ASM"
R"ASM(v_mfma_f32_16x16x32_bf16 v[158:161], a[212:215], v[84:87], v[158:161]
v_mfma_f32_16x16x32_bf16 v[162:165], a[216:219], v[84:87], v[162:165]
s_waitcnt vmcnt(7)
v_mfma_f32_16x16x32_bf16 v[146:149], a[220:223], v[88:91], v[146:149]
global_load_dwordx4 v[120:123], v4, s[16:17] offset:1024 sc1
v_mfma_f32_16x16x32_bf16 v[150:153], a[224:227], v[88:91], v[150:153]
v_max3_u32 v6, v6, v88, v89
v_max3_u32 v6, v6, v90, v91
v_mfma_f32_16x16x32_bf16 v[154:157], a[228:231], v[88:91], v[154:157]
v_mfma_f32_16x16x32_bf16 v[158:161], a[232:235], v[88:91], v[158:161]
v_mfma_f32_16x16x32_bf16 v[162:165], a[236:239], v[88:91], v[162:165]
s_waitcnt vmcnt(7)
v_mfma_f32_16x16x32_bf16 v[146:149], a[240:243], v[92:95], v[146:149]
global_load_dwordx4 v[124:127], v4, s[16:17] offset:2048 sc1
v_mfma_f32_16x16x32_bf16 v[150:153], a[244:247], v[92:95], v[150:153]
v_max3_u32 v6, v6, v92, v93
v_max3_u32 v6, v6, v94, v95
v_mfma_f32_16x16x32_bf16 v[154:157], a[248:251], v[92:95], v[154:157]
v_mfma_f32_16x16x32_bf16 v[158:161], a[252:255], v[92:95], v[158:161]
v_mfma_f32_16x16x32_bf16 v[162:165], v[232:235], v[92:95], v[162:165]
s_waitcnt vmcnt(7)
v_mfma_f32_16x16x32_bf16 v[146:149], v[236:239], v[96:99], v[146:149]
global_load_dwordx4 v[128:131], v4, s[16:17] offset:3072 sc1
v_mfma_f32_16x16x32_bf16 v[150:153], v[240:243], v[96:99], v[150:153]
v_max3_u32 v6, v6, v96, v97
v_max3_u32 v6, v6, v98, v99
v_mfma_f32_16x16x32_bf16 v[154:157], v[244:247], v[96:99], v[154:157]
v_mfma_f32_16x16x32_bf16 v[158:161], v[248:251], v[96:99], v[158:161]
v_cmp_eq_u32 vcc, -1, v6
v_mov_b32 v6, 0
v_mfma_f32_16x16x32_bf16 v[162:165], v[252:255], v[96:99], v[162:165]
s_cbranch_vccnz Lst_40_%=
Lsr_41_%=:
s_waitcnt vmcnt(7) lgkmcnt(4)
v_mfma_f32_16x16x32_bf16 v[166:169], a[0:3], v[100:103], v[16:19]
s_add_u32 s16, s16, 0x2000
s_addc_u32 s17, s17, 0
global_load_dwordx4 v[132:135], v4, s[16:17] offset:-4096 sc1
s_waitcnt lgkmcnt(3)
v_mfma_f32_16x16x32_bf16 v[170:173], a[4:7], v[100:103], v[20:23]
v_max3_u32 v6, v6, v100, v101
v_max3_u32 v6, v6, v102, v103
s_waitcnt lgkmcnt(2)
v_mfma_f32_16x16x32_bf16 v[174:177], a[8:11], v[100:103], v[24:27]
ds_read_b32 v143, v211 offset:196
s_waitcnt lgkmcnt(2)
v_mfma_f32_16x16x32_bf16 v[178:181], a[12:15], v[100:103], v[28:31]
ds_read_b128 v[186:189], v9 offset:5120
s_waitcnt lgkmcnt(2)
v_mfma_f32_16x16x32_bf16 v[182:185], a[16:19], v[100:103], v[32:35]
ds_read_b128 v[190:193], v9 offset:6144
s_waitcnt vmcnt(7)
v_mfma_f32_16x16x32_bf16 v[166:169], a[20:23], v[104:107], v[166:169]
global_load_dwordx4 v[136:139], v4, s[16:17] offset:-3072 sc1
v_mfma_f32_16x16x32_bf16 v[170:173], a[24:27], v[104:107], v[170:173]
v_max3_u32 v6, v6, v104, v105
v_max3_u32 v6, v6, v106, v107
v_mfma_f32_16x16x32_bf16 v[174:177], a[28:31], v[104:107], v[174:177]
ds_write_b128 v5, v[146:149] offset:0
v_mfma_f32_16x16x32_bf16 v[178:181], a[32:35], v[104:107], v[178:181]
ds_write_b128 v5, v[150:153] offset:1024
v_mfma_f32_16x16x32_bf16 v[182:185], a[36:39], v[104:107], v[182:185]
ds_write_b128 v5, v[154:157] offset:2048
s_waitcnt vmcnt(7)
v_mfma_f32_16x16x32_bf16 v[166:169], a[40:43], v[108:111], v[166:169]
global_load_dwordx4 v[36:39], v4, s[16:17] offset:-2048 sc1
v_mfma_f32_16x16x32_bf16 v[170:173], a[44:47], v[108:111], v[170:173]
v_max3_u32 v6, v6, v108, v109
v_max3_u32 v6, v6, v110, v111
v_mfma_f32_16x16x32_bf16 v[174:177], a[48:51], v[108:111], v[174:177]
ds_write_b128 v5, v[158:161] offset:3072
v_mfma_f32_16x16x32_bf16 v[178:181], a[52:55], v[108:111], v[178:181]
ds_write_b128 v5, v[162:165] offset:4096
v_mfma_f32_16x16x32_bf16 v[182:185], a[56:59], v[108:111], v[182:185]
ds_write_b32 v210, v211 offset:200
s_waitcnt vmcnt(7)
v_mfma_f32_16x16x32_bf16 v[166:169], a[60:63], v[112:115], v[166:169]
global_load_dwordx4 v[40:43], v4, s[16:17] offset:-1024 sc1
v_mfma_f32_16x16x32_bf16 v[170:173], a[64:67], v[112:115], v[170:173]
v_max3_u32 v6, v6, v112, v113
v_max3_u32 v6, v6, v114, v115
v_mfma_f32_16x16x32_bf16 v[174:177], a[68:71], v[112:115], v[174:177]
ds_read_b128 v[194:197], v9 offset:7168
v_mfma_f32_16x16x32_bf16 v[178:181], a[72:75], v[112:115], v[178:181]
ds_read_b128 v[198:201], v9 offset:8192
v_mfma_f32_16x16x32_bf16 v[182:185], a[76:79], v[112:115], v[182:185]
ds_read_b128 v[202:205], v9 offset:9216
s_waitcnt vmcnt(7)
v_mfma_f32_16x16x32_bf16 v[166:169], a[80:83], v[116:119], v[166:169]
global_load_dwordx4 v[44:47], v4, s[16:17] offset:0 sc1
v_mfma_f32_16x16x32_bf16 v[170:173], a[84:87], v[116:119], v[170:173]
v_max3_u32 v6, v6, v116, v117
v_max3_u32 v6, v6, v118, v119
v_mfma_f32_16x16x32_bf16 v[174:177], a[88:91], v[116:119], v[174:177]
s_waitcnt lgkmcnt(11)
v_cmp_eq_u32 s[46:47], 0, v143
s_nop 0
s_cmp_lg_u64 s[46:47], 0
s_cbranch_scc1 Lfs_42_%=
Lfr_43_%=:
v_mfma_f32_16x16x32_bf16 v[178:181], a[92:95], v[116:119], v[178:181]
v_max_u32 v7, v208, v209
v_cmp_eq_u32 s[46:47], -1, v7
s_nop 0
s_cmp_lg_u64 s[46:47], 0
s_cbranch_scc1 Lss_32_%=
Lsq_33_%=:
v_add_f32 v140, v208, v209
v_mov_b32 v7, v140
s_nop 1
v_permlane16_swap_b32 v140, v7
v_add_f32 v140, v140, v7
v_mfma_f32_16x16x32_bf16 v[182:185], a[96:99], v[116:119], v[182:185]
v_mov_b32 v7, v140
s_nop 1
v_permlane32_swap_b32 v140, v7
v_add_f32 v140, v140, v7
v_mov_b32 v7, 0x3727c5ac
v_fmac_f32 v7, 0x3acccccd, v140
v_rsq_f32 v140, v7
s_waitcnt vmcnt(7)
v_mfma_f32_16x16x32_bf16 v[166:169], a[100:103], v[120:123], v[166:169]
global_load_dwordx4 v[48:51], v14, s[16:17] offset:0 sc1
v_mfma_f32_16x16x32_bf16 v[170:173], a[104:107], v[120:123], v[170:173]
v_max3_u32 v6, v6, v120, v121
v_max3_u32 v6, v6, v122, v123
v_mfma_f32_16x16x32_bf16 v[174:177], a[108:111], v[120:123], v[174:177]
ds_read_b32 v144, v211 offset:200
v_mfma_f32_16x16x32_bf16 v[178:181], a[112:115], v[120:123], v[178:181]
ds_read_b128 v[52:55], v9 offset:10240
v_mfma_f32_16x16x32_bf16 v[182:185], a[116:119], v[120:123], v[182:185]
ds_read_b128 v[56:59], v9 offset:11264
s_waitcnt vmcnt(7)
v_mfma_f32_16x16x32_bf16 v[166:169], a[120:123], v[124:127], v[166:169]
ds_read_b128 v[60:63], v9 offset:12288
v_mfma_f32_16x16x32_bf16 v[170:173], a[124:127], v[124:127], v[170:173]
v_max3_u32 v6, v6, v124, v125
v_max3_u32 v6, v6, v126, v127
v_mfma_f32_16x16x32_bf16 v[174:177], a[128:131], v[124:127], v[174:177]
ds_read_b128 v[64:67], v9 offset:13312
v_mfma_f32_16x16x32_bf16 v[178:181], a[132:135], v[124:127], v[178:181]
ds_read_b128 v[68:71], v9 offset:14336
v_mfma_f32_16x16x32_bf16 v[182:185], a[136:139], v[124:127], v[182:185]
s_waitcnt lgkmcnt(5)
v_cmp_eq_u32 s[46:47], 0, v144
s_nop 0
s_cmp_lg_u64 s[46:47], 0
s_cbranch_scc1 Lfs_44_%=
Lfr_45_%=:
s_waitcnt vmcnt(6)
v_mfma_f32_16x16x32_bf16 v[166:169], a[140:143], v[128:131], v[166:169]
s_waitcnt lgkmcnt(4)
v_add_f32 v186, v186, v52
v_add_f32 v187, v187, v53
v_mfma_f32_16x16x32_bf16 v[170:173], a[144:147], v[128:131], v[170:173]
v_max3_u32 v6, v6, v128, v129
v_max3_u32 v6, v6, v130, v131
v_mfma_f32_16x16x32_bf16 v[174:177], a[148:151], v[128:131], v[174:177]
v_add_f32 v188, v188, v54
v_add_f32 v189, v189, v55
v_mfma_f32_16x16x32_bf16 v[178:181], a[152:155], v[128:131], v[178:181]
s_waitcnt lgkmcnt(3)
v_add_f32 v190, v190, v56
v_add_f32 v191, v191, v57
v_mfma_f32_16x16x32_bf16 v[182:185], a[156:159], v[128:131], v[182:185]
v_add_f32 v192, v192, v58
v_add_f32 v193, v193, v59
s_waitcnt vmcnt(5)
v_mfma_f32_16x16x32_bf16 v[166:169], a[160:163], v[132:135], v[166:169]
s_waitcnt lgkmcnt(2)
v_add_f32 v194, v194, v60
v_add_f32 v195, v195, v61
v_mfma_f32_16x16x32_bf16 v[170:173], a[164:167], v[132:135], v[170:173]
v_max3_u32 v6, v6, v132, v133
v_max3_u32 v6, v6, v134, v135
v_mfma_f32_16x16x32_bf16 v[174:177], a[168:171], v[132:135], v[174:177]
v_add_f32 v196, v196, v62
v_add_f32 v197, v197, v63
v_mfma_f32_16x16x32_bf16 v[178:181], a[172:175], v[132:135], v[178:181]
s_waitcnt lgkmcnt(1)
v_add_f32 v198, v198, v64
v_add_f32 v199, v199, v65
v_mfma_f32_16x16x32_bf16 v[182:185], a[176:179], v[132:135], v[182:185]
v_add_f32 v200, v200, v66
v_add_f32 v201, v201, v67
s_waitcnt vmcnt(4)
v_mfma_f32_16x16x32_bf16 v[166:169], a[180:183], v[136:139], v[166:169]
s_waitcnt lgkmcnt(0)
v_add_f32 v202, v202, v68
v_add_f32 v203, v203, v69
v_mfma_f32_16x16x32_bf16 v[170:173], a[184:187], v[136:139], v[170:173]
v_max3_u32 v6, v6, v136, v137
v_max3_u32 v6, v6, v138, v139
v_mfma_f32_16x16x32_bf16 v[174:177], a[188:191], v[136:139], v[174:177]
v_add_f32 v204, v204, v70
v_add_f32 v205, v205, v71
v_mfma_f32_16x16x32_bf16 v[178:181], a[192:195], v[136:139], v[178:181]
v_fmac_f32 v212, v186, v140
v_fmac_f32 v213, v187, v140
v_mfma_f32_16x16x32_bf16 v[182:185], a[196:199], v[136:139], v[182:185]
)ASM"
R"ASM(v_fmac_f32 v214, v188, v140
v_fmac_f32 v215, v189, v140
s_waitcnt vmcnt(3)
v_mfma_f32_16x16x32_bf16 v[166:169], a[200:203], v[36:39], v[166:169]
v_fmac_f32 v216, v190, v140
v_fmac_f32 v217, v191, v140
v_mfma_f32_16x16x32_bf16 v[170:173], a[204:207], v[36:39], v[170:173]
v_max3_u32 v6, v6, v36, v37
v_max3_u32 v6, v6, v38, v39
v_mfma_f32_16x16x32_bf16 v[174:177], a[208:211], v[36:39], v[174:177]
v_fmac_f32 v218, v192, v140
v_fmac_f32 v219, v193, v140
v_mfma_f32_16x16x32_bf16 v[178:181], a[212:215], v[36:39], v[178:181]
v_fmac_f32 v220, v194, v140
v_fmac_f32 v221, v195, v140
v_mfma_f32_16x16x32_bf16 v[182:185], a[216:219], v[36:39], v[182:185]
v_fmac_f32 v222, v196, v140
v_fmac_f32 v223, v197, v140
s_waitcnt vmcnt(2)
v_mfma_f32_16x16x32_bf16 v[166:169], a[220:223], v[40:43], v[166:169]
v_fmac_f32 v224, v198, v140
v_fmac_f32 v225, v199, v140
v_mfma_f32_16x16x32_bf16 v[170:173], a[224:227], v[40:43], v[170:173]
v_max3_u32 v6, v6, v40, v41
v_max3_u32 v6, v6, v42, v43
v_mfma_f32_16x16x32_bf16 v[174:177], a[228:231], v[40:43], v[174:177]
v_fmac_f32 v226, v200, v140
v_fmac_f32 v227, v201, v140
v_mfma_f32_16x16x32_bf16 v[178:181], a[232:235], v[40:43], v[178:181]
v_fmac_f32 v228, v202, v140
v_fmac_f32 v229, v203, v140
v_mfma_f32_16x16x32_bf16 v[182:185], a[236:239], v[40:43], v[182:185]
v_fmac_f32 v230, v204, v140
v_fmac_f32 v231, v205, v140
s_waitcnt vmcnt(1)
v_mfma_f32_16x16x32_bf16 v[166:169], a[240:243], v[44:47], v[166:169]
v_mfma_f32_16x16x32_bf16 v[170:173], a[244:247], v[44:47], v[170:173]
v_max3_u32 v6, v6, v44, v45
v_max3_u32 v6, v6, v46, v47
v_mfma_f32_16x16x32_bf16 v[174:177], a[248:251], v[44:47], v[174:177]
v_mfma_f32_16x16x32_bf16 v[178:181], a[252:255], v[44:47], v[178:181]
v_mfma_f32_16x16x32_bf16 v[182:185], v[232:235], v[44:47], v[182:185]
s_waitcnt vmcnt(0)
v_mfma_f32_16x16x32_bf16 v[166:169], v[236:239], v[48:51], v[166:169]
v_mfma_f32_16x16x32_bf16 v[170:173], v[240:243], v[48:51], v[170:173]
v_max3_u32 v6, v6, v48, v49
v_max3_u32 v6, v6, v50, v51
v_mfma_f32_16x16x32_bf16 v[174:177], v[244:247], v[48:51], v[174:177]
v_mfma_f32_16x16x32_bf16 v[178:181], v[248:251], v[48:51], v[178:181]
v_cmp_eq_u32 vcc, -1, v6
v_mov_b32 v6, 0
v_mfma_f32_16x16x32_bf16 v[182:185], v[252:255], v[48:51], v[182:185]
s_cbranch_vccnz Lst_46_%=
Lsr_47_%=:
s_nop 6
v_pk_fma_f32 v[228:229], v[182:183], v[140:141], v[228:229] op_sel_hi:[1,0,1]
v_cvt_pk_bf16_f32 v44, v228, v229
v_mov_b32 v46, v44
v_pk_fma_f32 v[230:231], v[184:185], v[140:141], v[230:231] op_sel_hi:[1,0,1]
v_cvt_pk_bf16_f32 v45, v230, v231
v_mov_b32 v47, v45
v_pk_fma_f32 v[212:213], v[166:167], v[140:141], v[212:213] op_sel_hi:[1,0,1]
v_cvt_pk_bf16_f32 v36, v212, v213
v_pk_fma_f32 v[214:215], v[168:169], v[140:141], v[214:215] op_sel_hi:[1,0,1]
v_cvt_pk_bf16_f32 v37, v214, v215
v_permlane16_swap_b32 v44, v46
v_permlane16_swap_b32 v45, v47
v_pk_fma_f32 v[216:217], v[170:171], v[140:141], v[216:217] op_sel_hi:[1,0,1]
v_cvt_pk_bf16_f32 v38, v216, v217
v_pk_fma_f32 v[218:219], v[172:173], v[140:141], v[218:219] op_sel_hi:[1,0,1]
v_cvt_pk_bf16_f32 v39, v218, v219
s_mov_b32 exec_lo, 0xffff
s_mov_b32 exec_hi, 0xffff
global_store_dwordx4 v8, v[44:47], s[24:25] offset:128 
s_mov_b64 exec, -1
v_pk_fma_f32 v[220:221], v[174:175], v[140:141], v[220:221] op_sel_hi:[1,0,1]
v_cvt_pk_bf16_f32 v40, v220, v221
v_permlane16_swap_b32 v36, v38
v_permlane16_swap_b32 v37, v39
v_pk_fma_f32 v[222:223], v[176:177], v[140:141], v[222:223] op_sel_hi:[1,0,1]
v_cvt_pk_bf16_f32 v41, v222, v223
global_store_dwordx4 v1, v[36:39], s[24:25]
v_pk_fma_f32 v[224:225], v[178:179], v[140:141], v[224:225] op_sel_hi:[1,0,1]
v_cvt_pk_bf16_f32 v42, v224, v225
v_pk_fma_f32 v[226:227], v[180:181], v[140:141], v[226:227] op_sel_hi:[1,0,1]
v_cvt_pk_bf16_f32 v43, v226, v227
s_nop 0
v_permlane16_swap_b32 v40, v42
v_permlane16_swap_b32 v41, v43
global_store_dwordx4 v1, v[40:43], s[24:25] offset:64 
s_branch Lend_48_%=
s_nop 2
; ==== out of line: waits and repeats of the hand-offs
Lcs_1_%=:
ds_read_b32 v7, v202
ds_read_b128 v[146:149], v206 offset:0
ds_read_b128 v[150:153], v206 offset:1024
ds_read_b128 v[154:157], v206 offset:2048
ds_read_b128 v[158:161], v206 offset:3072
ds_read_b128 v[162:165], v206 offset:4096
ds_read_b128 v[166:169], v206 offset:5120
s_waitcnt lgkmcnt(0)
v_cmp_eq_u32 vcc, 0, v7
s_sub_u32 s30, s30, 1
s_cmp_gt_i32 s30, 0
s_cselect_b64 vcc, vcc, 0
s_cbranch_vccnz Lcs_1_%=
s_branch Lcr_2_%=
Lfs_5_%=:
global_load_dword v213, v212, s[20:21] sc1
s_waitcnt vmcnt(0)
v_cmp_ne_u32 vcc, 16, v213
s_sub_u32 s49, s49, 1
s_cmp_gt_i32 s49, 0
s_cselect_b64 vcc, vcc, 0
s_cbranch_vccnz Lfs_5_%=
s_branch Lfr_6_%=
Lcs_7_%=:
ds_read_b32 v7, v202 offset:4
ds_read_b128 v[116:119], v215
s_waitcnt lgkmcnt(0)
v_cmp_eq_u32 vcc, 0, v7
s_sub_u32 s30, s30, 1
s_cmp_gt_i32 s30, 0
s_cselect_b64 vcc, vcc, 0
s_cbranch_vccnz Lcs_7_%=
s_branch Lcr_8_%=
Lg_9_%=:
global_load_dword v7, v207, s[20:21] sc1
global_load_dwordx4 v[34:37], v4, s[10:11] offset:-2048 sc1
global_load_dwordx4 v[38:41], v4, s[10:11] offset:-1024 sc1
global_load_dwordx4 v[42:45], v4, s[10:11] offset:0 sc1
s_waitcnt vmcnt(0)
v_max3_u32 v7, v7, v34, v35
v_max3_u32 v7, v7, v36, v37
v_max3_u32 v7, v7, v38, v39
v_max3_u32 v7, v7, v40, v41
v_max3_u32 v7, v7, v42, v43
v_max3_u32 v7, v7, v44, v45
v_cmp_eq_u32 vcc, -1, v7
s_sub_u32 s49, s49, 1
s_cmp_gt_i32 s49, 0
s_cselect_b64 vcc, vcc, 0
s_cbranch_vccnz Lg_9_%=
s_branch Lgd_10_%=
Lst_11_%=:
global_load_dwordx4 v[34:37], v4, s[10:11] offset:-2048 sc1
global_load_dwordx4 v[38:41], v4, s[10:11] offset:-1024 sc1
global_load_dwordx4 v[42:45], v4, s[10:11] offset:0 sc1
global_load_dwordx4 v[46:49], v4, s[10:11] offset:1024 sc1
global_load_dwordx4 v[50:53], v4, s[10:11] offset:2048 sc1
s_waitcnt vmcnt(0)
v_mov_b32 v6, 0
v_max3_u32 v6, v6, v34, v35
v_max3_u32 v6, v6, v36, v37
v_max3_u32 v6, v6, v38, v39
v_max3_u32 v6, v6, v40, v41
v_max3_u32 v6, v6, v42, v43
v_max3_u32 v6, v6, v44, v45
v_max3_u32 v6, v6, v46, v47
v_max3_u32 v6, v6, v48, v49
v_max3_u32 v6, v6, v50, v51
v_max3_u32 v6, v6, v52, v53
v_cmp_eq_u32 vcc, -1, v6
s_sub_u32 s49, s49, 1
v_mov_b32 v6, 0
s_cbranch_vccz Lsx_49_%=
s_cmp_gt_i32 s49, 0
s_cbranch_scc1 Lst_11_%=
Lsx_49_%=:
v_mfma_f32_16x16x32_bf16 v[114:117], a[176:179], v[34:37], 0
v_mfma_f32_16x16x32_bf16 v[118:121], a[180:183], v[34:37], 0
v_mfma_f32_16x16x32_bf16 v[122:125], a[184:187], v[34:37], 0
v_mfma_f32_16x16x32_bf16 v[126:129], a[188:191], v[34:37], 0
v_mfma_f32_16x16x32_bf16 v[130:133], a[192:195], v[34:37], 0
v_mfma_f32_16x16x32_bf16 v[134:137], a[196:199], v[34:37], 0
v_mfma_f32_16x16x32_bf16 v[114:117], a[200:203], v[38:41], v[114:117]
v_mfma_f32_16x16x32_bf16 v[118:121], a[204:207], v[38:41], v[118:121]
v_mfma_f32_16x16x32_bf16 v[122:125], a[208:211], v[38:41], v[122:125]
v_mfma_f32_16x16x32_bf16 v[126:129], a[212:215], v[38:41], v[126:129]
v_mfma_f32_16x16x32_bf16 v[130:133], a[216:219], v[38:41], v[130:133]
v_mfma_f32_16x16x32_bf16 v[134:137], a[220:223], v[38:41], v[134:137]
v_mfma_f32_16x16x32_bf16 v[114:117], a[224:227], v[42:45], v[114:117]
v_mfma_f32_16x16x32_bf16 v[118:121], a[228:231], v[42:45], v[118:121]
v_mfma_f32_16x16x32_bf16 v[122:125], a[232:235], v[42:45], v[122:125]
v_mfma_f32_16x16x32_bf16 v[126:129], a[236:239], v[42:45], v[126:129]
v_mfma_f32_16x16x32_bf16 v[130:133], a[240:243], v[42:45], v[130:133]
v_mfma_f32_16x16x32_bf16 v[134:137], a[244:247], v[42:45], v[134:137]
v_mfma_f32_16x16x32_bf16 v[114:117], a[248:251], v[46:49], v[114:117]
v_mfma_f32_16x16x32_bf16 v[118:121], a[252:255], v[46:49], v[118:121]
v_mfma_f32_16x16x32_bf16 v[122:125], v[216:219], v[46:49], v[122:125]
v_mfma_f32_16x16x32_bf16 v[126:129], v[220:223], v[46:49], v[126:129]
v_mfma_f32_16x16x32_bf16 v[130:133], v[224:227], v[46:49], v[130:133]
v_mfma_f32_16x16x32_bf16 v[134:137], v[228:231], v[46:49], v[134:137]
v_mfma_f32_16x16x32_bf16 v[114:117], v[232:235], v[50:53], v[114:117]
v_mfma_f32_16x16x32_bf16 v[118:121], v[236:239], v[50:53], v[118:121]
v_mfma_f32_16x16x32_bf16 v[122:125], v[240:243], v[50:53], v[122:125]
v_mfma_f32_16x16x32_bf16 v[126:129], v[244:247], v[50:53], v[126:129]
v_mfma_f32_16x16x32_bf16 v[130:133], v[248:251], v[50:53], v[130:133]
v_mfma_f32_16x16x32_bf16 v[134:137], v[252:255], v[50:53], v[134:137]
)ASM"
R"ASM(s_nop 7
s_branch Lsr_12_%=
Lst_13_%=:
global_load_dwordx4 v[54:57], v4, s[12:13] offset:-2048 sc1
global_load_dwordx4 v[58:61], v4, s[12:13] offset:-1024 sc1
global_load_dwordx4 v[62:65], v4, s[12:13] offset:0 sc1
global_load_dwordx4 v[66:69], v4, s[12:13] offset:1024 sc1
global_load_dwordx4 v[70:73], v4, s[12:13] offset:2048 sc1
s_waitcnt vmcnt(0)
v_mov_b32 v6, 0
v_max3_u32 v6, v6, v54, v55
v_max3_u32 v6, v6, v56, v57
v_max3_u32 v6, v6, v58, v59
v_max3_u32 v6, v6, v60, v61
v_max3_u32 v6, v6, v62, v63
v_max3_u32 v6, v6, v64, v65
v_max3_u32 v6, v6, v66, v67
v_max3_u32 v6, v6, v68, v69
v_max3_u32 v6, v6, v70, v71
v_max3_u32 v6, v6, v72, v73
v_cmp_eq_u32 vcc, -1, v6
s_sub_u32 s49, s49, 1
v_mov_b32 v6, 0
s_cbranch_vccz Lsx_50_%=
s_cmp_gt_i32 s49, 0
s_cbranch_scc1 Lst_13_%=
Lsx_50_%=:
v_mfma_f32_16x16x32_bf16 v[138:141], a[176:179], v[54:57], 0
v_mfma_f32_16x16x32_bf16 v[142:145], a[180:183], v[54:57], 0
v_mfma_f32_16x16x32_bf16 v[146:149], a[184:187], v[54:57], 0
v_mfma_f32_16x16x32_bf16 v[150:153], a[188:191], v[54:57], 0
v_mfma_f32_16x16x32_bf16 v[154:157], a[192:195], v[54:57], 0
v_mfma_f32_16x16x32_bf16 v[158:161], a[196:199], v[54:57], 0
v_mfma_f32_16x16x32_bf16 v[138:141], a[200:203], v[58:61], v[138:141]
v_mfma_f32_16x16x32_bf16 v[142:145], a[204:207], v[58:61], v[142:145]
v_mfma_f32_16x16x32_bf16 v[146:149], a[208:211], v[58:61], v[146:149]
v_mfma_f32_16x16x32_bf16 v[150:153], a[212:215], v[58:61], v[150:153]
v_mfma_f32_16x16x32_bf16 v[154:157], a[216:219], v[58:61], v[154:157]
v_mfma_f32_16x16x32_bf16 v[158:161], a[220:223], v[58:61], v[158:161]
v_mfma_f32_16x16x32_bf16 v[138:141], a[224:227], v[62:65], v[138:141]
v_mfma_f32_16x16x32_bf16 v[142:145], a[228:231], v[62:65], v[142:145]
v_mfma_f32_16x16x32_bf16 v[146:149], a[232:235], v[62:65], v[146:149]
v_mfma_f32_16x16x32_bf16 v[150:153], a[236:239], v[62:65], v[150:153]
v_mfma_f32_16x16x32_bf16 v[154:157], a[240:243], v[62:65], v[154:157]
v_mfma_f32_16x16x32_bf16 v[158:161], a[244:247], v[62:65], v[158:161]
v_mfma_f32_16x16x32_bf16 v[138:141], a[248:251], v[66:69], v[138:141]
v_mfma_f32_16x16x32_bf16 v[142:145], a[252:255], v[66:69], v[142:145]
v_mfma_f32_16x16x32_bf16 v[146:149], v[216:219], v[66:69], v[146:149]
v_mfma_f32_16x16x32_bf16 v[150:153], v[220:223], v[66:69], v[150:153]
v_mfma_f32_16x16x32_bf16 v[154:157], v[224:227], v[66:69], v[154:157]
v_mfma_f32_16x16x32_bf16 v[158:161], v[228:231], v[66:69], v[158:161]
v_mfma_f32_16x16x32_bf16 v[138:141], v[232:235], v[70:73], v[138:141]
v_mfma_f32_16x16x32_bf16 v[142:145], v[236:239], v[70:73], v[142:145]
v_mfma_f32_16x16x32_bf16 v[146:149], v[240:243], v[70:73], v[146:149]
v_mfma_f32_16x16x32_bf16 v[150:153], v[244:247], v[70:73], v[150:153]
v_mfma_f32_16x16x32_bf16 v[154:157], v[248:251], v[70:73], v[154:157]
v_mfma_f32_16x16x32_bf16 v[158:161], v[252:255], v[70:73], v[158:161]
s_nop 7
s_branch Lsr_14_%=
Lfs_15_%=:
ds_read_b32 v30, v29 offset:192
ds_read_b128 v[186:189], v13 offset:0
ds_read_b128 v[190:193], v13 offset:1024
ds_read_b128 v[194:197], v13 offset:2048
ds_read_b128 v[198:201], v13 offset:3072
ds_read_b128 v[202:205], v13 offset:4096
ds_read_b32 v21, v24 offset:0
s_waitcnt lgkmcnt(0)
v_cmp_eq_u32 s[46:47], 0, v30
s_sub_u32 s31, s31, 1
s_cmp_gt_i32 s31, 0
s_cselect_b64 s[46:47], s[46:47], 0
s_cmp_lg_u64 s[46:47], 0
s_cbranch_scc1 Lfs_15_%=
s_branch Lfr_16_%=
Lst_17_%=:
global_load_dwordx4 v[74:77], v4, s[14:15] offset:-2048 sc1
global_load_dwordx4 v[78:81], v4, s[14:15] offset:-1024 sc1
global_load_dwordx4 v[82:85], v4, s[14:15] offset:0 sc1
global_load_dwordx4 v[86:89], v4, s[14:15] offset:1024 sc1
global_load_dwordx4 v[90:93], v4, s[14:15] offset:2048 sc1
s_waitcnt vmcnt(0)
v_mov_b32 v6, 0
v_max3_u32 v6, v6, v74, v75
v_max3_u32 v6, v6, v76, v77
v_max3_u32 v6, v6, v78, v79
v_max3_u32 v6, v6, v80, v81
v_max3_u32 v6, v6, v82, v83
v_max3_u32 v6, v6, v84, v85
v_max3_u32 v6, v6, v86, v87
v_max3_u32 v6, v6, v88, v89
v_max3_u32 v6, v6, v90, v91
v_max3_u32 v6, v6, v92, v93
v_cmp_eq_u32 vcc, -1, v6
s_sub_u32 s49, s49, 1
v_mov_b32 v6, 0
s_cbranch_vccz Lsx_51_%=
s_cmp_gt_i32 s49, 0
s_cbranch_scc1 Lst_17_%=
Lsx_51_%=:
v_mfma_f32_16x16x32_bf16 v[114:117], a[176:179], v[74:77], 0
v_mfma_f32_16x16x32_bf16 v[118:121], a[180:183], v[74:77], 0
v_mfma_f32_16x16x32_bf16 v[122:125], a[184:187], v[74:77], 0
v_mfma_f32_16x16x32_bf16 v[126:129], a[188:191], v[74:77], 0
v_mfma_f32_16x16x32_bf16 v[130:133], a[192:195], v[74:77], 0
v_mfma_f32_16x16x32_bf16 v[134:137], a[196:199], v[74:77], 0
v_mfma_f32_16x16x32_bf16 v[114:117], a[200:203], v[78:81], v[114:117]
v_mfma_f32_16x16x32_bf16 v[118:121], a[204:207], v[78:81], v[118:121]
v_mfma_f32_16x16x32_bf16 v[122:125], a[208:211], v[78:81], v[122:125]
v_mfma_f32_16x16x32_bf16 v[126:129], a[212:215], v[78:81], v[126:129]
v_mfma_f32_16x16x32_bf16 v[130:133], a[216:219], v[78:81], v[130:133]
v_mfma_f32_16x16x32_bf16 v[134:137], a[220:223], v[78:81], v[134:137]
v_mfma_f32_16x16x32_bf16 v[114:117], a[224:227], v[82:85], v[114:117]
v_mfma_f32_16x16x32_bf16 v[118:121], a[228:231], v[82:85], v[118:121]
v_mfma_f32_16x16x32_bf16 v[122:125], a[232:235], v[82:85], v[122:125]
v_mfma_f32_16x16x32_bf16 v[126:129], a[236:239], v[82:85], v[126:129]
v_mfma_f32_16x16x32_bf16 v[130:133], a[240:243], v[82:85], v[130:133]
v_mfma_f32_16x16x32_bf16 v[134:137], a[244:247], v[82:85], v[134:137]
v_mfma_f32_16x16x32_bf16 v[114:117], a[248:251], v[86:89], v[114:117]
v_mfma_f32_16x16x32_bf16 v[118:121], a[252:255], v[86:89], v[118:121]
v_mfma_f32_16x16x32_bf16 v[122:125], v[216:219], v[86:89], v[122:125]
v_mfma_f32_16x16x32_bf16 v[126:129], v[220:223], v[86:89], v[126:129]
v_mfma_f32_16x16x32_bf16 v[130:133], v[224:227], v[86:89], v[130:133]
v_mfma_f32_16x16x32_bf16 v[134:137], v[228:231], v[86:89], v[134:137]
v_mfma_f32_16x16x32_bf16 v[114:117], v[232:235], v[90:93], v[114:117]
v_mfma_f32_16x16x32_bf16 v[118:121], v[236:239], v[90:93], v[118:121]
v_mfma_f32_16x16x32_bf16 v[122:125], v[240:243], v[90:93], v[122:125]
v_mfma_f32_16x16x32_bf16 v[126:129], v[244:247], v[90:93], v[126:129]
v_mfma_f32_16x16x32_bf16 v[130:133], v[248:251], v[90:93], v[130:133]
v_mfma_f32_16x16x32_bf16 v[134:137], v[252:255], v[90:93], v[134:137]
s_nop 7
s_branch Lsr_18_%=
Lfs_19_%=:
ds_read_b32 v31, v29 offset:196
ds_read_b128 v[34:37], v13 offset:5120
ds_read_b128 v[38:41], v13 offset:6144
ds_read_b128 v[42:45], v13 offset:7168
ds_read_b128 v[46:49], v13 offset:8192
ds_read_b128 v[50:53], v13 offset:9216
ds_read_b32 v22, v24 offset:64
s_waitcnt lgkmcnt(0)
v_cmp_eq_u32 s[46:47], 0, v31
s_sub_u32 s31, s31, 1
s_cmp_gt_i32 s31, 0
s_cselect_b64 s[46:47], s[46:47], 0
s_cmp_lg_u64 s[46:47], 0
s_cbranch_scc1 Lfs_19_%=
s_branch Lfr_20_%=
Lfs_21_%=:
ds_read_b32 v32, v29 offset:200
ds_read_b128 v[54:57], v33 offset:0
ds_read_b128 v[58:61], v33 offset:1024
ds_read_b128 v[62:65], v33 offset:2048
ds_read_b128 v[66:69], v33 offset:3072
ds_read_b128 v[70:73], v33 offset:4096
ds_read_b32 v23, v24 offset:128
s_waitcnt lgkmcnt(0)
v_cmp_eq_u32 s[46:47], 0, v32
s_sub_u32 s31, s31, 1
s_cmp_gt_i32 s31, 0
s_cselect_b64 s[46:47], s[46:47], 0
s_cmp_lg_u64 s[46:47], 0
s_cbranch_scc1 Lfs_21_%=
s_branch Lfr_22_%=
Lst_23_%=:
global_load_dwordx4 v[94:97], v4, s[16:17] offset:-2048 sc1
global_load_dwordx4 v[98:101], v4, s[16:17] offset:-1024 sc1
global_load_dwordx4 v[102:105], v4, s[16:17] offset:0 sc1
global_load_dwordx4 v[106:109], v4, s[16:17] offset:1024 sc1
global_load_dwordx4 v[110:113], v4, s[16:17] offset:2048 sc1
s_waitcnt vmcnt(0)
v_mov_b32 v6, 0
v_max3_u32 v6, v6, v94, v95
v_max3_u32 v6, v6, v96, v97
v_max3_u32 v6, v6, v98, v99
v_max3_u32 v6, v6, v100, v101
v_max3_u32 v6, v6, v102, v103
v_max3_u32 v6, v6, v104, v105
v_max3_u32 v6, v6, v106, v107
v_max3_u32 v6, v6, v108, v109
v_max3_u32 v6, v6, v110, v111
v_max3_u32 v6, v6, v112, v113
v_cmp_eq_u32 vcc, -1, v6
s_sub_u32 s49, s49, 1
v_mov_b32 v6, 0
s_cbranch_vccz Lsx_52_%=
s_cmp_gt_i32 s49, 0
s_cbranch_scc1 Lst_23_%=
Lsx_52_%=:
v_mfma_f32_16x16x32_bf16 v[138:141], a[176:179], v[94:97], 0
v_mfma_f32_16x16x32_bf16 v[142:145], a[180:183], v[94:97], v[186:189]
v_mfma_f32_16x16x32_bf16 v[146:149], a[184:187], v[94:97], v[190:193]
v_mfma_f32_16x16x32_bf16 v[150:153], a[188:191], v[94:97], v[194:197]
v_mfma_f32_16x16x32_bf16 v[154:157], a[192:195], v[94:97], v[198:201]
v_mfma_f32_16x16x32_bf16 v[158:161], a[196:199], v[94:97], v[202:205]
v_mfma_f32_16x16x32_bf16 v[138:141], a[200:203], v[98:101], v[138:141]
v_mfma_f32_16x16x32_bf16 v[142:145], a[204:207], v[98:101], v[142:145]
v_mfma_f32_16x16x32_bf16 v[146:149], a[208:211], v[98:101], v[146:149]
v_mfma_f32_16x16x32_bf16 v[150:153], a[212:215], v[98:101], v[150:153]
v_mfma_f32_16x16x32_bf16 v[154:157], a[216:219], v[98:101], v[154:157]
)ASM"
R"ASM(v_mfma_f32_16x16x32_bf16 v[158:161], a[220:223], v[98:101], v[158:161]
v_mfma_f32_16x16x32_bf16 v[138:141], a[224:227], v[102:105], v[138:141]
v_mfma_f32_16x16x32_bf16 v[142:145], a[228:231], v[102:105], v[142:145]
v_mfma_f32_16x16x32_bf16 v[146:149], a[232:235], v[102:105], v[146:149]
v_mfma_f32_16x16x32_bf16 v[150:153], a[236:239], v[102:105], v[150:153]
v_mfma_f32_16x16x32_bf16 v[154:157], a[240:243], v[102:105], v[154:157]
v_mfma_f32_16x16x32_bf16 v[158:161], a[244:247], v[102:105], v[158:161]
v_mfma_f32_16x16x32_bf16 v[138:141], a[248:251], v[106:109], v[138:141]
v_mfma_f32_16x16x32_bf16 v[142:145], a[252:255], v[106:109], v[142:145]
v_mfma_f32_16x16x32_bf16 v[146:149], v[216:219], v[106:109], v[146:149]
v_mfma_f32_16x16x32_bf16 v[150:153], v[220:223], v[106:109], v[150:153]
v_mfma_f32_16x16x32_bf16 v[154:157], v[224:227], v[106:109], v[154:157]
v_mfma_f32_16x16x32_bf16 v[158:161], v[228:231], v[106:109], v[158:161]
v_mfma_f32_16x16x32_bf16 v[138:141], v[232:235], v[110:113], v[138:141]
v_mfma_f32_16x16x32_bf16 v[142:145], v[236:239], v[110:113], v[142:145]
v_mfma_f32_16x16x32_bf16 v[146:149], v[240:243], v[110:113], v[146:149]
v_mfma_f32_16x16x32_bf16 v[150:153], v[244:247], v[110:113], v[150:153]
v_mfma_f32_16x16x32_bf16 v[154:157], v[248:251], v[110:113], v[154:157]
v_mfma_f32_16x16x32_bf16 v[158:161], v[252:255], v[110:113], v[158:161]
s_nop 7
s_branch Lsr_24_%=
Lg_26_%=:
global_load_dword v7, v16, s[20:21] sc1
global_load_dwordx4 a[176:179], v4, s[40:41] offset:-4096 sc1
global_load_dwordx4 a[180:183], v4, s[42:43] offset:-4096 sc1
global_load_dwordx4 a[184:187], v4, s[40:41] offset:-3072 sc1
global_load_dwordx4 a[188:191], v4, s[42:43] offset:-3072 sc1
s_waitcnt vmcnt(0)
v_accvgpr_read_b32 v20, a176
v_accvgpr_read_b32 v21, a177
v_accvgpr_read_b32 v22, a178
v_accvgpr_read_b32 v23, a179
v_max3_u32 v7, v7, v20, v21
v_max3_u32 v7, v7, v22, v23
v_accvgpr_read_b32 v20, a180
v_accvgpr_read_b32 v21, a181
v_accvgpr_read_b32 v22, a182
v_accvgpr_read_b32 v23, a183
v_max3_u32 v7, v7, v20, v21
v_max3_u32 v7, v7, v22, v23
v_accvgpr_read_b32 v20, a184
v_accvgpr_read_b32 v21, a185
v_accvgpr_read_b32 v22, a186
v_accvgpr_read_b32 v23, a187
v_max3_u32 v7, v7, v20, v21
v_max3_u32 v7, v7, v22, v23
v_accvgpr_read_b32 v20, a188
v_accvgpr_read_b32 v21, a189
v_accvgpr_read_b32 v22, a190
v_accvgpr_read_b32 v23, a191
v_max3_u32 v7, v7, v20, v21
v_max3_u32 v7, v7, v22, v23
v_cmp_eq_u32 vcc, -1, v7
s_sub_u32 s49, s49, 1
s_cmp_gt_i32 s49, 0
s_cselect_b64 vcc, vcc, 0
s_cbranch_vccnz Lg_26_%=
s_branch Lgd_27_%=
Lr1_29_%=:
s_waitcnt vmcnt(0) lgkmcnt(0)
s_nop 15
s_branch Lf1_25_%=
Lg_30_%=:
global_load_dword v7, v13, s[20:21] sc1
global_load_dwordx4 v[36:39], v4, s[10:11] offset:-4096 sc1
global_load_dwordx4 v[40:43], v4, s[10:11] offset:-3072 sc1
global_load_dwordx4 v[44:47], v4, s[10:11] offset:-2048 sc1
global_load_dwordx4 v[48:51], v4, s[10:11] offset:-1024 sc1
s_waitcnt vmcnt(0)
v_max3_u32 v7, v7, v36, v37
v_max3_u32 v7, v7, v38, v39
v_max3_u32 v7, v7, v40, v41
v_max3_u32 v7, v7, v42, v43
v_max3_u32 v7, v7, v44, v45
v_max3_u32 v7, v7, v46, v47
v_max3_u32 v7, v7, v48, v49
v_max3_u32 v7, v7, v50, v51
v_cmp_eq_u32 vcc, -1, v7
s_sub_u32 s49, s49, 1
s_cmp_gt_i32 s49, 0
s_cselect_b64 vcc, vcc, 0
s_cbranch_vccnz Lg_30_%=
s_branch Lgd_31_%=
Lss_32_%=:
global_load_dword v208, v15, s[38:39] sc1
global_load_dword v209, v15, s[38:39] offset:64 sc1
s_waitcnt vmcnt(0)
v_max_u32 v7, v208, v209
v_cmp_eq_u32 s[46:47], -1, v7
s_sub_u32 s49, s49, 1
s_cmp_gt_i32 s49, 0
s_cselect_b64 s[46:47], s[46:47], 0
s_cmp_lg_u64 s[46:47], 0
s_cbranch_scc1 Lss_32_%=
s_branch Lsq_33_%=
Lst_34_%=:
s_add_u32 s18, s10, 0xffffe000
s_addc_u32 s19, s11, -1
global_load_dwordx4 v[36:39], v4, s[18:19] offset:-4096 sc1
global_load_dwordx4 v[40:43], v4, s[18:19] offset:-3072 sc1
global_load_dwordx4 v[44:47], v4, s[18:19] offset:-2048 sc1
global_load_dwordx4 v[48:51], v4, s[18:19] offset:-1024 sc1
global_load_dwordx4 v[52:55], v4, s[18:19] offset:0 sc1
global_load_dwordx4 v[56:59], v4, s[18:19] offset:1024 sc1
global_load_dwordx4 v[60:63], v4, s[18:19] offset:2048 sc1
global_load_dwordx4 v[64:67], v4, s[18:19] offset:3072 sc1
global_load_dwordx4 v[68:71], v4, s[10:11] offset:-4096 sc1
global_load_dwordx4 v[72:75], v4, s[10:11] offset:-3072 sc1
global_load_dwordx4 v[76:79], v4, s[10:11] offset:-2048 sc1
global_load_dwordx4 v[80:83], v4, s[10:11] offset:-1024 sc1
global_load_dwordx4 v[84:87], v4, s[10:11] offset:0 sc1
global_load_dwordx4 v[88:91], v14, s[10:11] offset:0 sc1
s_waitcnt vmcnt(0)
v_mov_b32 v6, 0
v_max3_u32 v6, v6, v36, v37
v_max3_u32 v6, v6, v38, v39
v_max3_u32 v6, v6, v40, v41
v_max3_u32 v6, v6, v42, v43
v_max3_u32 v6, v6, v44, v45
v_max3_u32 v6, v6, v46, v47
v_max3_u32 v6, v6, v48, v49
v_max3_u32 v6, v6, v50, v51
v_max3_u32 v6, v6, v52, v53
v_max3_u32 v6, v6, v54, v55
v_max3_u32 v6, v6, v56, v57
v_max3_u32 v6, v6, v58, v59
v_max3_u32 v6, v6, v60, v61
v_max3_u32 v6, v6, v62, v63
v_max3_u32 v6, v6, v64, v65
v_max3_u32 v6, v6, v66, v67
v_max3_u32 v6, v6, v68, v69
v_max3_u32 v6, v6, v70, v71
v_max3_u32 v6, v6, v72, v73
v_max3_u32 v6, v6, v74, v75
v_max3_u32 v6, v6, v76, v77
v_max3_u32 v6, v6, v78, v79
v_max3_u32 v6, v6, v80, v81
v_max3_u32 v6, v6, v82, v83
v_max3_u32 v6, v6, v84, v85
v_max3_u32 v6, v6, v86, v87
v_max3_u32 v6, v6, v88, v89
v_max3_u32 v6, v6, v90, v91
v_cmp_eq_u32 vcc, -1, v6
s_sub_u32 s49, s49, 1
v_mov_b32 v6, 0
s_cbranch_vccz Lsx_53_%=
s_cmp_gt_i32 s49, 0
s_cbranch_scc1 Lst_34_%=
Lsx_53_%=:
v_mfma_f32_16x16x32_bf16 v[146:149], a[0:3], v[36:39], 0
v_mfma_f32_16x16x32_bf16 v[150:153], a[4:7], v[36:39], 0
v_mfma_f32_16x16x32_bf16 v[154:157], a[8:11], v[36:39], 0
v_mfma_f32_16x16x32_bf16 v[158:161], a[12:15], v[36:39], 0
v_mfma_f32_16x16x32_bf16 v[162:165], a[16:19], v[36:39], 0
v_mfma_f32_16x16x32_bf16 v[146:149], a[20:23], v[40:43], v[146:149]
v_mfma_f32_16x16x32_bf16 v[150:153], a[24:27], v[40:43], v[150:153]
v_mfma_f32_16x16x32_bf16 v[154:157], a[28:31], v[40:43], v[154:157]
v_mfma_f32_16x16x32_bf16 v[158:161], a[32:35], v[40:43], v[158:161]
v_mfma_f32_16x16x32_bf16 v[162:165], a[36:39], v[40:43], v[162:165]
v_mfma_f32_16x16x32_bf16 v[146:149], a[40:43], v[44:47], v[146:149]
v_mfma_f32_16x16x32_bf16 v[150:153], a[44:47], v[44:47], v[150:153]
v_mfma_f32_16x16x32_bf16 v[154:157], a[48:51], v[44:47], v[154:157]
v_mfma_f32_16x16x32_bf16 v[158:161], a[52:55], v[44:47], v[158:161]
v_mfma_f32_16x16x32_bf16 v[162:165], a[56:59], v[44:47], v[162:165]
v_mfma_f32_16x16x32_bf16 v[146:149], a[60:63], v[48:51], v[146:149]
v_mfma_f32_16x16x32_bf16 v[150:153], a[64:67], v[48:51], v[150:153]
v_mfma_f32_16x16x32_bf16 v[154:157], a[68:71], v[48:51], v[154:157]
v_mfma_f32_16x16x32_bf16 v[158:161], a[72:75], v[48:51], v[158:161]
v_mfma_f32_16x16x32_bf16 v[162:165], a[76:79], v[48:51], v[162:165]
v_mfma_f32_16x16x32_bf16 v[146:149], a[80:83], v[52:55], v[146:149]
v_mfma_f32_16x16x32_bf16 v[150:153], a[84:87], v[52:55], v[150:153]
v_mfma_f32_16x16x32_bf16 v[154:157], a[88:91], v[52:55], v[154:157]
v_mfma_f32_16x16x32_bf16 v[158:161], a[92:95], v[52:55], v[158:161]
v_mfma_f32_16x16x32_bf16 v[162:165], a[96:99], v[52:55], v[162:165]
v_mfma_f32_16x16x32_bf16 v[146:149], a[100:103], v[56:59], v[146:149]
v_mfma_f32_16x16x32_bf16 v[150:153], a[104:107], v[56:59], v[150:153]
v_mfma_f32_16x16x32_bf16 v[154:157], a[108:111], v[56:59], v[154:157]
v_mfma_f32_16x16x32_bf16 v[158:161], a[112:115], v[56:59], v[158:161]
v_mfma_f32_16x16x32_bf16 v[162:165], a[116:119], v[56:59], v[162:165]
v_mfma_f32_16x16x32_bf16 v[146:149], a[120:123], v[60:63], v[146:149]
v_mfma_f32_16x16x32_bf16 v[150:153], a[124:127], v[60:63], v[150:153]
v_mfma_f32_16x16x32_bf16 v[154:157], a[128:131], v[60:63], v[154:157]
v_mfma_f32_16x16x32_bf16 v[158:161], a[132:135], v[60:63], v[158:161]
v_mfma_f32_16x16x32_bf16 v[162:165], a[136:139], v[60:63], v[162:165]
v_mfma_f32_16x16x32_bf16 v[146:149], a[140:143], v[64:67], v[146:149]
v_mfma_f32_16x16x32_bf16 v[150:153], a[144:147], v[64:67], v[150:153]
v_mfma_f32_16x16x32_bf16 v[154:157], a[148:151], v[64:67], v[154:157]
v_mfma_f32_16x16x32_bf16 v[158:161], a[152:155], v[64:67], v[158:161]
v_mfma_f32_16x16x32_bf16 v[162:165], a[156:159], v[64:67], v[162:165]
v_mfma_f32_16x16x32_bf16 v[146:149], a[160:163], v[68:71], v[146:149]
v_mfma_f32_16x16x32_bf16 v[150:153], a[164:167], v[68:71], v[150:153]
v_mfma_f32_16x16x32_bf16 v[154:157], a[168:171], v[68:71], v[154:157]
v_mfma_f32_16x16x32_bf16 v[158:161], a[172:175], v[68:71], v[158:161]
v_mfma_f32_16x16x32_bf16 v[162:165], a[176:179], v[68:71], v[162:165]
v_mfma_f32_16x16x32_bf16 v[146:149], a[180:183], v[72:75], v[146:149]
v_mfma_f32_16x16x32_bf16 v[150:153], a[184:187], v[72:75], v[150:153]
v_mfma_f32_16x16x32_bf16 v[154:157], a[188:191], v[72:75], v[154:157]
v_mfma_f32_16x16x32_bf16 v[158:161], a[192:195], v[72:75], v[158:161]
v_mfma_f32_16x16x32_bf16 v[162:165], a[196:199], v[72:75], v[162:165]
v_mfma_f32_16x16x32_bf16 v[146:149], a[200:203], v[76:79], v[146:149]
)ASM"
R"ASM(v_mfma_f32_16x16x32_bf16 v[150:153], a[204:207], v[76:79], v[150:153]
v_mfma_f32_16x16x32_bf16 v[154:157], a[208:211], v[76:79], v[154:157]
v_mfma_f32_16x16x32_bf16 v[158:161], a[212:215], v[76:79], v[158:161]
v_mfma_f32_16x16x32_bf16 v[162:165], a[216:219], v[76:79], v[162:165]
v_mfma_f32_16x16x32_bf16 v[146:149], a[220:223], v[80:83], v[146:149]
v_mfma_f32_16x16x32_bf16 v[150:153], a[224:227], v[80:83], v[150:153]
v_mfma_f32_16x16x32_bf16 v[154:157], a[228:231], v[80:83], v[154:157]
v_mfma_f32_16x16x32_bf16 v[158:161], a[232:235], v[80:83], v[158:161]
v_mfma_f32_16x16x32_bf16 v[162:165], a[236:239], v[80:83], v[162:165]
v_mfma_f32_16x16x32_bf16 v[146:149], a[240:243], v[84:87], v[146:149]
v_mfma_f32_16x16x32_bf16 v[150:153], a[244:247], v[84:87], v[150:153]
v_mfma_f32_16x16x32_bf16 v[154:157], a[248:251], v[84:87], v[154:157]
v_mfma_f32_16x16x32_bf16 v[158:161], a[252:255], v[84:87], v[158:161]
v_mfma_f32_16x16x32_bf16 v[162:165], v[232:235], v[84:87], v[162:165]
v_mfma_f32_16x16x32_bf16 v[146:149], v[236:239], v[88:91], v[146:149]
v_mfma_f32_16x16x32_bf16 v[150:153], v[240:243], v[88:91], v[150:153]
v_mfma_f32_16x16x32_bf16 v[154:157], v[244:247], v[88:91], v[154:157]
v_mfma_f32_16x16x32_bf16 v[158:161], v[248:251], v[88:91], v[158:161]
v_mfma_f32_16x16x32_bf16 v[162:165], v[252:255], v[88:91], v[162:165]
s_nop 7
s_branch Lsr_35_%=
Lst_36_%=:
s_add_u32 s18, s12, 0xffffe000
s_addc_u32 s19, s13, -1
global_load_dwordx4 v[92:95], v4, s[18:19] offset:-4096 sc1
global_load_dwordx4 v[96:99], v4, s[18:19] offset:-3072 sc1
global_load_dwordx4 v[100:103], v4, s[18:19] offset:-2048 sc1
global_load_dwordx4 v[104:107], v4, s[18:19] offset:-1024 sc1
global_load_dwordx4 v[108:111], v4, s[18:19] offset:0 sc1
global_load_dwordx4 v[112:115], v4, s[18:19] offset:1024 sc1
global_load_dwordx4 v[116:119], v4, s[18:19] offset:2048 sc1
global_load_dwordx4 v[120:123], v4, s[18:19] offset:3072 sc1
global_load_dwordx4 v[124:127], v4, s[12:13] offset:-4096 sc1
global_load_dwordx4 v[128:131], v4, s[12:13] offset:-3072 sc1
global_load_dwordx4 v[132:135], v4, s[12:13] offset:-2048 sc1
global_load_dwordx4 v[136:139], v4, s[12:13] offset:-1024 sc1
global_load_dwordx4 v[36:39], v4, s[12:13] offset:0 sc1
global_load_dwordx4 v[40:43], v14, s[12:13] offset:0 sc1
s_waitcnt vmcnt(0)
v_mov_b32 v6, 0
v_max3_u32 v6, v6, v92, v93
v_max3_u32 v6, v6, v94, v95
v_max3_u32 v6, v6, v96, v97
v_max3_u32 v6, v6, v98, v99
v_max3_u32 v6, v6, v100, v101
v_max3_u32 v6, v6, v102, v103
v_max3_u32 v6, v6, v104, v105
v_max3_u32 v6, v6, v106, v107
v_max3_u32 v6, v6, v108, v109
v_max3_u32 v6, v6, v110, v111
v_max3_u32 v6, v6, v112, v113
v_max3_u32 v6, v6, v114, v115
v_max3_u32 v6, v6, v116, v117
v_max3_u32 v6, v6, v118, v119
v_max3_u32 v6, v6, v120, v121
v_max3_u32 v6, v6, v122, v123
v_max3_u32 v6, v6, v124, v125
v_max3_u32 v6, v6, v126, v127
v_max3_u32 v6, v6, v128, v129
v_max3_u32 v6, v6, v130, v131
v_max3_u32 v6, v6, v132, v133
v_max3_u32 v6, v6, v134, v135
v_max3_u32 v6, v6, v136, v137
v_max3_u32 v6, v6, v138, v139
v_max3_u32 v6, v6, v36, v37
v_max3_u32 v6, v6, v38, v39
v_max3_u32 v6, v6, v40, v41
v_max3_u32 v6, v6, v42, v43
v_cmp_eq_u32 vcc, -1, v6
s_sub_u32 s49, s49, 1
v_mov_b32 v6, 0
s_cbranch_vccz Lsx_54_%=
s_cmp_gt_i32 s49, 0
s_cbranch_scc1 Lst_36_%=
Lsx_54_%=:
v_mfma_f32_16x16x32_bf16 v[166:169], a[0:3], v[92:95], 0
v_mfma_f32_16x16x32_bf16 v[170:173], a[4:7], v[92:95], 0
v_mfma_f32_16x16x32_bf16 v[174:177], a[8:11], v[92:95], 0
v_mfma_f32_16x16x32_bf16 v[178:181], a[12:15], v[92:95], 0
v_mfma_f32_16x16x32_bf16 v[182:185], a[16:19], v[92:95], 0
v_mfma_f32_16x16x32_bf16 v[166:169], a[20:23], v[96:99], v[166:169]
v_mfma_f32_16x16x32_bf16 v[170:173], a[24:27], v[96:99], v[170:173]
v_mfma_f32_16x16x32_bf16 v[174:177], a[28:31], v[96:99], v[174:177]
v_mfma_f32_16x16x32_bf16 v[178:181], a[32:35], v[96:99], v[178:181]
v_mfma_f32_16x16x32_bf16 v[182:185], a[36:39], v[96:99], v[182:185]
v_mfma_f32_16x16x32_bf16 v[166:169], a[40:43], v[100:103], v[166:169]
v_mfma_f32_16x16x32_bf16 v[170:173], a[44:47], v[100:103], v[170:173]
v_mfma_f32_16x16x32_bf16 v[174:177], a[48:51], v[100:103], v[174:177]
v_mfma_f32_16x16x32_bf16 v[178:181], a[52:55], v[100:103], v[178:181]
v_mfma_f32_16x16x32_bf16 v[182:185], a[56:59], v[100:103], v[182:185]
v_mfma_f32_16x16x32_bf16 v[166:169], a[60:63], v[104:107], v[166:169]
v_mfma_f32_16x16x32_bf16 v[170:173], a[64:67], v[104:107], v[170:173]
v_mfma_f32_16x16x32_bf16 v[174:177], a[68:71], v[104:107], v[174:177]
v_mfma_f32_16x16x32_bf16 v[178:181], a[72:75], v[104:107], v[178:181]
v_mfma_f32_16x16x32_bf16 v[182:185], a[76:79], v[104:107], v[182:185]
v_mfma_f32_16x16x32_bf16 v[166:169], a[80:83], v[108:111], v[166:169]
v_mfma_f32_16x16x32_bf16 v[170:173], a[84:87], v[108:111], v[170:173]
v_mfma_f32_16x16x32_bf16 v[174:177], a[88:91], v[108:111], v[174:177]
v_mfma_f32_16x16x32_bf16 v[178:181], a[92:95], v[108:111], v[178:181]
v_mfma_f32_16x16x32_bf16 v[182:185], a[96:99], v[108:111], v[182:185]
v_mfma_f32_16x16x32_bf16 v[166:169], a[100:103], v[112:115], v[166:169]
v_mfma_f32_16x16x32_bf16 v[170:173], a[104:107], v[112:115], v[170:173]
v_mfma_f32_16x16x32_bf16 v[174:177], a[108:111], v[112:115], v[174:177]
v_mfma_f32_16x16x32_bf16 v[178:181], a[112:115], v[112:115], v[178:181]
v_mfma_f32_16x16x32_bf16 v[182:185], a[116:119], v[112:115], v[182:185]
v_mfma_f32_16x16x32_bf16 v[166:169], a[120:123], v[116:119], v[166:169]
v_mfma_f32_16x16x32_bf16 v[170:173], a[124:127], v[116:119], v[170:173]
v_mfma_f32_16x16x32_bf16 v[174:177], a[128:131], v[116:119], v[174:177]
v_mfma_f32_16x16x32_bf16 v[178:181], a[132:135], v[116:119], v[178:181]
v_mfma_f32_16x16x32_bf16 v[182:185], a[136:139], v[116:119], v[182:185]
v_mfma_f32_16x16x32_bf16 v[166:169], a[140:143], v[120:123], v[166:169]
v_mfma_f32_16x16x32_bf16 v[170:173], a[144:147], v[120:123], v[170:173]
v_mfma_f32_16x16x32_bf16 v[174:177], a[148:151], v[120:123], v[174:177]
v_mfma_f32_16x16x32_bf16 v[178:181], a[152:155], v[120:123], v[178:181]
v_mfma_f32_16x16x32_bf16 v[182:185], a[156:159], v[120:123], v[182:185]
v_mfma_f32_16x16x32_bf16 v[166:169], a[160:163], v[124:127], v[166:169]
v_mfma_f32_16x16x32_bf16 v[170:173], a[164:167], v[124:127], v[170:173]
v_mfma_f32_16x16x32_bf16 v[174:177], a[168:171], v[124:127], v[174:177]
v_mfma_f32_16x16x32_bf16 v[178:181], a[172:175], v[124:127], v[178:181]
v_mfma_f32_16x16x32_bf16 v[182:185], a[176:179], v[124:127], v[182:185]
v_mfma_f32_16x16x32_bf16 v[166:169], a[180:183], v[128:131], v[166:169]
v_mfma_f32_16x16x32_bf16 v[170:173], a[184:187], v[128:131], v[170:173]
v_mfma_f32_16x16x32_bf16 v[174:177], a[188:191], v[128:131], v[174:177]
v_mfma_f32_16x16x32_bf16 v[178:181], a[192:195], v[128:131], v[178:181]
v_mfma_f32_16x16x32_bf16 v[182:185], a[196:199], v[128:131], v[182:185]
v_mfma_f32_16x16x32_bf16 v[166:169], a[200:203], v[132:135], v[166:169]
v_mfma_f32_16x16x32_bf16 v[170:173], a[204:207], v[132:135], v[170:173]
v_mfma_f32_16x16x32_bf16 v[174:177], a[208:211], v[132:135], v[174:177]
v_mfma_f32_16x16x32_bf16 v[178:181], a[212:215], v[132:135], v[178:181]
v_mfma_f32_16x16x32_bf16 v[182:185], a[216:219], v[132:135], v[182:185]
v_mfma_f32_16x16x32_bf16 v[166:169], a[220:223], v[136:139], v[166:169]
v_mfma_f32_16x16x32_bf16 v[170:173], a[224:227], v[136:139], v[170:173]
v_mfma_f32_16x16x32_bf16 v[174:177], a[228:231], v[136:139], v[174:177]
v_mfma_f32_16x16x32_bf16 v[178:181], a[232:235], v[136:139], v[178:181]
v_mfma_f32_16x16x32_bf16 v[182:185], a[236:239], v[136:139], v[182:185]
v_mfma_f32_16x16x32_bf16 v[166:169], a[240:243], v[36:39], v[166:169]
v_mfma_f32_16x16x32_bf16 v[170:173], a[244:247], v[36:39], v[170:173]
v_mfma_f32_16x16x32_bf16 v[174:177], a[248:251], v[36:39], v[174:177]
v_mfma_f32_16x16x32_bf16 v[178:181], a[252:255], v[36:39], v[178:181]
v_mfma_f32_16x16x32_bf16 v[182:185], v[232:235], v[36:39], v[182:185]
v_mfma_f32_16x16x32_bf16 v[166:169], v[236:239], v[40:43], v[166:169]
v_mfma_f32_16x16x32_bf16 v[170:173], v[240:243], v[40:43], v[170:173]
v_mfma_f32_16x16x32_bf16 v[174:177], v[244:247], v[40:43], v[174:177]
v_mfma_f32_16x16x32_bf16 v[178:181], v[248:251], v[40:43], v[178:181]
v_mfma_f32_16x16x32_bf16 v[182:185], v[252:255], v[40:43], v[182:185]
s_nop 7
s_branch Lsr_37_%=
Lfs_38_%=:
ds_read_b32 v142, v211 offset:192
ds_read_b128 v[16:19], v9 offset:0
ds_read_b128 v[20:23], v9 offset:1024
ds_read_b128 v[24:27], v9 offset:2048
ds_read_b128 v[28:31], v9 offset:3072
ds_read_b128 v[32:35], v9 offset:4096
s_waitcnt lgkmcnt(0)
v_cmp_eq_u32 s[46:47], 0, v142
s_sub_u32 s31, s31, 1
s_cmp_gt_i32 s31, 0
s_cselect_b64 s[46:47], s[46:47], 0
s_cmp_lg_u64 s[46:47], 0
s_cbranch_scc1 Lfs_38_%=
s_branch Lfr_39_%=
Lst_40_%=:
s_add_u32 s18, s14, 0xffffe000
s_addc_u32 s19, s15, -1
global_load_dwordx4 v[44:47], v4, s[18:19] offset:-4096 sc1
global_load_dwordx4 v[48:51], v4, s[18:19] offset:-3072 sc1
global_load_dwordx4 v[52:55], v4, s[18:19] offset:-2048 sc1
global_load_dwordx4 v[56:59], v4, s[18:19] offset:-1024 sc1
global_load_dwordx4 v[60:63], v4, s[18:19] offset:0 sc1
global_load_dwordx4 v[64:67], v4, s[18:19] offset:1024 sc1
global_load_dwordx4 v[68:71], v4, s[18:19] offset:2048 sc1
global_load_dwordx4 v[72:75], v4, s[18:19] offset:3072 sc1
global_load_dwordx4 v[76:79], v4, s[14:15] offset:-4096 sc1
global_load_dwordx4 v[80:83], v4, s[14:15] offset:-3072 sc1
global_load_dwordx4 v[84:87], v4, s[14:15] offset:-2048 sc1
global_load_dwordx4 v[88:91], v4, s[14:15] offset:-1024 sc1
global_load_dwordx4 v[92:95], v4, s[14:15] offset:0 sc1
global_load_dwordx4 v[96:99], v14, s[14:15] offset:0 sc1
s_waitcnt vmcnt(0)
v_mov_b32 v6, 0
v_max3_u32 v6, v6, v44, v45
v_max3_u32 v6, v6, v46, v47
v_max3_u32 v6, v6, v48, v49
v_max3_u32 v6, v6, v50, v51
v_max3_u32 v6, v6, v52, v53
v_max3_u32 v6, v6, v54, v55
v_max3_u32 v6, v6, v56, v57
v_max3_u32 v6, v6, v58, v59
v_max3_u32 v6, v6, v60, v61
v_max3_u32 v6, v6, v62, v63
v_max3_u32 v6, v6, v64, v65
v_max3_u32 v6, v6, v66, v67
v_max3_u32 v6, v6, v68, v69
v_max3_u32 v6, v6, v70, v71
v_max3_u32 v6, v6, v72, v73
v_max3_u32 v6, v6, v74, v75
v_max3_u32 v6, v6, v76, v77
v_max3_u32 v6, v6, v78, v79
v_max3_u32 v6, v6, v80, v81
)ASM"
R"ASM(v_max3_u32 v6, v6, v82, v83
v_max3_u32 v6, v6, v84, v85
v_max3_u32 v6, v6, v86, v87
v_max3_u32 v6, v6, v88, v89
v_max3_u32 v6, v6, v90, v91
v_max3_u32 v6, v6, v92, v93
v_max3_u32 v6, v6, v94, v95
v_max3_u32 v6, v6, v96, v97
v_max3_u32 v6, v6, v98, v99
v_cmp_eq_u32 vcc, -1, v6
s_sub_u32 s49, s49, 1
v_mov_b32 v6, 0
s_cbranch_vccz Lsx_55_%=
s_cmp_gt_i32 s49, 0
s_cbranch_scc1 Lst_40_%=
Lsx_55_%=:
v_mfma_f32_16x16x32_bf16 v[146:149], a[0:3], v[44:47], 0
v_mfma_f32_16x16x32_bf16 v[150:153], a[4:7], v[44:47], 0
v_mfma_f32_16x16x32_bf16 v[154:157], a[8:11], v[44:47], 0
v_mfma_f32_16x16x32_bf16 v[158:161], a[12:15], v[44:47], 0
v_mfma_f32_16x16x32_bf16 v[162:165], a[16:19], v[44:47], 0
v_mfma_f32_16x16x32_bf16 v[146:149], a[20:23], v[48:51], v[146:149]
v_mfma_f32_16x16x32_bf16 v[150:153], a[24:27], v[48:51], v[150:153]
v_mfma_f32_16x16x32_bf16 v[154:157], a[28:31], v[48:51], v[154:157]
v_mfma_f32_16x16x32_bf16 v[158:161], a[32:35], v[48:51], v[158:161]
v_mfma_f32_16x16x32_bf16 v[162:165], a[36:39], v[48:51], v[162:165]
v_mfma_f32_16x16x32_bf16 v[146:149], a[40:43], v[52:55], v[146:149]
v_mfma_f32_16x16x32_bf16 v[150:153], a[44:47], v[52:55], v[150:153]
v_mfma_f32_16x16x32_bf16 v[154:157], a[48:51], v[52:55], v[154:157]
v_mfma_f32_16x16x32_bf16 v[158:161], a[52:55], v[52:55], v[158:161]
v_mfma_f32_16x16x32_bf16 v[162:165], a[56:59], v[52:55], v[162:165]
v_mfma_f32_16x16x32_bf16 v[146:149], a[60:63], v[56:59], v[146:149]
v_mfma_f32_16x16x32_bf16 v[150:153], a[64:67], v[56:59], v[150:153]
v_mfma_f32_16x16x32_bf16 v[154:157], a[68:71], v[56:59], v[154:157]
v_mfma_f32_16x16x32_bf16 v[158:161], a[72:75], v[56:59], v[158:161]
v_mfma_f32_16x16x32_bf16 v[162:165], a[76:79], v[56:59], v[162:165]
v_mfma_f32_16x16x32_bf16 v[146:149], a[80:83], v[60:63], v[146:149]
v_mfma_f32_16x16x32_bf16 v[150:153], a[84:87], v[60:63], v[150:153]
v_mfma_f32_16x16x32_bf16 v[154:157], a[88:91], v[60:63], v[154:157]
v_mfma_f32_16x16x32_bf16 v[158:161], a[92:95], v[60:63], v[158:161]
v_mfma_f32_16x16x32_bf16 v[162:165], a[96:99], v[60:63], v[162:165]
v_mfma_f32_16x16x32_bf16 v[146:149], a[100:103], v[64:67], v[146:149]
v_mfma_f32_16x16x32_bf16 v[150:153], a[104:107], v[64:67], v[150:153]
v_mfma_f32_16x16x32_bf16 v[154:157], a[108:111], v[64:67], v[154:157]
v_mfma_f32_16x16x32_bf16 v[158:161], a[112:115], v[64:67], v[158:161]
v_mfma_f32_16x16x32_bf16 v[162:165], a[116:119], v[64:67], v[162:165]
v_mfma_f32_16x16x32_bf16 v[146:149], a[120:123], v[68:71], v[146:149]
v_mfma_f32_16x16x32_bf16 v[150:153], a[124:127], v[68:71], v[150:153]
v_mfma_f32_16x16x32_bf16 v[154:157], a[128:131], v[68:71], v[154:157]
v_mfma_f32_16x16x32_bf16 v[158:161], a[132:135], v[68:71], v[158:161]
v_mfma_f32_16x16x32_bf16 v[162:165], a[136:139], v[68:71], v[162:165]
v_mfma_f32_16x16x32_bf16 v[146:149], a[140:143], v[72:75], v[146:149]
v_mfma_f32_16x16x32_bf16 v[150:153], a[144:147], v[72:75], v[150:153]
v_mfma_f32_16x16x32_bf16 v[154:157], a[148:151], v[72:75], v[154:157]
v_mfma_f32_16x16x32_bf16 v[158:161], a[152:155], v[72:75], v[158:161]
v_mfma_f32_16x16x32_bf16 v[162:165], a[156:159], v[72:75], v[162:165]
v_mfma_f32_16x16x32_bf16 v[146:149], a[160:163], v[76:79], v[146:149]
v_mfma_f32_16x16x32_bf16 v[150:153], a[164:167], v[76:79], v[150:153]
v_mfma_f32_16x16x32_bf16 v[154:157], a[168:171], v[76:79], v[154:157]
v_mfma_f32_16x16x32_bf16 v[158:161], a[172:175], v[76:79], v[158:161]
v_mfma_f32_16x16x32_bf16 v[162:165], a[176:179], v[76:79], v[162:165]
v_mfma_f32_16x16x32_bf16 v[146:149], a[180:183], v[80:83], v[146:149]
v_mfma_f32_16x16x32_bf16 v[150:153], a[184:187], v[80:83], v[150:153]
v_mfma_f32_16x16x32_bf16 v[154:157], a[188:191], v[80:83], v[154:157]
v_mfma_f32_16x16x32_bf16 v[158:161], a[192:195], v[80:83], v[158:161]
v_mfma_f32_16x16x32_bf16 v[162:165], a[196:199], v[80:83], v[162:165]
v_mfma_f32_16x16x32_bf16 v[146:149], a[200:203], v[84:87], v[146:149]
v_mfma_f32_16x16x32_bf16 v[150:153], a[204:207], v[84:87], v[150:153]
v_mfma_f32_16x16x32_bf16 v[154:157], a[208:211], v[84:87], v[154:157]
v_mfma_f32_16x16x32_bf16 v[158:161], a[212:215], v[84:87], v[158:161]
v_mfma_f32_16x16x32_bf16 v[162:165], a[216:219], v[84:87], v[162:165]
v_mfma_f32_16x16x32_bf16 v[146:149], a[220:223], v[88:91], v[146:149]
v_mfma_f32_16x16x32_bf16 v[150:153], a[224:227], v[88:91], v[150:153]
v_mfma_f32_16x16x32_bf16 v[154:157], a[228:231], v[88:91], v[154:157]
v_mfma_f32_16x16x32_bf16 v[158:161], a[232:235], v[88:91], v[158:161]
v_mfma_f32_16x16x32_bf16 v[162:165], a[236:239], v[88:91], v[162:165]
v_mfma_f32_16x16x32_bf16 v[146:149], a[240:243], v[92:95], v[146:149]
v_mfma_f32_16x16x32_bf16 v[150:153], a[244:247], v[92:95], v[150:153]
v_mfma_f32_16x16x32_bf16 v[154:157], a[248:251], v[92:95], v[154:157]
v_mfma_f32_16x16x32_bf16 v[158:161], a[252:255], v[92:95], v[158:161]
v_mfma_f32_16x16x32_bf16 v[162:165], v[232:235], v[92:95], v[162:165]
v_mfma_f32_16x16x32_bf16 v[146:149], v[236:239], v[96:99], v[146:149]
v_mfma_f32_16x16x32_bf16 v[150:153], v[240:243], v[96:99], v[150:153]
v_mfma_f32_16x16x32_bf16 v[154:157], v[244:247], v[96:99], v[154:157]
v_mfma_f32_16x16x32_bf16 v[158:161], v[248:251], v[96:99], v[158:161]
v_mfma_f32_16x16x32_bf16 v[162:165], v[252:255], v[96:99], v[162:165]
s_nop 7
s_branch Lsr_41_%=
Lfs_42_%=:
ds_read_b32 v143, v211 offset:196
ds_read_b128 v[186:189], v9 offset:5120
ds_read_b128 v[190:193], v9 offset:6144
ds_read_b128 v[194:197], v9 offset:7168
ds_read_b128 v[198:201], v9 offset:8192
ds_read_b128 v[202:205], v9 offset:9216
s_waitcnt lgkmcnt(0)
v_cmp_eq_u32 s[46:47], 0, v143
s_sub_u32 s31, s31, 1
s_cmp_gt_i32 s31, 0
s_cselect_b64 s[46:47], s[46:47], 0
s_cmp_lg_u64 s[46:47], 0
s_cbranch_scc1 Lfs_42_%=
s_branch Lfr_43_%=
Lfs_44_%=:
ds_read_b32 v144, v211 offset:200
ds_read_b128 v[52:55], v9 offset:10240
ds_read_b128 v[56:59], v9 offset:11264
ds_read_b128 v[60:63], v9 offset:12288
ds_read_b128 v[64:67], v9 offset:13312
ds_read_b128 v[68:71], v9 offset:14336
s_waitcnt lgkmcnt(0)
v_cmp_eq_u32 s[46:47], 0, v144
s_sub_u32 s31, s31, 1
s_cmp_gt_i32 s31, 0
s_cselect_b64 s[46:47], s[46:47], 0
s_cmp_lg_u64 s[46:47], 0
s_cbranch_scc1 Lfs_44_%=
s_branch Lfr_45_%=
Lst_46_%=:
s_add_u32 s18, s16, 0xffffe000
s_addc_u32 s19, s17, -1
global_load_dwordx4 v[100:103], v4, s[18:19] offset:-4096 sc1
global_load_dwordx4 v[104:107], v4, s[18:19] offset:-3072 sc1
global_load_dwordx4 v[108:111], v4, s[18:19] offset:-2048 sc1
global_load_dwordx4 v[112:115], v4, s[18:19] offset:-1024 sc1
global_load_dwordx4 v[116:119], v4, s[18:19] offset:0 sc1
global_load_dwordx4 v[120:123], v4, s[18:19] offset:1024 sc1
global_load_dwordx4 v[124:127], v4, s[18:19] offset:2048 sc1
global_load_dwordx4 v[128:131], v4, s[18:19] offset:3072 sc1
global_load_dwordx4 v[132:135], v4, s[16:17] offset:-4096 sc1
global_load_dwordx4 v[136:139], v4, s[16:17] offset:-3072 sc1
global_load_dwordx4 v[36:39], v4, s[16:17] offset:-2048 sc1
global_load_dwordx4 v[40:43], v4, s[16:17] offset:-1024 sc1
global_load_dwordx4 v[44:47], v4, s[16:17] offset:0 sc1
global_load_dwordx4 v[48:51], v14, s[16:17] offset:0 sc1
s_waitcnt vmcnt(0)
v_mov_b32 v6, 0
v_max3_u32 v6, v6, v100, v101
v_max3_u32 v6, v6, v102, v103
v_max3_u32 v6, v6, v104, v105
v_max3_u32 v6, v6, v106, v107
v_max3_u32 v6, v6, v108, v109
v_max3_u32 v6, v6, v110, v111
v_max3_u32 v6, v6, v112, v113
v_max3_u32 v6, v6, v114, v115
v_max3_u32 v6, v6, v116, v117
v_max3_u32 v6, v6, v118, v119
v_max3_u32 v6, v6, v120, v121
v_max3_u32 v6, v6, v122, v123
v_max3_u32 v6, v6, v124, v125
v_max3_u32 v6, v6, v126, v127
v_max3_u32 v6, v6, v128, v129
v_max3_u32 v6, v6, v130, v131
v_max3_u32 v6, v6, v132, v133
v_max3_u32 v6, v6, v134, v135
v_max3_u32 v6, v6, v136, v137
v_max3_u32 v6, v6, v138, v139
v_max3_u32 v6, v6, v36, v37
v_max3_u32 v6, v6, v38, v39
v_max3_u32 v6, v6, v40, v41
v_max3_u32 v6, v6, v42, v43
v_max3_u32 v6, v6, v44, v45
v_max3_u32 v6, v6, v46, v47
v_max3_u32 v6, v6, v48, v49
v_max3_u32 v6, v6, v50, v51
v_cmp_eq_u32 vcc, -1, v6
s_sub_u32 s49, s49, 1
v_mov_b32 v6, 0
s_cbranch_vccz Lsx_56_%=
s_cmp_gt_i32 s49, 0
s_cbranch_scc1 Lst_46_%=
Lsx_56_%=:
v_mfma_f32_16x16x32_bf16 v[166:169], a[0:3], v[100:103], v[16:19]
v_mfma_f32_16x16x32_bf16 v[170:173], a[4:7], v[100:103], v[20:23]
v_mfma_f32_16x16x32_bf16 v[174:177], a[8:11], v[100:103], v[24:27]
v_mfma_f32_16x16x32_bf16 v[178:181], a[12:15], v[100:103], v[28:31]
v_mfma_f32_16x16x32_bf16 v[182:185], a[16:19], v[100:103], v[32:35]
v_mfma_f32_16x16x32_bf16 v[166:169], a[20:23], v[104:107], v[166:169]
v_mfma_f32_16x16x32_bf16 v[170:173], a[24:27], v[104:107], v[170:173]
v_mfma_f32_16x16x32_bf16 v[174:177], a[28:31], v[104:107], v[174:177]
v_mfma_f32_16x16x32_bf16 v[178:181], a[32:35], v[104:107], v[178:181]
v_mfma_f32_16x16x32_bf16 v[182:185], a[36:39], v[104:107], v[182:185]
v_mfma_f32_16x16x32_bf16 v[166:169], a[40:43], v[108:111], v[166:169]
v_mfma_f32_16x16x32_bf16 v[170:173], a[44:47], v[108:111], v[170:173]
v_mfma_f32_16x16x32_bf16 v[174:177], a[48:51], v[108:111], v[174:177]
v_mfma_f32_16x16x32_bf16 v[178:181], a[52:55], v[108:111], v[178:181]
v_mfma_f32_16x16x32_bf16 v[182:185], a[56:59], v[108:111], v[182:185]
v_mfma_f32_16x16x32_bf16 v[166:169], a[60:63], v[112:115], v[166:169]
v_mfma_f32_16x16x32_bf16 v[170:173], a[64:67], v[112:115], v[170:173]
v_mfma_f32_16x16x32_bf16 v[174:177], a[68:71], v[112:115], v[174:177]
v_mfma_f32_16x16x32_bf16 v[178:181], a[72:75], v[112:115], v[178:181]
v_mfma_f32_16x16x32_bf16 v[182:185], a[76:79], v[112:115], v[182:185]
v_mfma_f32_16x16x32_bf16 v[166:169], a[80:83], v[116:119], v[166:169]
v_mfma_f32_16x16x32_bf16 v[170:173], a[84:87], v[116:119], v[170:173]
v_mfma_f32_16x16x32_bf16 v[174:177], a[88:91], v[116:119], v[174:177]
v_mfma_f32_16x16x32_bf16 v[178:181], a[92:95], v[116:119], v[178:181]
v_mfma_f32_16x16x32_bf16 v[182:185], a[96:99], v[116:119], v[182:185]
v_mfma_f32_16x16x32_bf16 v[166:169], a[100:103], v[120:123], v[166:169]
v_mfma_f32_16x16x32_bf16 v[170:173], a[104:107], v[120:123], v[170:173]
v_mfma_f32_16x16x32_bf16 v[174:177], a[108:111], v[120:123], v[174:177]
)ASM"
R"ASM(v_mfma_f32_16x16x32_bf16 v[178:181], a[112:115], v[120:123], v[178:181]
v_mfma_f32_16x16x32_bf16 v[182:185], a[116:119], v[120:123], v[182:185]
v_mfma_f32_16x16x32_bf16 v[166:169], a[120:123], v[124:127], v[166:169]
v_mfma_f32_16x16x32_bf16 v[170:173], a[124:127], v[124:127], v[170:173]
v_mfma_f32_16x16x32_bf16 v[174:177], a[128:131], v[124:127], v[174:177]
v_mfma_f32_16x16x32_bf16 v[178:181], a[132:135], v[124:127], v[178:181]
v_mfma_f32_16x16x32_bf16 v[182:185], a[136:139], v[124:127], v[182:185]
v_mfma_f32_16x16x32_bf16 v[166:169], a[140:143], v[128:131], v[166:169]
v_mfma_f32_16x16x32_bf16 v[170:173], a[144:147], v[128:131], v[170:173]
v_mfma_f32_16x16x32_bf16 v[174:177], a[148:151], v[128:131], v[174:177]
v_mfma_f32_16x16x32_bf16 v[178:181], a[152:155], v[128:131], v[178:181]
v_mfma_f32_16x16x32_bf16 v[182:185], a[156:159], v[128:131], v[182:185]
v_mfma_f32_16x16x32_bf16 v[166:169], a[160:163], v[132:135], v[166:169]
v_mfma_f32_16x16x32_bf16 v[170:173], a[164:167], v[132:135], v[170:173]
v_mfma_f32_16x16x32_bf16 v[174:177], a[168:171], v[132:135], v[174:177]
v_mfma_f32_16x16x32_bf16 v[178:181], a[172:175], v[132:135], v[178:181]
v_mfma_f32_16x16x32_bf16 v[182:185], a[176:179], v[132:135], v[182:185]
v_mfma_f32_16x16x32_bf16 v[166:169], a[180:183], v[136:139], v[166:169]
v_mfma_f32_16x16x32_bf16 v[170:173], a[184:187], v[136:139], v[170:173]
v_mfma_f32_16x16x32_bf16 v[174:177], a[188:191], v[136:139], v[174:177]
v_mfma_f32_16x16x32_bf16 v[178:181], a[192:195], v[136:139], v[178:181]
v_mfma_f32_16x16x32_bf16 v[182:185], a[196:199], v[136:139], v[182:185]
v_mfma_f32_16x16x32_bf16 v[166:169], a[200:203], v[36:39], v[166:169]
v_mfma_f32_16x16x32_bf16 v[170:173], a[204:207], v[36:39], v[170:173]
v_mfma_f32_16x16x32_bf16 v[174:177], a[208:211], v[36:39], v[174:177]
v_mfma_f32_16x16x32_bf16 v[178:181], a[212:215], v[36:39], v[178:181]
v_mfma_f32_16x16x32_bf16 v[182:185], a[216:219], v[36:39], v[182:185]
v_mfma_f32_16x16x32_bf16 v[166:169], a[220:223], v[40:43], v[166:169]
v_mfma_f32_16x16x32_bf16 v[170:173], a[224:227], v[40:43], v[170:173]
v_mfma_f32_16x16x32_bf16 v[174:177], a[228:231], v[40:43], v[174:177]
v_mfma_f32_16x16x32_bf16 v[178:181], a[232:235], v[40:43], v[178:181]
v_mfma_f32_16x16x32_bf16 v[182:185], a[236:239], v[40:43], v[182:185]
v_mfma_f32_16x16x32_bf16 v[166:169], a[240:243], v[44:47], v[166:169]
v_mfma_f32_16x16x32_bf16 v[170:173], a[244:247], v[44:47], v[170:173]
v_mfma_f32_16x16x32_bf16 v[174:177], a[248:251], v[44:47], v[174:177]
v_mfma_f32_16x16x32_bf16 v[178:181], a[252:255], v[44:47], v[178:181]
v_mfma_f32_16x16x32_bf16 v[182:185], v[232:235], v[44:47], v[182:185]
v_mfma_f32_16x16x32_bf16 v[166:169], v[236:239], v[48:51], v[166:169]
v_mfma_f32_16x16x32_bf16 v[170:173], v[240:243], v[48:51], v[170:173]
v_mfma_f32_16x16x32_bf16 v[174:177], v[244:247], v[48:51], v[174:177]
v_mfma_f32_16x16x32_bf16 v[178:181], v[248:251], v[48:51], v[178:181]
v_mfma_f32_16x16x32_bf16 v[182:185], v[252:255], v[48:51], v[182:185]
s_nop 7
s_branch Lsr_47_%=
Lfb_3_%=:
s_waitcnt vmcnt(0) lgkmcnt(0)
s_nop 15
; ==== out of line: two-pass softmax with the true row maximum (taken when a row sum left [2^-60, 2^60])
s_lshl_b32 s36, s98, 6
s_mul_i32 s36, s36, 0x1600
s_add_u32 s36, s36, 0x290000
s_add_u32 s46, s20, s36
s_addc_u32 s47, s21, 0
s_mov_b32 s35, 64
v_mov_b32 v104, 0xff800000
s_nop 4
Lfl_57_%=:
global_load_dwordx4 v[174:177], v1, s[46:47]
global_load_dwordx2 v[178:179], v2, s[46:47]
s_add_u32 s46, s46, 0x1600
s_addc_u32 s47, s47, 0
s_waitcnt vmcnt(1)
v_mfma_f32_32x32x16_bf16 v[114:129], v[174:177], v[10:13], 0
s_waitcnt vmcnt(0)
v_mfma_f32_32x32x8_bf16 v[114:129], v[178:179], v[14:15], v[114:129]
s_nop 11
v_max3_f32 v104, v104, v114, v115
v_max3_f32 v104, v104, v116, v117
v_max3_f32 v104, v104, v118, v119
v_max3_f32 v104, v104, v120, v121
v_max3_f32 v104, v104, v122, v123
v_max3_f32 v104, v104, v124, v125
v_max3_f32 v104, v104, v126, v127
v_max3_f32 v104, v104, v128, v129
s_sub_u32 s35, s35, 1
s_cmp_lg_u32 s35, 0
s_waitcnt vmcnt(0)
s_nop 1
s_cbranch_scc1 Lfl_57_%=
v_mov_b32 v105, v104
s_nop 1
v_permlane32_swap_b32 v104, v105
v_max_f32 v104, v104, v105
v_sub_f32 v130, 0, v104
v_sub_f32 v131, 0, v104
v_sub_f32 v132, 0, v104
v_sub_f32 v133, 0, v104
v_sub_f32 v134, 0, v104
v_sub_f32 v135, 0, v104
v_sub_f32 v136, 0, v104
v_sub_f32 v137, 0, v104
v_sub_f32 v138, 0, v104
v_sub_f32 v139, 0, v104
v_sub_f32 v140, 0, v104
v_sub_f32 v141, 0, v104
v_sub_f32 v142, 0, v104
v_sub_f32 v143, 0, v104
v_sub_f32 v144, 0, v104
v_sub_f32 v145, 0, v104
s_lshl_b32 s36, s98, 6
s_mul_i32 s36, s36, 0x1600
s_add_u32 s36, s36, 0x290000
s_add_u32 s46, s20, s36
s_addc_u32 s47, s21, 0
s_mov_b32 s35, 64
v_mov_b32 v34, 0
v_mov_b32 v35, 0
v_mov_b32 v36, 0
v_mov_b32 v37, 0
v_mov_b32 v38, 0
v_mov_b32 v39, 0
v_mov_b32 v40, 0
v_mov_b32 v41, 0
v_mov_b32 v42, 0
v_mov_b32 v43, 0
v_mov_b32 v44, 0
v_mov_b32 v45, 0
v_mov_b32 v46, 0
v_mov_b32 v47, 0
v_mov_b32 v48, 0
v_mov_b32 v49, 0
s_nop 4
Lfl_58_%=:
global_load_dwordx4 v[174:177], v1, s[46:47]
global_load_dwordx2 v[178:179], v2, s[46:47]
global_load_dwordx4 v[180:183], v3, s[46:47]
global_load_dwordx4 v[184:187], v3, s[46:47] offset:768
s_add_u32 s46, s46, 0x1600
s_addc_u32 s47, s47, 0
s_waitcnt vmcnt(3)
v_mfma_f32_32x32x16_bf16 v[114:129], v[174:177], v[10:13], v[130:145]
s_waitcnt vmcnt(2)
v_mfma_f32_32x32x8_bf16 v[114:129], v[178:179], v[14:15], v[114:129]
s_nop 11
v_exp_f32 v114, v114
v_exp_f32 v115, v115
v_exp_f32 v116, v116
v_exp_f32 v117, v117
v_exp_f32 v118, v118
v_exp_f32 v119, v119
v_exp_f32 v120, v120
v_exp_f32 v121, v121
v_exp_f32 v122, v122
v_exp_f32 v123, v123
v_exp_f32 v124, v124
v_exp_f32 v125, v125
v_exp_f32 v126, v126
v_exp_f32 v127, v127
v_exp_f32 v128, v128
v_exp_f32 v129, v129
s_waitcnt vmcnt(1)
v_smfmac_f32_32x32x32_bf16 v[34:49], v[180:183], v[114:121], v5
s_waitcnt vmcnt(0)
v_smfmac_f32_32x32x32_bf16 v[34:49], v[184:187], v[122:129], v5
s_sub_u32 s35, s35, 1
s_cmp_lg_u32 s35, 0
s_waitcnt vmcnt(0)
s_nop 8
s_cbranch_scc1 Lfl_58_%=
s_lshl_b32 s36, s98, 6
s_mul_i32 s36, s36, 0x1600
s_add_u32 s36, s36, 0x290000
s_add_u32 s46, s20, s36
s_addc_u32 s47, s21, 0
s_mov_b32 s35, 64
v_mov_b32 v104, 0xff800000
s_nop 4
Lfl_59_%=:
global_load_dwordx4 v[174:177], v1, s[46:47]
global_load_dwordx2 v[178:179], v2, s[46:47]
s_add_u32 s46, s46, 0x1600
s_addc_u32 s47, s47, 0
s_waitcnt vmcnt(1)
v_mfma_f32_32x32x16_bf16 v[114:129], v[174:177], v[16:19], 0
s_waitcnt vmcnt(0)
v_mfma_f32_32x32x8_bf16 v[114:129], v[178:179], v[20:21], v[114:129]
s_nop 11
v_max3_f32 v104, v104, v114, v115
v_max3_f32 v104, v104, v116, v117
v_max3_f32 v104, v104, v118, v119
v_max3_f32 v104, v104, v120, v121
v_max3_f32 v104, v104, v122, v123
v_max3_f32 v104, v104, v124, v125
v_max3_f32 v104, v104, v126, v127
v_max3_f32 v104, v104, v128, v129
s_sub_u32 s35, s35, 1
s_cmp_lg_u32 s35, 0
s_waitcnt vmcnt(0)
s_nop 1
s_cbranch_scc1 Lfl_59_%=
v_mov_b32 v105, v104
s_nop 1
v_permlane32_swap_b32 v104, v105
v_max_f32 v104, v104, v105
v_sub_f32 v130, 0, v104
v_sub_f32 v131, 0, v104
v_sub_f32 v132, 0, v104
v_sub_f32 v133, 0, v104
v_sub_f32 v134, 0, v104
v_sub_f32 v135, 0, v104
)ASM"
R"ASM(v_sub_f32 v136, 0, v104
v_sub_f32 v137, 0, v104
v_sub_f32 v138, 0, v104
v_sub_f32 v139, 0, v104
v_sub_f32 v140, 0, v104
v_sub_f32 v141, 0, v104
v_sub_f32 v142, 0, v104
v_sub_f32 v143, 0, v104
v_sub_f32 v144, 0, v104
v_sub_f32 v145, 0, v104
s_lshl_b32 s36, s98, 6
s_mul_i32 s36, s36, 0x1600
s_add_u32 s36, s36, 0x290000
s_add_u32 s46, s20, s36
s_addc_u32 s47, s21, 0
s_mov_b32 s35, 64
v_mov_b32 v50, 0
v_mov_b32 v51, 0
v_mov_b32 v52, 0
v_mov_b32 v53, 0
v_mov_b32 v54, 0
v_mov_b32 v55, 0
v_mov_b32 v56, 0
v_mov_b32 v57, 0
v_mov_b32 v58, 0
v_mov_b32 v59, 0
v_mov_b32 v60, 0
v_mov_b32 v61, 0
v_mov_b32 v62, 0
v_mov_b32 v63, 0
v_mov_b32 v64, 0
v_mov_b32 v65, 0
s_nop 4
Lfl_60_%=:
global_load_dwordx4 v[174:177], v1, s[46:47]
global_load_dwordx2 v[178:179], v2, s[46:47]
global_load_dwordx4 v[180:183], v3, s[46:47]
global_load_dwordx4 v[184:187], v3, s[46:47] offset:768
s_add_u32 s46, s46, 0x1600
s_addc_u32 s47, s47, 0
s_waitcnt vmcnt(3)
v_mfma_f32_32x32x16_bf16 v[114:129], v[174:177], v[16:19], v[130:145]
s_waitcnt vmcnt(2)
v_mfma_f32_32x32x8_bf16 v[114:129], v[178:179], v[20:21], v[114:129]
s_nop 11
v_exp_f32 v114, v114
v_exp_f32 v115, v115
v_exp_f32 v116, v116
v_exp_f32 v117, v117
v_exp_f32 v118, v118
v_exp_f32 v119, v119
v_exp_f32 v120, v120
v_exp_f32 v121, v121
v_exp_f32 v122, v122
v_exp_f32 v123, v123
v_exp_f32 v124, v124
v_exp_f32 v125, v125
v_exp_f32 v126, v126
v_exp_f32 v127, v127
v_exp_f32 v128, v128
v_exp_f32 v129, v129
s_waitcnt vmcnt(1)
v_smfmac_f32_32x32x32_bf16 v[50:65], v[180:183], v[114:121], v5
s_waitcnt vmcnt(0)
v_smfmac_f32_32x32x32_bf16 v[50:65], v[184:187], v[122:129], v5
s_sub_u32 s35, s35, 1
s_cmp_lg_u32 s35, 0
s_waitcnt vmcnt(0)
s_nop 8
s_cbranch_scc1 Lfl_60_%=
v_mov_b32 v98, v42
v_mov_b32 v102, v42
s_nop 1
v_permlane32_swap_b32 v102, v98
v_rcp_f32 v98, v98
v_mov_b32 v100, v58
v_mov_b32 v103, v58
s_nop 1
v_permlane32_swap_b32 v103, v100
v_rcp_f32 v100, v100
s_nop 4
s_branch Lback_4_%=
Lend_48_%=:
k2_code_end:

)ASM"
  :: "s"(__builtin_amdgcn_kernarg_segment_ptr()), "s"(blockIdx.x), "v"(threadIdx.x), "s"((unsigned)(size_t)lds) : "v1", "v2", "v3", "v4", "v5", "v6", "v7", "v8", "v9", "v10", "v11", "v12", "v13", "v14", "v15", "v16", "v17", "v18", "v19", "v20", "v21", "v22", "v23", "v24", "v25", "v26", "v27", "v28", "v29", "v30", "v31", "v32", "v33", "v34", "v35", "v36", "v37", "v38", "v39", "v40", "v41", "v42", "v43", "v44", "v45", "v46", "v47", "v48", "v49", "v50", "v51", "v52", "v53", "v54", "v55", "v56", "v57", "v58", "v59", "v60", "v61", "v62", "v63", "v64", "v65", "v66", "v67", "v68", "v69", "v70", "v71", "v72", "v73", "v74", "v75", "v76", "v77", "v78", "v79", "v80", "v81", "v82", "v83", "v84", "v85", "v86", "v87", "v88", "v89", "v90", "v91", "v92", "v93", "v94", "v95", "v96", "v97", "v98", "v99", "v100", "v101", "v102", "v103", "v104", "v105", "v106", "v107", "v108", "v109", "v110", "v111", "v112", "v113", "v114", "v115", "v116", "v117", "v118", "v119", "v120", "v121", "v122", "v123", "v124", "v125", "v126", "v127", "v128", "v129", "v130", "v131", "v132", "v133", "v134", "v135", "v136", "v137", "v138", "v139", "v140", "v141", "v142", "v143", "v144", "v145", "v146", "v147", "v148", "v149", "v150", "v151", "v152", "v153", "v154", "v155", "v156", "v157", "v158", "v159", "v160", "v161", "v162", "v163", "v164", "v165", "v166", "v167", "v168", "v169", "v170", "v171", "v172", "v173", "v174", "v175", "v176", "v177", "v178", "v179", "v180", "v181", "v182", "v183", "v184", "v185", "v186", "v187", "v188", "v189", "v190", "v191", "v192", "v193", "v194", "v195", "v196", "v197", "v198", "v199", "v200", "v201", "v202", "v203", "v204", "v205", "v206", "v207", "v208", "v209", "v210", "v211", "v212", "v213", "v214", "v215", "v216", "v217", "v218", "v219", "v220", "v221", "v222", "v223", "v224", "v225", "v226", "v227", "v228", "v229", "v230", "v231", "v232", "v233", "v234", "v235", "v236", "v237", "v238", "v239", "v240", "v241", "v242", "v243", "v244", "v245", "v246", "v247", "v248", "v249", "v250", "v251", "v252", "v253", "v254", "v255", "a0", "a1", "a2", "a3", "a4", "a5", "a6", "a7", "a8", "a9", "a10", "a11", "a12", "a13", "a14", "a15", "a16", "a17", "a18", "a19", "a20", "a21", "a22", "a23", "a24", "a25", "a26", "a27", "a28", "a29", "a30", "a31", "a32", "a33", "a34", "a35", "a36", "a37", "a38", "a39", "a40", "a41", "a42", "a43", "a44", "a45", "a46", "a47", "a48", "a49", "a50", "a51", "a52", "a53", "a54", "a55", "a56", "a57", "a58", "a59", "a60", "a61", "a62", "a63", "a64", "a65", "a66", "a67", "a68", "a69", "a70", "a71", "a72", "a73", "a74", "a75", "a76", "a77", "a78", "a79", "a80", "a81", "a82", "a83", "a84", "a85", "a86", "a87", "a88", "a89", "a90", "a91", "a92", "a93", "a94", "a95", "a96", "a97", "a98", "a99", "a100", "a101", "a102", "a103", "a104", "a105", "a106", "a107", "a108", "a109", "a110", "a111", "a112", "a113", "a114", "a115", "a116", "a117", "a118", "a119", "a120", "a121", "a122", "a123", "a124", "a125", "a126", "a127", "a128", "a129", "a130", "a131", "a132", "a133", "a134", "a135", "a136", "a137", "a138", "a139", "a140", "a141", "a142", "a143", "a144", "a145", "a146", "a147", "a148", "a149", "a150", "a151", "a152", "a153", "a154", "a155", "a156", "a157", "a158", "a159", "a160", "a161", "a162", "a163", "a164", "a165", "a166", "a167", "a168", "a169", "a170", "a171", "a172", "a173", "a174", "a175", "a176", "a177", "a178", "a179", "a180", "a181", "a182", "a183", "a184", "a185", "a186", "a187", "a188", "a189", "a190", "a191", "a192", "a193", "a194", "a195", "a196", "a197", "a198", "a199", "a200", "a201", "a202", "a203", "a204", "a205", "a206", "a207", "a208", "a209", "a210", "a211", "a212", "a213", "a214", "a215", "a216", "a217", "a218", "a219", "a220", "a221", "a222", "a223", "a224", "a225", "a226", "a227", "a228", "a229", "a230", "a231", "a232", "a233", "a234", "a235", "a236", "a237", "a238", "a239", "a240", "a241", "a242", "a243", "a244", "a245", "a246", "a247", "a248", "a249", "a250", "a251", "a252", "a253", "a254", "a255", "s4", "s5", "s6", "s7", "s8", "s9", "s10", "s11", "s12", "s13", "s14", "s15", "s16", "s17", "s18", "s19", "s20", "s21", "s22", "s23", "s24", "s25", "s26", "s27", "s28", "s29", "s30", "s31", "s33", "s34", "s35", "s36", "s37", "s38", "s39", "s40", "s41", "s42", "s43", "s44", "s45", "s46", "s47", "s48", "s49", "s50", "s51", "s52", "s53", "s54", "s55", "s56", "s57", "s58", "s59", "s60", "s61", "s62", "s63", "s64", "s65", "s66", "s67", "s68", "s69", "s70", "s71", "s72", "s73", "s74", "s75", "s76", "s77", "s78", "s79", "s80", "s81", "s82", "s83", "s84", "s85", "s86", "s87", "s88", "s89", "s90", "s91", "s92", "s93", "s94", "s95", "s96", "s97", "s98", "s99", "vcc", "scc", "memory");
}

extern "C" void kernel_launch(void* const* d_in, const int* in_sizes, int n_in, void* d_out, int out_size, void* d_ws, size_t ws_size, hipStream_t stream) {
  char* ws = (char*)d_ws;
  k1_ln_qkv<<<256, 512, 0, stream>>>(ws, d_in[0], d_in[1], d_in[2], d_in[5], d_in[4]);
  k2_attn_ffn<<<256, 256, 0, stream>>>(ws, d_in[0], d_out);
}
```
